```python
import torch

def _softmax(x, axis=-1):
    return torch.softmax(x, dim=axis)
def _gelu(x):
    return torch.nn.functional.gelu(x, approximate="tanh")
def _silu(x):
    return torch.nn.functional.silu(x)
def _sigmoid(x):
    return torch.sigmoid(x)
def _layernorm(x, axis=-1, eps=1e-5):
    m = x.mean(dim=axis, keepdim=True)
    v = x.var(dim=axis, keepdim=True, unbiased=False)
    return (x - m) / torch.sqrt(v + eps)
def _scatter_add(tgt, idx, src, axis=0):
    return tgt.clone().index_add_(axis, idx, src)
def _segment_sum(src, seg, n):
    out = torch.zeros((n,) + src.shape[1:], dtype=src.dtype)
    return out.index_add_(0, seg, src)
def _pad(x, pw):
    flat = tuple(v for pair in reversed(pw) for v in pair)
    return torch.nn.functional.pad(x, flat)

kernel_name = 'standalone__gqa_block_cached__near_ridge'

def setup_inputs():

    _w = (lambda a: (lambda t: t.to(torch.bfloat16) if t.is_floating_point() else t)(torch.as_tensor(a)))
    B, Sq, D = 8, 160, 448
    Hq, Hkv, Sk, Dh = 16, 8, 2048, 256
    assert Hq % Hkv == 0, (Hq, Hkv)
    return {
        "x": _w((lambda *s: torch.randn(*s, dtype=torch.bfloat16) * 0.8)(B, Sq, D)),
        "k_cache": _w((lambda *s: torch.randn(*s, dtype=torch.bfloat16) * 0.8)(B, Hkv, Sk, Dh)),
        "v_cache": _w((lambda *s: torch.randn(*s, dtype=torch.bfloat16) * 0.05)(B, Hkv, Sk, Dh)),
        "wq": _w((lambda *s: torch.randn(*s, dtype=torch.bfloat16) * 0.8)(D, Hq * Dh) * (D ** -0.5)),
        "wo": _w((lambda *s: torch.randn(*s, dtype=torch.bfloat16) * 0.05)(Hq * Dh, D) * ((Hq * Dh) ** -0.5)),
    }

def reference(x, k_cache, v_cache, wq, wo):
    B, Sq, D = x.shape
    Hq, Dh = 16, 256
    Hkv = k_cache.shape[1]
    rep = Hq // Hkv
    q = (lambda x, *d: x.permute(*d))((x @ wq).reshape(B, Sq, Hq, Dh), 0, 2, 1, 3)
    k = (lambda x, n, axis: torch.repeat_interleave(x, n, dim=axis))(k_cache, rep, axis=1)
    v = (lambda x, n, axis: torch.repeat_interleave(x, n, dim=axis))(v_cache, rep, axis=1)
    scores = torch.einsum("bhqd,bhkd->bhqk", q, k) * (Dh ** -0.5)
    p = _softmax(scores, axis=-1)
    o = torch.einsum("bhqk,bhkd->bhqd", p, v)
    return (lambda x, *d: x.permute(*d))(o, 0, 2, 1, 3).reshape(B, Sq, Hq * Dh) @ wo
```

```cpp
#include <hip/hip_runtime.h>
#include <hip/hip_bf16.h>
typedef unsigned short u16;
__device__ __forceinline__ float bf2f(u16 x){ return __uint_as_float(((unsigned)x)<<16); }
__device__ __forceinline__ u16 f2bf(float f){ __hip_bfloat16 h = __float2bfloat16(f); return *(u16*)&h; }
constexpr int B=8,SQ=160,D=448,HQ=16,HKV=8,SK=2048,DH=256;
__global__ void gemm_naive(const u16* A,const u16* Bm,u16* C,int M,int N,int K){
  int n = blockIdx.x*64+threadIdx.x%64; int m = blockIdx.y*4+threadIdx.x/64;
  if(n>=N||m>=M) return;
  float acc=0; for(int k=0;k<K;++k) acc += bf2f(A[(size_t)m*K+k])*bf2f(Bm[(size_t)k*N+n]);
  C[(size_t)m*N+n]=f2bf(acc);
}
__global__ void attn_naive(const u16* Q,const u16* Kc,const u16* Vc,u16* O){
  __shared__ float qs[DH]; __shared__ float s[SK]; __shared__ float red[256];
  int q=blockIdx.x, h=blockIdx.y, b=blockIdx.z, t=threadIdx.x;
  qs[t]=bf2f(Q[((size_t)(b*SQ+q))*HQ*DH+h*DH+t]); __syncthreads();
  const u16* Kp=Kc+((size_t)(b*HKV+h/2))*SK*DH; const u16* Vp=Vc+((size_t)(b*HKV+h/2))*SK*DH;
  float mx=-1e30f;
  for(int k=t;k<SK;k+=256){ float a=0; for(int d=0;d<DH;++d) a+=qs[d]*bf2f(Kp[(size_t)k*DH+d]); a*=0.0625f; s[k]=a; mx=fmaxf(mx,a);}
  red[t]=mx; __syncthreads(); for(int o=128;o>0;o>>=1){ if(t<o) red[t]=fmaxf(red[t],red[t+o]); __syncthreads(); }
  mx=red[0]; __syncthreads(); float sm=0;
  for(int k=t;k<SK;k+=256){ float e=__expf(s[k]-mx); s[k]=e; sm+=e; }
  red[t]=sm; __syncthreads(); for(int o=128;o>0;o>>=1){ if(t<o) red[t]+=red[t+o]; __syncthreads(); }
  sm=red[0]; float acc=0; for(int k=0;k<SK;++k) acc+=s[k]*bf2f(Vp[(size_t)k*DH+t]);
  O[((size_t)(b*SQ+q))*HQ*DH+h*DH+t]=f2bf(acc/sm);
}
extern "C" void kernel_launch(void* const* d_in, const int* in_sizes, int n_in, void* d_out, int out_size, void* d_ws, size_t ws_size, hipStream_t stream) {
  const u16* x=(const u16*)d_in[0]; const u16* kc=(const u16*)d_in[1]; const u16* vc=(const u16*)d_in[2]; const u16* wq=(const u16*)d_in[3]; const u16* wo=(const u16*)d_in[4];
  u16* q=(u16*)d_ws; u16* o=q+(size_t)B*SQ*HQ*DH;
  gemm_naive<<<dim3(HQ*DH/64,B*SQ/4),256,0,stream>>>(x,wq,q,B*SQ,HQ*DH,D);
  attn_naive<<<dim3(SQ,HQ,B),256,0,stream>>>(q,kc,vc,o);
  gemm_naive<<<dim3(D/64,B*SQ/4),256,0,stream>>>(o,wo,(u16*)d_out,B*SQ,D,HQ*DH);
}
```

```cpp
#include <hip/hip_runtime.h>
#include <hip/hip_bf16.h>
typedef unsigned short u16;
__device__ __forceinline__ float bf2f(u16 x){ return __uint_as_float(((unsigned)x)<<16); }
__device__ __forceinline__ u16 f2bf(float f){ __hip_bfloat16 h = __float2bfloat16(f); return *(u16*)&h; }
__global__ __launch_bounds__(256) void allin(const u16* x,const u16* Kc,const u16* Vc,const u16* wq,const u16* wo,u16* out){
  const unsigned long long t0=__builtin_amdgcn_s_memrealtime();
  __shared__ float xs[448]; __shared__ float q[4096]; __shared__ float o[4096]; __shared__ float s[2048]; __shared__ float red[256];
  int t=threadIdx.x;
  for(int r=blockIdx.x;r<1280;r+=256){ int b=r/160;
    __syncthreads(); for(int k=t;k<448;k+=256) xs[k]=bf2f(x[r*448+k]); __syncthreads();
    for(int n=t;n<4096;n+=256){ float a=0; for(int k=0;k<448;++k) a+=xs[k]*bf2f(wq[(size_t)k*4096+n]); q[n]=bf2f(f2bf(a)); } __syncthreads();
    for(int h=0;h<16;++h){ const u16* Kp=Kc+((size_t)(b*8+h/2))*2048*256; const u16* Vp=Vc+((size_t)(b*8+h/2))*2048*256; float mx=-1e30f;
      for(int k=t;k<2048;k+=256){ float a=0; const uint4* kp=(const uint4*)(Kp+(size_t)k*256); for(int d=0;d<32;++d){ uint4 v=kp[d]; const float* qq=q+h*256+d*8;
          a+=qq[0]*__uint_as_float(v.x<<16)+qq[1]*__uint_as_float(v.x&0xffff0000u)+qq[2]*__uint_as_float(v.y<<16)+qq[3]*__uint_as_float(v.y&0xffff0000u)+qq[4]*__uint_as_float(v.z<<16)+qq[5]*__uint_as_float(v.z&0xffff0000u)+qq[6]*__uint_as_float(v.w<<16)+qq[7]*__uint_as_float(v.w&0xffff0000u);} a*=0.0625f; s[k]=a; mx=fmaxf(mx,a);}
      red[t]=mx; __syncthreads(); for(int w=128;w>0;w>>=1){ if(t<w) red[t]=fmaxf(red[t],red[t+w]); __syncthreads(); } mx=red[0]; __syncthreads(); float sm=0;
      for(int k=t;k<2048;k+=256){ float e=__expf(s[k]-mx); s[k]=e; sm+=e; } red[t]=sm; __syncthreads(); for(int w=128;w>0;w>>=1){ if(t<w) red[t]+=red[t+w]; __syncthreads(); }
      sm=red[0]; float acc=0; for(int k=0;k<2048;++k) acc+=s[k]*bf2f(Vp[(size_t)k*256+t]); o[h*256+t]=bf2f(f2bf(acc/sm)); __syncthreads(); }
    for(int n=t;n<448;n+=256){ float a=0; for(int k=0;k<4096;++k) a+=o[k]*bf2f(wo[(size_t)k*448+n]); out[r*448+n]=f2bf(a); } }
  while(__builtin_amdgcn_s_memrealtime()-t0 < 3000000ull) __builtin_amdgcn_s_sleep(1);
}
__global__ void emptyk(char* ws){ if(ws==nullptr) ws[0]=1; }
extern "C" void kernel_launch(void* const* d_in, const int* in_sizes, int n_in, void* d_out, int out_size, void* d_ws, size_t ws_size, hipStream_t stream) {
  allin<<<256,256,0,stream>>>((const u16*)d_in[0],(const u16*)d_in[1],(const u16*)d_in[2],(const u16*)d_in[3],(const u16*)d_in[4],(u16*)d_out);
}
```

```cpp
#include <hip/hip_runtime.h>
#include <hip/hip_bf16.h>

extern "C" __global__ __launch_bounds__(256, 1)
void gqa_block_kernel(const char* x, const char* kc, const char* vc, const char* wq, const char* wo, char* ws, char* out) {
  const unsigned w = __builtin_amdgcn_readfirstlane(threadIdx.x >> 6);
  asm volatile(
"s_load_dwordx8 s[16:23], %0, 0\n"
"s_load_dwordx4 s[24:27], %0, 32\n"
"s_load_dwordx2 s[28:29], %0, 48\n"
"s_mov_b32 s70, %2\n"
"s_and_b32 s71, %1, 7\n"
"s_lshr_b32 s99, %1, 3\n"
"s_lshr_b32 s63, %1, 5\n"
"s_bfe_u32 s96, %1, 0x10004\n"
"s_and_b32 s79, s99, 1\n"
"s_lshl1_add_u32 s79, s71, s79\n"
"s_lshl_b32 s60, s79, 9\n"
"s_sub_u32 s86, 3670016, s60\n"
"s_mov_b32 s87, 0x20000\n"
"s_mul_i32 s59, s63, 143360\n"
"s_mov_b32 s82, 143360\n"
"s_mov_b32 s83, 0x20000\n"
"v_mbcnt_lo_u32_b32 v0, -1, 0\n"
"v_mbcnt_hi_u32_b32 v0, -1, v0\n"
"s_mov_b32 s58, s60\n"
"s_mov_b32 s56, s59\n"
"v_lshrrev_b32 v4, 5, v0\n"
"v_and_b32 v6, 31, v0\n"
"v_lshlrev_b32 v5, 2, v4\n"
"v_xor_b32 v5, v5, v6\n"
"v_lshlrev_b32 v5, 4, v5\n"
"v_lshl_add_u32 v39, v4, 13, v5\n"
"v_xor_b32 v40, 128, v39\n"
"v_add_u32 v40, 15360, v40\n"
"v_add_u32 v41, 30720, v39\n"
"v_add_u32 v42, 30720, v40\n"
"s_lshl_b32 s88, s70, 17\n"
"s_lshl_b32 s90, s70, 13\n"
"v_lshrrev_b32 v4, 3, v0\n"
"v_mul_u32_u24 v4, 896, v4\n"
"v_lshrrev_b32 v5, 4, v0\n"
"v_and_b32 v6, 7, v0\n"
"v_xor_b32 v5, v5, v6\n"
"s_and_b32 s60, s70, 1\n"
"s_lshl_b32 s60, s60, 2\n"
"v_xor_b32 v5, s60, v5\n"
"v_lshl_add_u32 v43, v5, 4, v4\n"
"v_xor_b32 v5, 4, v5\n"
"v_lshl_add_u32 v44, v5, 4, v4\n"
"v_add_u32 v44, 6144, v44\n"
"v_add_u32 v45, 12288, v43\n"
"v_add_u32 v46, 12288, v44\n"
"v_add_u32 v47, 28672, v43\n"
"s_mul_i32 s89, s70, 35840\n"
"s_mul_i32 s91, s70, 5120\n"
"s_waitcnt lgkmcnt(0)\n"
"s_add_u32 s84, s22, s58\n"
"s_addc_u32 s85, s23, 0\n"
"s_and_b32 s85, s85, 0xffff\n"
"s_add_u32 s80, s16, s56\n"
"s_addc_u32 s81, s17, 0\n"
"s_and_b32 s81, s81, 0xffff\n"
"s_add_u32 m0, s90, 106496\n"
"s_nop 0\n"
"buffer_load_dwordx4 v39, s[84:87], s88 offen lds\n"
"buffer_load_dwordx4 v40, s[84:87], s88 offen offset:1024 lds\n"
"buffer_load_dwordx4 v41, s[84:87], s88 offen offset:2048 lds\n"
"buffer_load_dwordx4 v42, s[84:87], s88 offen offset:3072 lds\n"
"s_add_u32 s88, s88, 65536\n"
"s_add_u32 m0, s90, 110592\n"
"s_nop 0\n"
"buffer_load_dwordx4 v39, s[84:87], s88 offen lds\n"
"buffer_load_dwordx4 v40, s[84:87], s88 offen offset:1024 lds\n"
"buffer_load_dwordx4 v41, s[84:87], s88 offen offset:2048 lds\n"
"buffer_load_dwordx4 v42, s[84:87], s88 offen offset:3072 lds\n"
"s_add_u32 s88, s88, 458752\n"
"s_add_u32 m0, s91, 139264\n"
"s_nop 0\n"
"buffer_load_dwordx4 v43, s[80:83], s89 offen lds\n"
"buffer_load_dwordx4 v44, s[80:83], s89 offen offset:1024 lds\n"
"buffer_load_dwordx4 v45, s[80:83], s89 offen offset:2048 lds\n"
"buffer_load_dwordx4 v46, s[80:83], s89 offen offset:3072 lds\n"
"s_add_u32 m0, s91, 143360\n"
"s_nop 0\n"
"buffer_load_dwordx4 v47, s[80:83], s89 offen lds\n"
"s_add_u32 s89, s89, 128\n"
"v_lshrrev_b32 v4, 4, v0\n"
"v_bfe_u32 v5, v0, 2, 2\n"
"v_and_b32 v6, 3, v0\n"
"v_lshlrev_b32 v4, 12, v4\n"
"v_lshl_add_u32 v4, v5, 9, v4\n"
"v_lshl_add_u32 v4, v6, 4, v4\n"
"v_lshl_add_u32 v4, v5, 6, v4\n"
"s_and_b32 s60, s70, 1\n"
"s_lshl_b32 s60, s60, 8\n"
"s_add_u32 s60, s60, 106496\n"
"v_add_u32 v33, s60, v4\n"
"v_xor_b32 v34, 64, v33\n"
"v_xor_b32 v35, 128, v33\n"
"v_xor_b32 v36, 192, v33\n"
"v_and_b32 v4, 15, v0\n"
"v_lshrrev_b32 v5, 1, v4\n"
"v_lshrrev_b32 v6, 4, v0\n"
"v_xor_b32 v5, v5, v6\n"
"v_lshlrev_b32 v5, 4, v5\n"
"v_lshl_add_u32 v4, v4, 7, v5\n"
"s_lshr_b32 s60, s70, 1\n"
"s_mul_i32 s60, s60, 10240\n"
"s_add_u32 s60, s60, 139264\n"
"v_add_u32 v37, s60, v4\n"
"v_xor_b32 v38, 64, v37\n"
"s_getpc_b64 s[64:65]\n"
"PC0_%=:\n"
"s_add_u32 m0, s90, 0\n"
"s_nop 0\n"
"buffer_load_dwordx4 v39, s[84:87], s88 offen lds\n"
"buffer_load_dwordx4 v40, s[84:87], s88 offen offset:1024 lds\n"
"buffer_load_dwordx4 v41, s[84:87], s88 offen offset:2048 lds\n"
"buffer_load_dwordx4 v42, s[84:87], s88 offen offset:3072 lds\n"
"s_add_u32 s88, s88, 65536\n"
"s_add_u32 m0, s90, 4096\n"
"s_nop 0\n"
"buffer_load_dwordx4 v39, s[84:87], s88 offen lds\n"
"buffer_load_dwordx4 v40, s[84:87], s88 offen offset:1024 lds\n"
"buffer_load_dwordx4 v41, s[84:87], s88 offen offset:2048 lds\n"
"buffer_load_dwordx4 v42, s[84:87], s88 offen offset:3072 lds\n"
"s_add_u32 s88, s88, 458752\n"
"s_add_u32 m0, s91, 32768\n"
"s_nop 0\n"
"buffer_load_dwordx4 v43, s[80:83], s89 offen lds\n"
"buffer_load_dwordx4 v44, s[80:83], s89 offen offset:1024 lds\n"
"buffer_load_dwordx4 v45, s[80:83], s89 offen offset:2048 lds\n"
"buffer_load_dwordx4 v46, s[80:83], s89 offen offset:3072 lds\n"
"s_add_u32 m0, s91, 36864\n"
"s_nop 0\n"
"buffer_load_dwordx4 v47, s[80:83], s89 offen lds\n"
"s_add_u32 s89, s89, 128\n"
"s_waitcnt vmcnt(13)\n"
"s_barrier\n"
"ds_read_b128 a[20:23], v37 offset:0\n"
"ds_read_b128 a[24:27], v37 offset:2048\n"
"ds_read_b128 a[28:31], v37 offset:4096\n"
"ds_read_b128 a[32:35], v37 offset:6144\n"
"ds_read_b128 a[36:39], v37 offset:8192\n"
"ds_read_b64_tr_b16 a[0:1], v33 offset:0\n"
"ds_read_b64_tr_b16 a[2:3], v33 offset:2048\n"
"ds_read_b64_tr_b16 a[4:5], v33 offset:8\n"
"ds_read_b64_tr_b16 a[6:7], v33 offset:2056\n"
"ds_read_b64_tr_b16 a[8:9], v34 offset:0\n"
"ds_read_b64_tr_b16 a[10:11], v34 offset:2048\n"
"ds_read_b64_tr_b16 a[12:13], v34 offset:8\n"
"ds_read_b64_tr_b16 a[14:15], v34 offset:2056\n"
"s_waitcnt lgkmcnt(6)\n"
"v_mfma_f32_16x16x32_bf16 v[48:51], a[0:3], a[20:23], 0\n"
"s_add_u32 m0, s90, 53248\n"
"ds_read_b64_tr_b16 a[16:17], v35 offset:0\n"
"s_lshl_b32 s60, s70, 13\n"
"v_mfma_f32_16x16x32_bf16 v[52:55], a[0:3], a[24:27], 0\n"
"ds_read_b64_tr_b16 a[18:19], v35 offset:2048\n"
"v_lshlrev_b32 v208, 7, v0\n"
"v_mfma_f32_16x16x32_bf16 v[56:59], a[0:3], a[28:31], 0\n"
"buffer_load_dwordx4 v39, s[84:87], s88 offen lds\n"
"v_add_u32 v208, s60, v208\n"
"v_mfma_f32_16x16x32_bf16 v[60:63], a[0:3], a[32:35], 0\n"
"s_mov_b32 s60, PC1_%=-PC0_%=-4\n"
"v_min_u32 v209, s60, v208\n"
"v_mfma_f32_16x16x32_bf16 v[64:67], a[0:3], a[36:39], 0\n"
"s_waitcnt lgkmcnt(2)\n"
"buffer_load_dwordx4 v40, s[84:87], s88 offen offset:1024 lds\n"
"v_mfma_f32_16x16x32_bf16 v[68:71], a[4:7], a[20:23], 0\n"
"ds_read_b64_tr_b16 a[0:1], v35 offset:8\n"
"v_mfma_f32_16x16x32_bf16 v[72:75], a[4:7], a[24:27], 0\n"
"ds_read_b64_tr_b16 a[2:3], v35 offset:2056\n"
"v_mfma_f32_16x16x32_bf16 v[76:79], a[4:7], a[28:31], 0\n"
"global_load_dword v210, v209, s[64:65]\n"
"v_mfma_f32_16x16x32_bf16 v[80:83], a[4:7], a[32:35], 0\n"
"buffer_load_dwordx4 v41, s[84:87], s88 offen offset:2048 lds\n"
"s_lshl3_add_u32 s60, s63, s71\n"
"v_mfma_f32_16x16x32_bf16 v[84:87], a[4:7], a[36:39], 0\n"
"s_lshl1_add_u32 s60, s60, s96\n"
"s_lshr_b32 s59, s60, 13\n"
"v_mfma_f32_16x16x32_bf16 v[88:91], a[8:11], a[20:23], 0\n"
"ds_read_b64_tr_b16 a[4:5], v36 offset:0\n"
"s_lshl_b32 s60, s60, 19\n"
"v_mfma_f32_16x16x32_bf16 v[92:95], a[8:11], a[24:27], 0\n"
"ds_read_b64_tr_b16 a[6:7], v36 offset:2048\n"
"s_add_u32 s36, s18, s60\n"
"s_addc_u32 s37, s19, s59\n"
"v_mfma_f32_16x16x32_bf16 v[96:99], a[8:11], a[28:31], 0\n"
"buffer_load_dwordx4 v42, s[84:87], s88 offen offset:3072 lds\n"
"s_and_b32 s37, s37, 0xffff\n"
"v_mfma_f32_16x16x32_bf16 v[100:103], a[8:11], a[32:35], 0\n"
"s_add_u32 s88, s88, 65536\n"
"s_mov_b32 s38, 0x80000\n"
"s_mov_b32 s39, 0x20000\n"
"v_mfma_f32_16x16x32_bf16 v[104:107], a[8:11], a[36:39], 0\n"
"s_add_u32 s40, s20, s60\n"
"s_addc_u32 s41, s21, s59\n"
"v_mfma_f32_16x16x32_bf16 v[108:111], a[12:15], a[20:23], 0\n"
"s_add_u32 m0, s90, 57344\n"
"ds_read_b64_tr_b16 a[8:9], v36 offset:8\n"
"s_and_b32 s41, s41, 0xffff\n"
"v_mfma_f32_16x16x32_bf16 v[112:115], a[12:15], a[24:27], 0\n"
"ds_read_b64_tr_b16 a[10:11], v36 offset:2056\n"
"s_mov_b32 s42, 0x80000\n"
"v_mfma_f32_16x16x32_bf16 v[116:119], a[12:15], a[28:31], 0\n"
"s_mov_b32 s43, 0x20000\n"
"s_mul_i32 s60, s79, 229376\n"
"v_mfma_f32_16x16x32_bf16 v[120:123], a[12:15], a[32:35], 0\n"
"buffer_load_dwordx4 v39, s[84:87], s88 offen lds\n"
"s_add_u32 s20, s24, s60\n"
"s_addc_u32 s21, s25, 0\n"
"v_mfma_f32_16x16x32_bf16 v[124:127], a[12:15], a[36:39], 0\n"
"s_waitcnt lgkmcnt(2)\n"
"s_mul_i32 s60, %1, 49152\n"
"s_add_u32 s60, s60, 1048576\n"
"v_mfma_f32_16x16x32_bf16 v[128:131], a[16:19], a[20:23], 0\n"
"ds_read_b64_tr_b16 a[12:13], v33 offset:16384\n"
"s_add_u32 s16, s26, s60\n"
"s_addc_u32 s17, s27, 0\n"
"v_mfma_f32_16x16x32_bf16 v[132:135], a[16:19], a[24:27], 0\n"
"ds_read_b64_tr_b16 a[14:15], v33 offset:18432\n"
"s_xor_b32 s59, %1, 16\n"
"v_mfma_f32_16x16x32_bf16 v[136:139], a[16:19], a[28:31], 0\n"
"ds_read_b128 a[40:43], v38 offset:0\n"
"s_mul_i32 s60, s59, 49152\n"
"v_mfma_f32_16x16x32_bf16 v[140:143], a[16:19], a[32:35], 0\n"
"ds_read_b128 a[44:47], v38 offset:2048\n"
"s_add_u32 s60, s60, 1048576\n"
"v_mfma_f32_16x16x32_bf16 v[144:147], a[16:19], a[36:39], 0\n"
"buffer_load_dwordx4 v40, s[84:87], s88 offen offset:1024 lds\n"
"s_add_u32 s18, s26, s60\n"
"s_addc_u32 s19, s27, 0\n"
"v_mfma_f32_16x16x32_bf16 v[148:151], a[0:3], a[20:23], 0\n"
"ds_read_b64_tr_b16 a[16:17], v33 offset:16392\n"
"s_lshl_b32 s60, s59, 8\n"
"v_mfma_f32_16x16x32_bf16 v[152:155], a[0:3], a[24:27], 0\n"
"ds_read_b64_tr_b16 a[18:19], v33 offset:18440\n"
"s_add_u32 s30, s26, s60\n"
"s_addc_u32 s31, s27, 0\n"
"v_mfma_f32_16x16x32_bf16 v[156:159], a[0:3], a[28:31], 0\n"
"ds_read_b128 a[48:51], v38 offset:4096\n"
"s_lshl_b32 s60, %1, 8\n"
"v_mfma_f32_16x16x32_bf16 v[160:163], a[0:3], a[32:35], 0\n"
"ds_read_b128 a[52:55], v38 offset:6144\n"
"s_add_u32 s24, s26, s60\n"
"s_addc_u32 s25, s27, 0\n"
"v_mfma_f32_16x16x32_bf16 v[164:167], a[0:3], a[36:39], 0\n"
"s_and_b32 s60, s60, 0xf000\n"
"s_add_u32 s32, s26, s60\n"
"s_addc_u32 s33, s27, 0\n"
"v_mfma_f32_16x16x32_bf16 v[168:171], a[4:7], a[20:23], 0\n"
"ds_read_b64_tr_b16 a[0:1], v34 offset:16384\n"
"s_lshr_b32 s60, s60, 4\n"
"v_mfma_f32_16x16x32_bf16 v[172:175], a[4:7], a[24:27], 0\n"
"ds_read_b64_tr_b16 a[2:3], v34 offset:18432\n"
"s_add_u32 s60, s60, 65536\n"
"v_mfma_f32_16x16x32_bf16 v[176:179], a[4:7], a[28:31], 0\n"
"ds_read_b128 a[56:59], v38 offset:8192\n"
"s_add_u32 s94, s26, s60\n"
"s_addc_u32 s95, s27, 0\n"
"v_mfma_f32_16x16x32_bf16 v[180:183], a[4:7], a[32:35], 0\n"
"s_lshr_b32 s59, %1, 4\n"
"s_mul_i32 s97, s59, 14336\n"
"v_mfma_f32_16x16x32_bf16 v[184:187], a[4:7], a[36:39], 0\n"
"s_waitcnt lgkmcnt(3)\n"
"buffer_load_dwordx4 v41, s[84:87], s88 offen offset:2048 lds\n"
"s_mul_i32 s60, s59, 71680\n"
"v_mfma_f32_16x16x32_bf16 v[188:191], a[8:11], a[20:23], 0\n"
"ds_read_b64_tr_b16 a[4:5], v34 offset:16392\n"
"s_add_u32 s28, s28, s60\n"
"s_addc_u32 s29, s29, 0\n"
"v_mfma_f32_16x16x32_bf16 v[192:195], a[8:11], a[24:27], 0\n"
"ds_read_b64_tr_b16 a[6:7], v34 offset:18440\n"
"s_add_u32 s60, s60, 16777216\n"
"v_mfma_f32_16x16x32_bf16 v[196:199], a[8:11], a[28:31], 0\n"
"s_add_u32 s34, s26, s60\n"
"s_addc_u32 s35, s27, 0\n"
"v_mfma_f32_16x16x32_bf16 v[200:203], a[8:11], a[32:35], 0\n"
"buffer_load_dwordx4 v42, s[84:87], s88 offen offset:3072 lds\n"
"s_mul_i32 s60, s79, 1146880\n"
"v_mfma_f32_16x16x32_bf16 v[204:207], a[8:11], a[36:39], 0\n"
"s_mul_i32 s59, s70, 17920\n"
"s_add_u32 s60, s60, s59\n"
"v_mfma_f32_16x16x32_bf16 v[48:51], a[12:15], a[40:43], v[48:51]\n"
"s_add_u32 s88, s88, 458752\n"
"ds_read_b64_tr_b16 a[8:9], v35 offset:16384\n"
"s_add_u32 s22, s34, s60\n"
"s_addc_u32 s23, s35, 0\n"
"v_mfma_f32_16x16x32_bf16 v[52:55], a[12:15], a[44:47], v[52:55]\n"
"s_add_u32 m0, s91, 86016\n"
"ds_read_b64_tr_b16 a[10:11], v35 offset:18432\n"
"s_and_b32 s98, %1, 15\n"
"v_mfma_f32_16x16x32_bf16 v[56:59], a[12:15], a[48:51], v[56:59]\n"
"buffer_load_dwordx4 v43, s[80:83], s89 offen lds\n"
"s_lshl_b32 s98, s98, 2\n"
"v_mfma_f32_16x16x32_bf16 v[60:63], a[12:15], a[52:55], v[60:63]\n"
"s_waitcnt lgkmcnt(2)\n"
"v_lshrrev_b32 v4, 5, v0\n"
"s_lshl_b32 s60, s70, 3\n"
"v_mfma_f32_16x16x32_bf16 v[64:67], a[12:15], a[56:59], v[64:67]\n"
"buffer_load_dwordx4 v44, s[80:83], s89 offen offset:1024 lds\n"
"v_or_b32 v5, s60, v4\n"
"v_mfma_f32_16x16x32_bf16 v[68:71], a[16:19], a[40:43], v[68:71]\n"
"ds_read_b64_tr_b16 a[12:13], v35 offset:16392\n"
"v_and_b32 v6, 31, v0\n"
"v_mfma_f32_16x16x32_bf16 v[72:75], a[16:19], a[44:47], v[72:75]\n"
"ds_read_b64_tr_b16 a[14:15], v35 offset:18440\n"
"v_xor_b32 v5, v5, v6\n"
"v_mfma_f32_16x16x32_bf16 v[76:79], a[16:19], a[48:51], v[76:79]\n"
"v_lshlrev_b32 v5, 4, v5\n"
"v_lshl_add_u32 v17, v4, 9, v5\n"
"v_mfma_f32_16x16x32_bf16 v[80:83], a[16:19], a[52:55], v[80:83]\n"
"buffer_load_dwordx4 v45, s[80:83], s89 offen offset:2048 lds\n"
"v_xor_b32 v18, 32, v17\n"
"v_mfma_f32_16x16x32_bf16 v[84:87], a[16:19], a[56:59], v[84:87]\n"
"v_xor_b32 v19, 64, v17\n"
"v_xor_b32 v20, 96, v17\n"
"v_mfma_f32_16x16x32_bf16 v[88:91], a[0:3], a[40:43], v[88:91]\n"
"ds_read_b64_tr_b16 a[16:17], v36 offset:16384\n"
"v_lshrrev_b32 v4, 4, v0\n"
"v_mfma_f32_16x16x32_bf16 v[92:95], a[0:3], a[44:47], v[92:95]\n"
"ds_read_b64_tr_b16 a[18:19], v36 offset:18432\n"
"v_and_b32 v6, 15, v0\n"
"v_mfma_f32_16x16x32_bf16 v[96:99], a[0:3], a[48:51], v[96:99]\n"
"buffer_load_dwordx4 v46, s[80:83], s89 offen offset:3072 lds\n"
"v_lshlrev_b32 v5, 1, v4\n"
"v_mfma_f32_16x16x32_bf16 v[100:103], a[0:3], a[52:55], v[100:103]\n"
"s_add_u32 m0, s91, 90112\n"
"v_xor_b32 v5, v5, v6\n"
"v_lshlrev_b32 v5, 4, v5\n"
"v_mfma_f32_16x16x32_bf16 v[104:107], a[0:3], a[56:59], v[104:107]\n"
"buffer_load_dwordx4 v47, s[80:83], s89 offen lds\n"
"v_lshl_add_u32 v13, v4, 9, v5\n"
"v_mfma_f32_16x16x32_bf16 v[108:111], a[4:7], a[40:43], v[108:111]\n"
"s_add_u32 s89, s89, 128\n"
"ds_read_b64_tr_b16 a[0:1], v36 offset:16392\n"
"v_xor_b32 v14, 128, v13\n"
"v_mfma_f32_16x16x32_bf16 v[112:115], a[4:7], a[44:47], v[112:115]\n"
"ds_read_b64_tr_b16 a[2:3], v36 offset:18440\n"
"v_add_u32 v14, 1024, v14\n"
"v_mfma_f32_16x16x32_bf16 v[116:119], a[4:7], a[48:51], v[116:119]\n"
"v_add_u32 v15, 2048, v13\n"
"v_add_u32 v16, 2048, v14\n"
"v_mfma_f32_16x16x32_bf16 v[120:123], a[4:7], a[52:55], v[120:123]\n"
"s_lshl_b32 s44, s70, 12\n"
"s_add_u32 s45, s44, 16384\n"
"v_mfma_f32_16x16x32_bf16 v[124:127], a[4:7], a[56:59], v[124:127]\n"
"s_waitcnt lgkmcnt(2)\n"
"s_and_b32 s60, s70, 1\n"
"s_lshl_b32 s60, s60, 14\n"
"v_mfma_f32_16x16x32_bf16 v[128:131], a[8:11], a[40:43], v[128:131]\n"
"s_lshr_b32 s59, s70, 1\n"
"s_mul_i32 s46, s59, 8448\n"
"v_mfma_f32_16x16x32_bf16 v[132:135], a[8:11], a[44:47], v[132:135]\n"
"s_add_u32 s46, s46, s60\n"
"s_xor_b32 s47, s46, 8192\n"
"v_mfma_f32_16x16x32_bf16 v[136:139], a[8:11], a[48:51], v[136:139]\n"
"s_waitcnt vmcnt(14) lgkmcnt(0)\n"
"s_barrier\n"
"v_mfma_f32_16x16x32_bf16 v[140:143], a[8:11], a[52:55], v[140:143]\n"
"s_add_u32 m0, s90, 106496\n"
"v_subrev_u32 v33, 106496, v33\n"
"v_subrev_u32 v34, 106496, v34\n"
"v_mfma_f32_16x16x32_bf16 v[144:147], a[8:11], a[56:59], v[144:147]\n"
"v_subrev_u32 v35, 106496, v35\n"
"v_subrev_u32 v36, 106496, v36\n"
"v_mfma_f32_16x16x32_bf16 v[148:151], a[12:15], a[40:43], v[148:151]\n"
"v_subrev_u32 v37, 106496, v37\n"
"v_subrev_u32 v38, 106496, v38\n"
"v_mfma_f32_16x16x32_bf16 v[152:155], a[12:15], a[44:47], v[152:155]\n"
"ds_read_b128 a[20:23], v37 offset:0\n"
"s_lshl_b32 s60, s70, 12\n"
"v_mfma_f32_16x16x32_bf16 v[156:159], a[12:15], a[48:51], v[156:159]\n"
"ds_read_b64_tr_b16 a[4:5], v33 offset:0\n"
"s_add_u32 s48, s60, 0\n"
"v_mfma_f32_16x16x32_bf16 v[160:163], a[12:15], a[52:55], v[160:163]\n"
"ds_read_b64_tr_b16 a[6:7], v33 offset:2048\n"
"s_add_u32 s49, s60, 32768\n"
"v_mfma_f32_16x16x32_bf16 v[164:167], a[12:15], a[56:59], v[164:167]\n"
"ds_read_b128 a[24:27], v37 offset:2048\n"
"s_add_u32 s50, s60, 16384\n"
"v_mfma_f32_16x16x32_bf16 v[168:171], a[16:19], a[40:43], v[168:171]\n"
"ds_read_b128 a[28:31], v37 offset:4096\n"
"s_add_u32 s51, s60, 49152\n"
"v_mfma_f32_16x16x32_bf16 v[172:175], a[16:19], a[44:47], v[172:175]\n"
"ds_read_b128 a[32:35], v37 offset:6144\n"
"s_add_u32 s52, s60, 65536\n"
"v_mfma_f32_16x16x32_bf16 v[176:179], a[16:19], a[48:51], v[176:179]\n"
"ds_read_b128 a[36:39], v37 offset:8192\n"
"s_add_u32 s53, s60, 81920\n"
"v_mfma_f32_16x16x32_bf16 v[180:183], a[16:19], a[52:55], v[180:183]\n"
"ds_read_b64_tr_b16 a[8:9], v33 offset:8\n"
"s_add_u32 s54, s60, 98304\n"
"v_mfma_f32_16x16x32_bf16 v[184:187], a[16:19], a[56:59], v[184:187]\n"
"ds_read_b64_tr_b16 a[10:11], v33 offset:2056\n"
"s_add_u32 s55, s60, 114688\n"
"v_mfma_f32_16x16x32_bf16 v[188:191], a[0:3], a[40:43], v[188:191]\n"
"ds_read_b64_tr_b16 a[12:13], v34 offset:0\n"
"s_and_b32 s60, s70, 1\n"
"v_mfma_f32_16x16x32_bf16 v[192:195], a[0:3], a[44:47], v[192:195]\n"
"ds_read_b64_tr_b16 a[14:15], v34 offset:2048\n"
"s_lshl_b32 s60, s60, 13\n"
"v_mfma_f32_16x16x32_bf16 v[196:199], a[0:3], a[48:51], v[196:199]\n"
"ds_read_b64_tr_b16 a[16:17], v34 offset:8\n"
"v_xor_b32 v5, v4, v6\n"
"v_mfma_f32_16x16x32_bf16 v[200:203], a[0:3], a[52:55], v[200:203]\n"
"ds_read_b64_tr_b16 a[18:19], v34 offset:2056\n"
"v_lshlrev_b32 v5, 4, v5\n"
"v_mfma_f32_16x16x32_bf16 v[204:207], a[0:3], a[56:59], v[204:207]\n"
"s_waitcnt lgkmcnt(4)\n"
"v_lshl_add_u32 v5, v6, 9, v5\n"
"v_add_u32 v29, s60, v5\n"
"v_mfma_f32_16x16x32_bf16 v[48:51], a[4:7], a[20:23], v[48:51]\n"
"ds_read_b64_tr_b16 a[0:1], v35 offset:0\n"
"v_xor_b32 v30, 64, v29\n"
"v_mfma_f32_16x16x32_bf16 v[52:55], a[4:7], a[24:27], v[52:55]\n"
"ds_read_b64_tr_b16 a[2:3], v35 offset:2048\n"
"v_xor_b32 v31, 128, v29\n"
"v_mfma_f32_16x16x32_bf16 v[56:59], a[4:7], a[28:31], v[56:59]\n"
"buffer_load_dwordx4 v39, s[84:87], s88 offen lds\n"
"v_xor_b32 v32, 192, v29\n"
"v_mfma_f32_16x16x32_bf16 v[60:63], a[4:7], a[32:35], v[60:63]\n"
"v_lshrrev_b32 v5, 2, v0\n"
"v_and_b32 v1, 7, v5\n"
"v_mfma_f32_16x16x32_bf16 v[64:67], a[4:7], a[36:39], v[64:67]\n"
"buffer_load_dwordx4 v40, s[84:87], s88 offen offset:1024 lds\n"
"v_lshlrev_b32 v1, 5, v1\n"
"v_mfma_f32_16x16x32_bf16 v[68:71], a[8:11], a[20:23], v[68:71]\n"
"ds_read_b64_tr_b16 a[4:5], v35 offset:8\n"
"v_lshl_add_u32 v1, v5, 8, v1\n"
"v_mfma_f32_16x16x32_bf16 v[72:75], a[8:11], a[24:27], v[72:75]\n"
"ds_read_b64_tr_b16 a[6:7], v35 offset:2056\n"
"v_and_b32 v5, 3, v0\n"
"v_mfma_f32_16x16x32_bf16 v[76:79], a[8:11], a[28:31], v[76:79]\n"
"v_lshl_add_u32 v1, v5, 3, v1\n"
"s_add_u32 s60, s60, 65536\n"
"v_mfma_f32_16x16x32_bf16 v[80:83], a[8:11], a[32:35], v[80:83]\n"
"buffer_load_dwordx4 v41, s[84:87], s88 offen offset:2048 lds\n"
"v_add_u32 v21, s60, v1\n"
"v_mfma_f32_16x16x32_bf16 v[84:87], a[8:11], a[36:39], v[84:87]\n"
"s_waitcnt lgkmcnt(2)\n"
"v_xor_b32 v22, 32, v21\n"
"v_xor_b32 v23, 64, v21\n"
"v_mfma_f32_16x16x32_bf16 v[88:91], a[12:15], a[20:23], v[88:91]\n"
"ds_read_b64_tr_b16 a[8:9], v36 offset:0\n"
"v_xor_b32 v24, 96, v21\n"
"v_mfma_f32_16x16x32_bf16 v[92:95], a[12:15], a[24:27], v[92:95]\n"
"ds_read_b64_tr_b16 a[10:11], v36 offset:2048\n"
"v_xor_b32 v25, 128, v21\n"
"v_mfma_f32_16x16x32_bf16 v[96:99], a[12:15], a[28:31], v[96:99]\n"
"buffer_load_dwordx4 v42, s[84:87], s88 offen offset:3072 lds\n"
"v_xor_b32 v26, 160, v21\n"
"v_mfma_f32_16x16x32_bf16 v[100:103], a[12:15], a[32:35], v[100:103]\n"
"s_add_u32 s88, s88, 65536\n"
"v_xor_b32 v27, 192, v21\n"
"v_xor_b32 v28, 224, v21\n"
"v_mfma_f32_16x16x32_bf16 v[104:107], a[12:15], a[36:39], v[104:107]\n"
"s_mul_i32 s60, s70, 5120\n"
"s_add_u32 s60, s60, 131072\n"
"v_mfma_f32_16x16x32_bf16 v[108:111], a[16:19], a[20:23], v[108:111]\n"
"s_add_u32 m0, s90, 110592\n"
"ds_read_b64_tr_b16 a[12:13], v36 offset:8\n"
"s_xor_b32 s59, s70, 1\n"
"v_mfma_f32_16x16x32_bf16 v[112:115], a[16:19], a[24:27], v[112:115]\n"
"ds_read_b64_tr_b16 a[14:15], v36 offset:2056\n"
"s_mul_i32 s58, s59, 5120\n"
"v_mfma_f32_16x16x32_bf16 v[116:119], a[16:19], a[28:31], v[116:119]\n"
"s_add_u32 s58, s58, 131072\n"
"v_lshlrev_b32 v1, 4, v0\n"
"v_mfma_f32_16x16x32_bf16 v[120:123], a[16:19], a[32:35], v[120:123]\n"
"buffer_load_dwordx4 v39, s[84:87], s88 offen lds\n"
"v_add_u32 v12, s60, v1\n"
"v_mfma_f32_16x16x32_bf16 v[124:127], a[16:19], a[36:39], v[124:127]\n"
"v_add_u32 v11, s58, v1\n"
"s_mul_i32 s60, s70, 1536\n"
"v_mfma_f32_16x16x32_bf16 v[128:131], a[0:3], a[20:23], v[128:131]\n"
"ds_read_b64_tr_b16 a[16:17], v33 offset:16384\n"
"s_add_u32 s60, s60, 151552\n"
"v_mfma_f32_16x16x32_bf16 v[132:135], a[0:3], a[24:27], v[132:135]\n"
"ds_read_b64_tr_b16 a[18:19], v33 offset:18432\n"
"s_mul_i32 s58, s59, 1536\n"
"v_mfma_f32_16x16x32_bf16 v[136:139], a[0:3], a[28:31], v[136:139]\n"
"ds_read_b128 a[40:43], v38 offset:0\n"
"s_add_u32 s58, s58, 151552\n"
"v_mfma_f32_16x16x32_bf16 v[140:143], a[0:3], a[32:35], v[140:143]\n"
"ds_read_b128 a[44:47], v38 offset:2048\n"
"v_lshlrev_b32 v1, 2, v0\n"
"v_mfma_f32_16x16x32_bf16 v[144:147], a[0:3], a[36:39], v[144:147]\n"
"s_waitcnt lgkmcnt(4)\n"
"buffer_load_dwordx4 v40, s[84:87], s88 offen offset:1024 lds\n"
"v_add_u32 v8, s60, v1\n"
"v_mfma_f32_16x16x32_bf16 v[148:151], a[4:7], a[20:23], v[148:151]\n"
"ds_read_b64_tr_b16 a[0:1], v33 offset:16392\n"
"v_add_u32 v7, s58, v1\n"
"v_mfma_f32_16x16x32_bf16 v[152:155], a[4:7], a[24:27], v[152:155]\n"
"ds_read_b64_tr_b16 a[2:3], v33 offset:18440\n"
"v_mov_b32 v1, 0\n"
"v_mfma_f32_16x16x32_bf16 v[156:159], a[4:7], a[28:31], v[156:159]\n"
"ds_read_b128 a[48:51], v38 offset:4096\n"
"s_mov_b32 s56, 0\n"
"v_mfma_f32_16x16x32_bf16 v[160:163], a[4:7], a[32:35], v[160:163]\n"
"ds_read_b128 a[52:55], v38 offset:6144\n"
"s_mov_b32 s57, 0\n"
"v_mfma_f32_16x16x32_bf16 v[164:167], a[4:7], a[36:39], v[164:167]\n"
"s_mov_b32 s58, 0\n"
"s_mov_b32 s62, 0x41800000\n"
"v_mfma_f32_16x16x32_bf16 v[168:171], a[8:11], a[20:23], v[168:171]\n"
"ds_read_b64_tr_b16 a[4:5], v34 offset:16384\n"
"s_mov_b32 s72, 0x3db8aa3b\n"
"v_mfma_f32_16x16x32_bf16 v[172:175], a[8:11], a[24:27], v[172:175]\n"
"ds_read_b64_tr_b16 a[6:7], v34 offset:18432\n"
"s_mov_b32 s73, 0x3db8aa3b\n"
"v_mfma_f32_16x16x32_bf16 v[176:179], a[8:11], a[28:31], v[176:179]\n"
"ds_read_b128 a[56:59], v38 offset:8192\n"
"s_mul_i32 s60, s70, 20480\n"
"v_mfma_f32_16x16x32_bf16 v[180:183], a[8:11], a[32:35], v[180:183]\n"
"s_add_u32 s60, s60, 81920\n"
"s_xor_b32 s59, s70, 1\n"
"v_mfma_f32_16x16x32_bf16 v[184:187], a[8:11], a[36:39], v[184:187]\n"
"buffer_load_dwordx4 v41, s[84:87], s88 offen offset:2048 lds\n"
"s_mul_i32 s59, s59, 20480\n"
"v_mfma_f32_16x16x32_bf16 v[188:191], a[12:15], a[20:23], v[188:191]\n"
"ds_read_b64_tr_b16 a[8:9], v34 offset:16392\n"
"s_add_u32 s59, s59, 81920\n"
"v_mfma_f32_16x16x32_bf16 v[192:195], a[12:15], a[24:27], v[192:195]\n"
"ds_read_b64_tr_b16 a[10:11], v34 offset:18440\n"
"v_lshlrev_b32 v1, 4, v0\n"
"v_mfma_f32_16x16x32_bf16 v[196:199], a[12:15], a[28:31], v[196:199]\n"
"v_add_u32 v2, s60, v1\n"
"v_add_u32 v3, s59, v1\n"
"v_mfma_f32_16x16x32_bf16 v[200:203], a[12:15], a[32:35], v[200:203]\n"
"buffer_load_dwordx4 v42, s[84:87], s88 offen offset:3072 lds\n"
"v_mov_b32 v212, 0\n"
"v_mfma_f32_16x16x32_bf16 v[204:207], a[12:15], a[36:39], v[204:207]\n"
"s_waitcnt lgkmcnt(2)\n"
"v_and_b32 v220, 15, v0\n"
"v_lshlrev_b32 v219, 2, v220\n"
"v_mfma_f32_16x16x32_bf16 v[48:51], a[16:19], a[40:43], v[48:51]\n"
"s_add_u32 s88, s88, 458752\n"
"ds_read_b64_tr_b16 a[12:13], v35 offset:16384\n"
"v_lshlrev_b32 v220, 8, v220\n"
"v_mfma_f32_16x16x32_bf16 v[52:55], a[16:19], a[44:47], v[52:55]\n"
"s_add_u32 m0, s91, 139264\n"
"ds_read_b64_tr_b16 a[14:15], v35 offset:18432\n"
"v_mfma_f32_16x16x32_bf16 v[56:59], a[16:19], a[48:51], v[56:59]\n"
"buffer_load_dwordx4 v43, s[80:83], s89 offen lds\n"
"v_mfma_f32_16x16x32_bf16 v[60:63], a[16:19], a[52:55], v[60:63]\n"
"global_load_dword v213, v212, s[24:25] sc1\n"
"v_mfma_f32_16x16x32_bf16 v[64:67], a[16:19], a[56:59], v[64:67]\n"
"buffer_load_dwordx4 v44, s[80:83], s89 offen offset:1024 lds\n"
"v_mfma_f32_16x16x32_bf16 v[68:71], a[0:3], a[40:43], v[68:71]\n"
"ds_read_b64_tr_b16 a[16:17], v35 offset:16392\n"
"v_mfma_f32_16x16x32_bf16 v[72:75], a[0:3], a[44:47], v[72:75]\n"
"ds_read_b64_tr_b16 a[18:19], v35 offset:18440\n"
"v_mfma_f32_16x16x32_bf16 v[76:79], a[0:3], a[48:51], v[76:79]\n"
"global_load_dword v214, v212, s[30:31] sc1\n"
"v_mfma_f32_16x16x32_bf16 v[80:83], a[0:3], a[52:55], v[80:83]\n"
"buffer_load_dwordx4 v45, s[80:83], s89 offen offset:2048 lds\n"
"v_mfma_f32_16x16x32_bf16 v[84:87], a[0:3], a[56:59], v[84:87]\n"
"global_load_dword v221, v219, s[94:95] sc1\n"
"v_mfma_f32_16x16x32_bf16 v[88:91], a[4:7], a[40:43], v[88:91]\n"
"ds_read_b64_tr_b16 a[0:1], v36 offset:16384\n"
"v_mfma_f32_16x16x32_bf16 v[92:95], a[4:7], a[44:47], v[92:95]\n"
"ds_read_b64_tr_b16 a[2:3], v36 offset:18432\n"
"v_mfma_f32_16x16x32_bf16 v[96:99], a[4:7], a[48:51], v[96:99]\n"
"buffer_load_dwordx4 v46, s[80:83], s89 offen offset:3072 lds\n"
"v_mfma_f32_16x16x32_bf16 v[100:103], a[4:7], a[52:55], v[100:103]\n"
"s_add_u32 m0, s91, 143360\n"
"v_mfma_f32_16x16x32_bf16 v[104:107], a[4:7], a[56:59], v[104:107]\n"
"s_waitcnt lgkmcnt(2)\n"
"buffer_load_dwordx4 v47, s[80:83], s89 offen lds\n"
"v_mfma_f32_16x16x32_bf16 v[108:111], a[8:11], a[40:43], v[108:111]\n"
"s_add_u32 s89, s89, 128\n"
"ds_read_b64_tr_b16 a[4:5], v36 offset:16392\n"
"v_mfma_f32_16x16x32_bf16 v[112:115], a[8:11], a[44:47], v[112:115]\n"
"ds_read_b64_tr_b16 a[6:7], v36 offset:18440\n"
"v_mfma_f32_16x16x32_bf16 v[116:119], a[8:11], a[48:51], v[116:119]\n"
"v_mfma_f32_16x16x32_bf16 v[120:123], a[8:11], a[52:55], v[120:123]\n"
"v_mfma_f32_16x16x32_bf16 v[124:127], a[8:11], a[56:59], v[124:127]\n"
"v_mfma_f32_16x16x32_bf16 v[128:131], a[12:15], a[40:43], v[128:131]\n"
"v_mfma_f32_16x16x32_bf16 v[132:135], a[12:15], a[44:47], v[132:135]\n"
"v_mfma_f32_16x16x32_bf16 v[136:139], a[12:15], a[48:51], v[136:139]\n"
"s_waitcnt vmcnt(16) lgkmcnt(0)\n"
"s_barrier\n"
"v_mfma_f32_16x16x32_bf16 v[140:143], a[12:15], a[52:55], v[140:143]\n"
"s_add_u32 m0, s90, 0\n"
"v_add_u32 v33, 53248, v33\n"
"v_add_u32 v34, 53248, v34\n"
"v_mfma_f32_16x16x32_bf16 v[144:147], a[12:15], a[56:59], v[144:147]\n"
"v_add_u32 v35, 53248, v35\n"
"v_add_u32 v36, 53248, v36\n"
"v_mfma_f32_16x16x32_bf16 v[148:151], a[16:19], a[40:43], v[148:151]\n"
"v_add_u32 v37, 53248, v37\n"
"v_add_u32 v38, 53248, v38\n"
"v_mfma_f32_16x16x32_bf16 v[152:155], a[16:19], a[44:47], v[152:155]\n"
"ds_read_b128 a[20:23], v37 offset:0\n"
"v_mfma_f32_16x16x32_bf16 v[156:159], a[16:19], a[48:51], v[156:159]\n"
"ds_read_b64_tr_b16 a[8:9], v33 offset:0\n"
"v_mfma_f32_16x16x32_bf16 v[160:163], a[16:19], a[52:55], v[160:163]\n"
"ds_read_b64_tr_b16 a[10:11], v33 offset:2048\n"
"v_mfma_f32_16x16x32_bf16 v[164:167], a[16:19], a[56:59], v[164:167]\n"
"ds_read_b128 a[24:27], v37 offset:2048\n"
"v_mfma_f32_16x16x32_bf16 v[168:171], a[0:3], a[40:43], v[168:171]\n"
"ds_read_b128 a[28:31], v37 offset:4096\n"
"v_mfma_f32_16x16x32_bf16 v[172:175], a[0:3], a[44:47], v[172:175]\n"
"ds_read_b128 a[32:35], v37 offset:6144\n"
"v_mfma_f32_16x16x32_bf16 v[176:179], a[0:3], a[48:51], v[176:179]\n"
"ds_read_b128 a[36:39], v37 offset:8192\n"
"v_mfma_f32_16x16x32_bf16 v[180:183], a[0:3], a[52:55], v[180:183]\n"
"ds_read_b64_tr_b16 a[12:13], v33 offset:8\n"
"v_mfma_f32_16x16x32_bf16 v[184:187], a[0:3], a[56:59], v[184:187]\n"
"ds_read_b64_tr_b16 a[14:15], v33 offset:2056\n"
"v_mfma_f32_16x16x32_bf16 v[188:191], a[4:7], a[40:43], v[188:191]\n"
"ds_read_b64_tr_b16 a[16:17], v34 offset:0\n"
"v_mfma_f32_16x16x32_bf16 v[192:195], a[4:7], a[44:47], v[192:195]\n"
"ds_read_b64_tr_b16 a[18:19], v34 offset:2048\n"
"v_mfma_f32_16x16x32_bf16 v[196:199], a[4:7], a[48:51], v[196:199]\n"
"ds_read_b64_tr_b16 a[0:1], v34 offset:8\n"
"v_mfma_f32_16x16x32_bf16 v[200:203], a[4:7], a[52:55], v[200:203]\n"
"ds_read_b64_tr_b16 a[2:3], v34 offset:2056\n"
"v_mfma_f32_16x16x32_bf16 v[204:207], a[4:7], a[56:59], v[204:207]\n"
"s_waitcnt lgkmcnt(6)\n"
"v_mfma_f32_16x16x32_bf16 v[48:51], a[8:11], a[20:23], v[48:51]\n"
"ds_read_b64_tr_b16 a[4:5], v35 offset:0\n"
"v_mfma_f32_16x16x32_bf16 v[52:55], a[8:11], a[24:27], v[52:55]\n"
"ds_read_b64_tr_b16 a[6:7], v35 offset:2048\n"
"v_mfma_f32_16x16x32_bf16 v[56:59], a[8:11], a[28:31], v[56:59]\n"
"buffer_load_dwordx4 v39, s[84:87], s88 offen lds\n"
"v_mfma_f32_16x16x32_bf16 v[60:63], a[8:11], a[32:35], v[60:63]\n"
"v_mfma_f32_16x16x32_bf16 v[64:67], a[8:11], a[36:39], v[64:67]\n"
"s_waitcnt lgkmcnt(4)\n"
"buffer_load_dwordx4 v40, s[84:87], s88 offen offset:1024 lds\n"
"v_mfma_f32_16x16x32_bf16 v[68:71], a[12:15], a[20:23], v[68:71]\n"
"ds_read_b64_tr_b16 a[8:9], v35 offset:8\n"
"v_mfma_f32_16x16x32_bf16 v[72:75], a[12:15], a[24:27], v[72:75]\n"
"ds_read_b64_tr_b16 a[10:11], v35 offset:2056\n"
"v_mfma_f32_16x16x32_bf16 v[76:79], a[12:15], a[28:31], v[76:79]\n"
"v_mfma_f32_16x16x32_bf16 v[80:83], a[12:15], a[32:35], v[80:83]\n"
"buffer_load_dwordx4 v41, s[84:87], s88 offen offset:2048 lds\n"
"v_mfma_f32_16x16x32_bf16 v[84:87], a[12:15], a[36:39], v[84:87]\n"
"v_mfma_f32_16x16x32_bf16 v[88:91], a[16:19], a[20:23], v[88:91]\n"
"ds_read_b64_tr_b16 a[12:13], v36 offset:0\n"
"v_mfma_f32_16x16x32_bf16 v[92:95], a[16:19], a[24:27], v[92:95]\n"
"ds_read_b64_tr_b16 a[14:15], v36 offset:2048\n"
"v_mfma_f32_16x16x32_bf16 v[96:99], a[16:19], a[28:31], v[96:99]\n"
"buffer_load_dwordx4 v42, s[84:87], s88 offen offset:3072 lds\n"
"v_mfma_f32_16x16x32_bf16 v[100:103], a[16:19], a[32:35], v[100:103]\n"
"s_add_u32 s88, s88, 65536\n"
"v_mfma_f32_16x16x32_bf16 v[104:107], a[16:19], a[36:39], v[104:107]\n"
"s_waitcnt lgkmcnt(2)\n"
"v_mfma_f32_16x16x32_bf16 v[108:111], a[0:3], a[20:23], v[108:111]\n"
"s_add_u32 m0, s90, 4096\n"
"ds_read_b64_tr_b16 a[16:17], v36 offset:8\n"
"v_mfma_f32_16x16x32_bf16 v[112:115], a[0:3], a[24:27], v[112:115]\n"
"ds_read_b64_tr_b16 a[18:19], v36 offset:2056\n"
"v_mfma_f32_16x16x32_bf16 v[116:119], a[0:3], a[28:31], v[116:119]\n"
"v_mfma_f32_16x16x32_bf16 v[120:123], a[0:3], a[32:35], v[120:123]\n"
"buffer_load_dwordx4 v39, s[84:87], s88 offen lds\n"
"v_mfma_f32_16x16x32_bf16 v[124:127], a[0:3], a[36:39], v[124:127]\n"
"v_mfma_f32_16x16x32_bf16 v[128:131], a[4:7], a[20:23], v[128:131]\n"
"ds_read_b64_tr_b16 a[0:1], v33 offset:16384\n"
"v_mfma_f32_16x16x32_bf16 v[132:135], a[4:7], a[24:27], v[132:135]\n"
"ds_read_b64_tr_b16 a[2:3], v33 offset:18432\n"
"v_mfma_f32_16x16x32_bf16 v[136:139], a[4:7], a[28:31], v[136:139]\n"
"ds_read_b128 a[40:43], v38 offset:0\n"
"v_mfma_f32_16x16x32_bf16 v[140:143], a[4:7], a[32:35], v[140:143]\n"
"ds_read_b128 a[44:47], v38 offset:2048\n"
"v_mfma_f32_16x16x32_bf16 v[144:147], a[4:7], a[36:39], v[144:147]\n"
"buffer_load_dwordx4 v40, s[84:87], s88 offen offset:1024 lds\n"
"v_mfma_f32_16x16x32_bf16 v[148:151], a[8:11], a[20:23], v[148:151]\n"
"ds_read_b64_tr_b16 a[4:5], v33 offset:16392\n"
"v_mfma_f32_16x16x32_bf16 v[152:155], a[8:11], a[24:27], v[152:155]\n"
"ds_read_b64_tr_b16 a[6:7], v33 offset:18440\n"
"v_mfma_f32_16x16x32_bf16 v[156:159], a[8:11], a[28:31], v[156:159]\n"
"ds_read_b128 a[48:51], v38 offset:4096\n"
"v_mfma_f32_16x16x32_bf16 v[160:163], a[8:11], a[32:35], v[160:163]\n"
"ds_read_b128 a[52:55], v38 offset:6144\n"
"v_mfma_f32_16x16x32_bf16 v[164:167], a[8:11], a[36:39], v[164:167]\n"
"s_waitcnt lgkmcnt(5)\n"
"v_mfma_f32_16x16x32_bf16 v[168:171], a[12:15], a[20:23], v[168:171]\n"
"ds_read_b64_tr_b16 a[8:9], v34 offset:16384\n"
"v_mfma_f32_16x16x32_bf16 v[172:175], a[12:15], a[24:27], v[172:175]\n"
"ds_read_b64_tr_b16 a[10:11], v34 offset:18432\n"
"v_mfma_f32_16x16x32_bf16 v[176:179], a[12:15], a[28:31], v[176:179]\n"
"ds_read_b128 a[56:59], v38 offset:8192\n"
"v_mfma_f32_16x16x32_bf16 v[180:183], a[12:15], a[32:35], v[180:183]\n"
"v_mfma_f32_16x16x32_bf16 v[184:187], a[12:15], a[36:39], v[184:187]\n"
"buffer_load_dwordx4 v41, s[84:87], s88 offen offset:2048 lds\n"
"v_mfma_f32_16x16x32_bf16 v[188:191], a[16:19], a[20:23], v[188:191]\n"
"ds_read_b64_tr_b16 a[12:13], v34 offset:16392\n"
"v_mfma_f32_16x16x32_bf16 v[192:195], a[16:19], a[24:27], v[192:195]\n"
"ds_read_b64_tr_b16 a[14:15], v34 offset:18440\n"
"v_mfma_f32_16x16x32_bf16 v[196:199], a[16:19], a[28:31], v[196:199]\n"
"v_mfma_f32_16x16x32_bf16 v[200:203], a[16:19], a[32:35], v[200:203]\n"
"buffer_load_dwordx4 v42, s[84:87], s88 offen offset:3072 lds\n"
"v_mfma_f32_16x16x32_bf16 v[204:207], a[16:19], a[36:39], v[204:207]\n"
"v_mfma_f32_16x16x32_bf16 v[48:51], a[0:3], a[40:43], v[48:51]\n"
"s_waitcnt lgkmcnt(3)\n"
"s_add_u32 s88, s88, 458752\n"
"ds_read_b64_tr_b16 a[16:17], v35 offset:16384\n"
"v_mfma_f32_16x16x32_bf16 v[52:55], a[0:3], a[44:47], v[52:55]\n"
"s_add_u32 m0, s91, 32768\n"
"ds_read_b64_tr_b16 a[18:19], v35 offset:18432\n"
"v_mfma_f32_16x16x32_bf16 v[56:59], a[0:3], a[48:51], v[56:59]\n"
"buffer_load_dwordx4 v43, s[80:83], s89 offen lds\n"
"v_mfma_f32_16x16x32_bf16 v[60:63], a[0:3], a[52:55], v[60:63]\n"
"s_waitcnt lgkmcnt(2)\n"
"v_mfma_f32_16x16x32_bf16 v[64:67], a[0:3], a[56:59], v[64:67]\n"
"buffer_load_dwordx4 v44, s[80:83], s89 offen offset:1024 lds\n"
"v_mfma_f32_16x16x32_bf16 v[68:71], a[4:7], a[40:43], v[68:71]\n"
"ds_read_b64_tr_b16 a[0:1], v35 offset:16392\n"
"v_mfma_f32_16x16x32_bf16 v[72:75], a[4:7], a[44:47], v[72:75]\n"
"ds_read_b64_tr_b16 a[2:3], v35 offset:18440\n"
"v_mfma_f32_16x16x32_bf16 v[76:79], a[4:7], a[48:51], v[76:79]\n"
"v_mfma_f32_16x16x32_bf16 v[80:83], a[4:7], a[52:55], v[80:83]\n"
"buffer_load_dwordx4 v45, s[80:83], s89 offen offset:2048 lds\n"
"v_mfma_f32_16x16x32_bf16 v[84:87], a[4:7], a[56:59], v[84:87]\n"
"v_mfma_f32_16x16x32_bf16 v[88:91], a[8:11], a[40:43], v[88:91]\n"
"ds_read_b64_tr_b16 a[4:5], v36 offset:16384\n"
"v_mfma_f32_16x16x32_bf16 v[92:95], a[8:11], a[44:47], v[92:95]\n"
"ds_read_b64_tr_b16 a[6:7], v36 offset:18432\n"
"v_mfma_f32_16x16x32_bf16 v[96:99], a[8:11], a[48:51], v[96:99]\n"
"buffer_load_dwordx4 v46, s[80:83], s89 offen offset:3072 lds\n"
"v_mfma_f32_16x16x32_bf16 v[100:103], a[8:11], a[52:55], v[100:103]\n"
"s_add_u32 m0, s91, 36864\n"
"v_mfma_f32_16x16x32_bf16 v[104:107], a[8:11], a[56:59], v[104:107]\n"
"buffer_load_dwordx4 v47, s[80:83], s89 offen lds\n"
"v_mfma_f32_16x16x32_bf16 v[108:111], a[12:15], a[40:43], v[108:111]\n"
"s_add_u32 s89, s89, 128\n"
"ds_read_b64_tr_b16 a[8:9], v36 offset:16392\n"
"v_mfma_f32_16x16x32_bf16 v[112:115], a[12:15], a[44:47], v[112:115]\n"
"ds_read_b64_tr_b16 a[10:11], v36 offset:18440\n"
"v_mfma_f32_16x16x32_bf16 v[116:119], a[12:15], a[48:51], v[116:119]\n"
"v_mfma_f32_16x16x32_bf16 v[120:123], a[12:15], a[52:55], v[120:123]\n"
"v_mfma_f32_16x16x32_bf16 v[124:127], a[12:15], a[56:59], v[124:127]\n"
"s_waitcnt lgkmcnt(2)\n"
"v_mfma_f32_16x16x32_bf16 v[128:131], a[16:19], a[40:43], v[128:131]\n"
"v_mfma_f32_16x16x32_bf16 v[132:135], a[16:19], a[44:47], v[132:135]\n"
"v_mfma_f32_16x16x32_bf16 v[136:139], a[16:19], a[48:51], v[136:139]\n"
"s_waitcnt vmcnt(13) lgkmcnt(0)\n"
"s_barrier\n"
"v_readfirstlane_b32 s76, v213\n"
"v_readfirstlane_b32 s77, v214\n"
"s_add_u32 s76, s76, 1\n"
"s_add_u32 s77, s77, 1\n"
"s_lshl_b32 s60, s70, 2\n"
"v_mov_b32 v213, s77\n"
"v_mov_b32 v212, s60\n"
"global_store_dword v212, v213, s[30:31] offset:128 sc1\n"
"v_add_u32 v221, 1, v221\n"
"s_lshr_b32 s60, s98, 2\n"
"v_add_u32 v220, s98, v220\n"
"v_add_u32 v223, 48128, v219\n"
"v_readlane_b32 s78, v221, s60\n"
"s_cmp_lg_u32 s70, 0\n"
"s_cbranch_scc1 NOGRP_%=\n"
"s_mov_b64 exec, 0xffff\n"
"global_store_dword v220, v221, s[32:33] offset:144 sc1\n"
"global_store_dword v223, v221, s[16:17] sc1\n"
"s_mov_b64 exec, -1\n"
"NOGRP_%=:\n"
"v_mfma_f32_16x16x32_bf16 v[140:143], a[16:19], a[52:55], v[140:143]\n"
"s_add_u32 m0, s90, 53248\n"
"v_add_u32 v33, 53248, v33\n"
"v_add_u32 v34, 53248, v34\n"
"v_mfma_f32_16x16x32_bf16 v[144:147], a[16:19], a[56:59], v[144:147]\n"
"v_add_u32 v35, 53248, v35\n"
"v_add_u32 v36, 53248, v36\n"
"v_mfma_f32_16x16x32_bf16 v[148:151], a[0:3], a[40:43], v[148:151]\n"
"v_add_u32 v37, 53248, v37\n"
"v_add_u32 v38, 53248, v38\n"
"v_mfma_f32_16x16x32_bf16 v[152:155], a[0:3], a[44:47], v[152:155]\n"
"ds_read_b128 a[20:23], v37 offset:0\n"
"v_mfma_f32_16x16x32_bf16 v[156:159], a[0:3], a[48:51], v[156:159]\n"
"ds_read_b64_tr_b16 a[12:13], v33 offset:0\n"
"v_mfma_f32_16x16x32_bf16 v[160:163], a[0:3], a[52:55], v[160:163]\n"
"ds_read_b64_tr_b16 a[14:15], v33 offset:2048\n"
"v_mfma_f32_16x16x32_bf16 v[164:167], a[0:3], a[56:59], v[164:167]\n"
"ds_read_b128 a[24:27], v37 offset:2048\n"
"v_mfma_f32_16x16x32_bf16 v[168:171], a[4:7], a[40:43], v[168:171]\n"
"ds_read_b128 a[28:31], v37 offset:4096\n"
"v_mfma_f32_16x16x32_bf16 v[172:175], a[4:7], a[44:47], v[172:175]\n"
"ds_read_b128 a[32:35], v37 offset:6144\n"
"v_mfma_f32_16x16x32_bf16 v[176:179], a[4:7], a[48:51], v[176:179]\n"
"ds_read_b128 a[36:39], v37 offset:8192\n"
"v_mfma_f32_16x16x32_bf16 v[180:183], a[4:7], a[52:55], v[180:183]\n"
"ds_read_b64_tr_b16 a[16:17], v33 offset:8\n"
"v_mfma_f32_16x16x32_bf16 v[184:187], a[4:7], a[56:59], v[184:187]\n"
"ds_read_b64_tr_b16 a[18:19], v33 offset:2056\n"
"v_mfma_f32_16x16x32_bf16 v[188:191], a[8:11], a[40:43], v[188:191]\n"
"ds_read_b64_tr_b16 a[0:1], v34 offset:0\n"
"v_mfma_f32_16x16x32_bf16 v[192:195], a[8:11], a[44:47], v[192:195]\n"
"ds_read_b64_tr_b16 a[2:3], v34 offset:2048\n"
"v_mfma_f32_16x16x32_bf16 v[196:199], a[8:11], a[48:51], v[196:199]\n"
"ds_read_b64_tr_b16 a[4:5], v34 offset:8\n"
"v_mfma_f32_16x16x32_bf16 v[200:203], a[8:11], a[52:55], v[200:203]\n"
"ds_read_b64_tr_b16 a[6:7], v34 offset:2056\n"
"v_mfma_f32_16x16x32_bf16 v[204:207], a[8:11], a[56:59], v[204:207]\n"
"s_waitcnt lgkmcnt(6)\n"
"v_mfma_f32_16x16x32_bf16 v[48:51], a[12:15], a[20:23], v[48:51]\n"
"ds_read_b64_tr_b16 a[8:9], v35 offset:0\n"
"v_mfma_f32_16x16x32_bf16 v[52:55], a[12:15], a[24:27], v[52:55]\n"
"ds_read_b64_tr_b16 a[10:11], v35 offset:2048\n"
"v_mfma_f32_16x16x32_bf16 v[56:59], a[12:15], a[28:31], v[56:59]\n"
"buffer_load_dwordx4 v39, s[84:87], s88 offen lds\n"
"v_mfma_f32_16x16x32_bf16 v[60:63], a[12:15], a[32:35], v[60:63]\n"
"v_mfma_f32_16x16x32_bf16 v[64:67], a[12:15], a[36:39], v[64:67]\n"
"s_waitcnt lgkmcnt(4)\n"
"buffer_load_dwordx4 v40, s[84:87], s88 offen offset:1024 lds\n"
"v_mfma_f32_16x16x32_bf16 v[68:71], a[16:19], a[20:23], v[68:71]\n"
"ds_read_b64_tr_b16 a[12:13], v35 offset:8\n"
"v_mfma_f32_16x16x32_bf16 v[72:75], a[16:19], a[24:27], v[72:75]\n"
"ds_read_b64_tr_b16 a[14:15], v35 offset:2056\n"
"v_mfma_f32_16x16x32_bf16 v[76:79], a[16:19], a[28:31], v[76:79]\n"
"v_mfma_f32_16x16x32_bf16 v[80:83], a[16:19], a[32:35], v[80:83]\n"
"buffer_load_dwordx4 v41, s[84:87], s88 offen offset:2048 lds\n"
"v_mfma_f32_16x16x32_bf16 v[84:87], a[16:19], a[36:39], v[84:87]\n"
"v_mfma_f32_16x16x32_bf16 v[88:91], a[0:3], a[20:23], v[88:91]\n"
"ds_read_b64_tr_b16 a[16:17], v36 offset:0\n"
"v_mfma_f32_16x16x32_bf16 v[92:95], a[0:3], a[24:27], v[92:95]\n"
"ds_read_b64_tr_b16 a[18:19], v36 offset:2048\n"
"v_mfma_f32_16x16x32_bf16 v[96:99], a[0:3], a[28:31], v[96:99]\n"
"buffer_load_dwordx4 v42, s[84:87], s88 offen offset:3072 lds\n"
"v_mfma_f32_16x16x32_bf16 v[100:103], a[0:3], a[32:35], v[100:103]\n"
"s_add_u32 s88, s88, 65536\n"
"v_mfma_f32_16x16x32_bf16 v[104:107], a[0:3], a[36:39], v[104:107]\n"
"s_waitcnt lgkmcnt(2)\n"
"v_mfma_f32_16x16x32_bf16 v[108:111], a[4:7], a[20:23], v[108:111]\n"
"s_add_u32 m0, s90, 57344\n"
"ds_read_b64_tr_b16 a[0:1], v36 offset:8\n"
"v_mfma_f32_16x16x32_bf16 v[112:115], a[4:7], a[24:27], v[112:115]\n"
"ds_read_b64_tr_b16 a[2:3], v36 offset:2056\n"
"v_mfma_f32_16x16x32_bf16 v[116:119], a[4:7], a[28:31], v[116:119]\n"
"v_mfma_f32_16x16x32_bf16 v[120:123], a[4:7], a[32:35], v[120:123]\n"
"buffer_load_dwordx4 v39, s[84:87], s88 offen lds\n"
"v_mfma_f32_16x16x32_bf16 v[124:127], a[4:7], a[36:39], v[124:127]\n"
"v_mfma_f32_16x16x32_bf16 v[128:131], a[8:11], a[20:23], v[128:131]\n"
"ds_read_b64_tr_b16 a[4:5], v33 offset:16384\n"
"v_mfma_f32_16x16x32_bf16 v[132:135], a[8:11], a[24:27], v[132:135]\n"
"ds_read_b64_tr_b16 a[6:7], v33 offset:18432\n"
"v_mfma_f32_16x16x32_bf16 v[136:139], a[8:11], a[28:31], v[136:139]\n"
"ds_read_b128 a[40:43], v38 offset:0\n"
"v_mfma_f32_16x16x32_bf16 v[140:143], a[8:11], a[32:35], v[140:143]\n"
"ds_read_b128 a[44:47], v38 offset:2048\n"
"v_mfma_f32_16x16x32_bf16 v[144:147], a[8:11], a[36:39], v[144:147]\n"
"buffer_load_dwordx4 v40, s[84:87], s88 offen offset:1024 lds\n"
"v_mfma_f32_16x16x32_bf16 v[148:151], a[12:15], a[20:23], v[148:151]\n"
"ds_read_b64_tr_b16 a[8:9], v33 offset:16392\n"
"v_mfma_f32_16x16x32_bf16 v[152:155], a[12:15], a[24:27], v[152:155]\n"
"ds_read_b64_tr_b16 a[10:11], v33 offset:18440\n"
"v_mfma_f32_16x16x32_bf16 v[156:159], a[12:15], a[28:31], v[156:159]\n"
"ds_read_b128 a[48:51], v38 offset:4096\n"
"v_mfma_f32_16x16x32_bf16 v[160:163], a[12:15], a[32:35], v[160:163]\n"
"ds_read_b128 a[52:55], v38 offset:6144\n"
"v_mfma_f32_16x16x32_bf16 v[164:167], a[12:15], a[36:39], v[164:167]\n"
"s_waitcnt lgkmcnt(5)\n"
"v_mfma_f32_16x16x32_bf16 v[168:171], a[16:19], a[20:23], v[168:171]\n"
"ds_read_b64_tr_b16 a[12:13], v34 offset:16384\n"
"v_mfma_f32_16x16x32_bf16 v[172:175], a[16:19], a[24:27], v[172:175]\n"
"ds_read_b64_tr_b16 a[14:15], v34 offset:18432\n"
"v_mfma_f32_16x16x32_bf16 v[176:179], a[16:19], a[28:31], v[176:179]\n"
"ds_read_b128 a[56:59], v38 offset:8192\n"
"v_mfma_f32_16x16x32_bf16 v[180:183], a[16:19], a[32:35], v[180:183]\n"
"v_mfma_f32_16x16x32_bf16 v[184:187], a[16:19], a[36:39], v[184:187]\n"
"buffer_load_dwordx4 v41, s[84:87], s88 offen offset:2048 lds\n"
"v_mfma_f32_16x16x32_bf16 v[188:191], a[0:3], a[20:23], v[188:191]\n"
"ds_read_b64_tr_b16 a[16:17], v34 offset:16392\n"
"v_mfma_f32_16x16x32_bf16 v[192:195], a[0:3], a[24:27], v[192:195]\n"
"ds_read_b64_tr_b16 a[18:19], v34 offset:18440\n"
"v_mfma_f32_16x16x32_bf16 v[196:199], a[0:3], a[28:31], v[196:199]\n"
"v_mfma_f32_16x16x32_bf16 v[200:203], a[0:3], a[32:35], v[200:203]\n"
"buffer_load_dwordx4 v42, s[84:87], s88 offen offset:3072 lds\n"
"v_mfma_f32_16x16x32_bf16 v[204:207], a[0:3], a[36:39], v[204:207]\n"
"v_mfma_f32_16x16x32_bf16 v[48:51], a[4:7], a[40:43], v[48:51]\n"
"s_waitcnt lgkmcnt(3)\n"
"s_add_u32 s88, s88, 458752\n"
"ds_read_b64_tr_b16 a[0:1], v35 offset:16384\n"
"v_mfma_f32_16x16x32_bf16 v[52:55], a[4:7], a[44:47], v[52:55]\n"
"s_add_u32 m0, s91, 86016\n"
"ds_read_b64_tr_b16 a[2:3], v35 offset:18432\n"
"v_mfma_f32_16x16x32_bf16 v[56:59], a[4:7], a[48:51], v[56:59]\n"
"buffer_load_dwordx4 v43, s[80:83], s89 offen lds\n"
"v_mfma_f32_16x16x32_bf16 v[60:63], a[4:7], a[52:55], v[60:63]\n"
"s_waitcnt lgkmcnt(2)\n"
"v_mfma_f32_16x16x32_bf16 v[64:67], a[4:7], a[56:59], v[64:67]\n"
"buffer_load_dwordx4 v44, s[80:83], s89 offen offset:1024 lds\n"
"v_mfma_f32_16x16x32_bf16 v[68:71], a[8:11], a[40:43], v[68:71]\n"
"ds_read_b64_tr_b16 a[4:5], v35 offset:16392\n"
"v_mfma_f32_16x16x32_bf16 v[72:75], a[8:11], a[44:47], v[72:75]\n"
"ds_read_b64_tr_b16 a[6:7], v35 offset:18440\n"
"v_mfma_f32_16x16x32_bf16 v[76:79], a[8:11], a[48:51], v[76:79]\n"
"v_mfma_f32_16x16x32_bf16 v[80:83], a[8:11], a[52:55], v[80:83]\n"
"buffer_load_dwordx4 v45, s[80:83], s89 offen offset:2048 lds\n"
"v_mfma_f32_16x16x32_bf16 v[84:87], a[8:11], a[56:59], v[84:87]\n"
"v_mfma_f32_16x16x32_bf16 v[88:91], a[12:15], a[40:43], v[88:91]\n"
"ds_read_b64_tr_b16 a[8:9], v36 offset:16384\n"
"v_mfma_f32_16x16x32_bf16 v[92:95], a[12:15], a[44:47], v[92:95]\n"
"ds_read_b64_tr_b16 a[10:11], v36 offset:18432\n"
"v_mfma_f32_16x16x32_bf16 v[96:99], a[12:15], a[48:51], v[96:99]\n"
"buffer_load_dwordx4 v46, s[80:83], s89 offen offset:3072 lds\n"
"v_mfma_f32_16x16x32_bf16 v[100:103], a[12:15], a[52:55], v[100:103]\n"
"s_add_u32 m0, s91, 90112\n"
"v_mfma_f32_16x16x32_bf16 v[104:107], a[12:15], a[56:59], v[104:107]\n"
"buffer_load_dwordx4 v47, s[80:83], s89 offen lds\n"
"v_mfma_f32_16x16x32_bf16 v[108:111], a[16:19], a[40:43], v[108:111]\n"
"s_add_u32 s89, s89, 128\n"
"ds_read_b64_tr_b16 a[12:13], v36 offset:16392\n"
"v_mfma_f32_16x16x32_bf16 v[112:115], a[16:19], a[44:47], v[112:115]\n"
"ds_read_b64_tr_b16 a[14:15], v36 offset:18440\n"
"v_mfma_f32_16x16x32_bf16 v[116:119], a[16:19], a[48:51], v[116:119]\n"
"v_mfma_f32_16x16x32_bf16 v[120:123], a[16:19], a[52:55], v[120:123]\n"
"v_mfma_f32_16x16x32_bf16 v[124:127], a[16:19], a[56:59], v[124:127]\n"
"s_waitcnt lgkmcnt(2)\n"
"v_mfma_f32_16x16x32_bf16 v[128:131], a[0:3], a[40:43], v[128:131]\n"
"v_mfma_f32_16x16x32_bf16 v[132:135], a[0:3], a[44:47], v[132:135]\n"
"v_mfma_f32_16x16x32_bf16 v[136:139], a[0:3], a[48:51], v[136:139]\n"
"s_waitcnt vmcnt(14) lgkmcnt(0)\n"
"s_barrier\n"
"v_mfma_f32_16x16x32_bf16 v[140:143], a[0:3], a[52:55], v[140:143]\n"
"s_add_u32 m0, s90, 106496\n"
"v_subrev_u32 v33, 106496, v33\n"
"v_subrev_u32 v34, 106496, v34\n"
"v_mfma_f32_16x16x32_bf16 v[144:147], a[0:3], a[56:59], v[144:147]\n"
"v_subrev_u32 v35, 106496, v35\n"
"v_subrev_u32 v36, 106496, v36\n"
"v_mfma_f32_16x16x32_bf16 v[148:151], a[4:7], a[40:43], v[148:151]\n"
"v_subrev_u32 v37, 106496, v37\n"
"v_subrev_u32 v38, 106496, v38\n"
"v_mfma_f32_16x16x32_bf16 v[152:155], a[4:7], a[44:47], v[152:155]\n"
"ds_read_b128 a[20:23], v37 offset:0\n"
"v_mfma_f32_16x16x32_bf16 v[156:159], a[4:7], a[48:51], v[156:159]\n"
"ds_read_b64_tr_b16 a[16:17], v33 offset:0\n"
"v_mfma_f32_16x16x32_bf16 v[160:163], a[4:7], a[52:55], v[160:163]\n"
"ds_read_b64_tr_b16 a[18:19], v33 offset:2048\n"
"v_mfma_f32_16x16x32_bf16 v[164:167], a[4:7], a[56:59], v[164:167]\n"
"ds_read_b128 a[24:27], v37 offset:2048\n"
"v_mfma_f32_16x16x32_bf16 v[168:171], a[8:11], a[40:43], v[168:171]\n"
"ds_read_b128 a[28:31], v37 offset:4096\n"
"v_mfma_f32_16x16x32_bf16 v[172:175], a[8:11], a[44:47], v[172:175]\n"
"ds_read_b128 a[32:35], v37 offset:6144\n"
"v_mfma_f32_16x16x32_bf16 v[176:179], a[8:11], a[48:51], v[176:179]\n"
"ds_read_b128 a[36:39], v37 offset:8192\n"
"v_mfma_f32_16x16x32_bf16 v[180:183], a[8:11], a[52:55], v[180:183]\n"
"ds_read_b64_tr_b16 a[0:1], v33 offset:8\n"
"v_mfma_f32_16x16x32_bf16 v[184:187], a[8:11], a[56:59], v[184:187]\n"
"ds_read_b64_tr_b16 a[2:3], v33 offset:2056\n"
"v_mfma_f32_16x16x32_bf16 v[188:191], a[12:15], a[40:43], v[188:191]\n"
"ds_read_b64_tr_b16 a[4:5], v34 offset:0\n"
"v_mfma_f32_16x16x32_bf16 v[192:195], a[12:15], a[44:47], v[192:195]\n"
"ds_read_b64_tr_b16 a[6:7], v34 offset:2048\n"
"v_mfma_f32_16x16x32_bf16 v[196:199], a[12:15], a[48:51], v[196:199]\n"
"ds_read_b64_tr_b16 a[8:9], v34 offset:8\n"
"v_mfma_f32_16x16x32_bf16 v[200:203], a[12:15], a[52:55], v[200:203]\n"
"ds_read_b64_tr_b16 a[10:11], v34 offset:2056\n"
"v_mfma_f32_16x16x32_bf16 v[204:207], a[12:15], a[56:59], v[204:207]\n"
"s_waitcnt lgkmcnt(6)\n"
"v_mfma_f32_16x16x32_bf16 v[48:51], a[16:19], a[20:23], v[48:51]\n"
"ds_read_b64_tr_b16 a[12:13], v35 offset:0\n"
"v_mfma_f32_16x16x32_bf16 v[52:55], a[16:19], a[24:27], v[52:55]\n"
"ds_read_b64_tr_b16 a[14:15], v35 offset:2048\n"
"v_mfma_f32_16x16x32_bf16 v[56:59], a[16:19], a[28:31], v[56:59]\n"
"buffer_load_dwordx4 v39, s[84:87], s88 offen lds\n"
"v_mfma_f32_16x16x32_bf16 v[60:63], a[16:19], a[32:35], v[60:63]\n"
"v_mfma_f32_16x16x32_bf16 v[64:67], a[16:19], a[36:39], v[64:67]\n"
"s_waitcnt lgkmcnt(4)\n"
"buffer_load_dwordx4 v40, s[84:87], s88 offen offset:1024 lds\n"
"v_mfma_f32_16x16x32_bf16 v[68:71], a[0:3], a[20:23], v[68:71]\n"
"ds_read_b64_tr_b16 a[16:17], v35 offset:8\n"
"v_mfma_f32_16x16x32_bf16 v[72:75], a[0:3], a[24:27], v[72:75]\n"
"ds_read_b64_tr_b16 a[18:19], v35 offset:2056\n"
"v_mfma_f32_16x16x32_bf16 v[76:79], a[0:3], a[28:31], v[76:79]\n"
"v_mfma_f32_16x16x32_bf16 v[80:83], a[0:3], a[32:35], v[80:83]\n"
"buffer_load_dwordx4 v41, s[84:87], s88 offen offset:2048 lds\n"
"v_mfma_f32_16x16x32_bf16 v[84:87], a[0:3], a[36:39], v[84:87]\n"
"v_mfma_f32_16x16x32_bf16 v[88:91], a[4:7], a[20:23], v[88:91]\n"
"ds_read_b64_tr_b16 a[0:1], v36 offset:0\n"
"v_mfma_f32_16x16x32_bf16 v[92:95], a[4:7], a[24:27], v[92:95]\n"
"ds_read_b64_tr_b16 a[2:3], v36 offset:2048\n"
"v_mfma_f32_16x16x32_bf16 v[96:99], a[4:7], a[28:31], v[96:99]\n"
"buffer_load_dwordx4 v42, s[84:87], s88 offen offset:3072 lds\n"
"v_mfma_f32_16x16x32_bf16 v[100:103], a[4:7], a[32:35], v[100:103]\n"
"s_add_u32 s88, s88, 65536\n"
"v_mfma_f32_16x16x32_bf16 v[104:107], a[4:7], a[36:39], v[104:107]\n"
"s_waitcnt lgkmcnt(2)\n"
"v_mfma_f32_16x16x32_bf16 v[108:111], a[8:11], a[20:23], v[108:111]\n"
"s_add_u32 m0, s90, 110592\n"
"ds_read_b64_tr_b16 a[4:5], v36 offset:8\n"
"v_mfma_f32_16x16x32_bf16 v[112:115], a[8:11], a[24:27], v[112:115]\n"
"ds_read_b64_tr_b16 a[6:7], v36 offset:2056\n"
"v_mfma_f32_16x16x32_bf16 v[116:119], a[8:11], a[28:31], v[116:119]\n"
"v_mfma_f32_16x16x32_bf16 v[120:123], a[8:11], a[32:35], v[120:123]\n"
"buffer_load_dwordx4 v39, s[84:87], s88 offen lds\n"
"v_mfma_f32_16x16x32_bf16 v[124:127], a[8:11], a[36:39], v[124:127]\n"
"v_mfma_f32_16x16x32_bf16 v[128:131], a[12:15], a[20:23], v[128:131]\n"
"ds_read_b64_tr_b16 a[8:9], v33 offset:16384\n"
"v_mfma_f32_16x16x32_bf16 v[132:135], a[12:15], a[24:27], v[132:135]\n"
"ds_read_b64_tr_b16 a[10:11], v33 offset:18432\n"
"v_mfma_f32_16x16x32_bf16 v[136:139], a[12:15], a[28:31], v[136:139]\n"
"ds_read_b128 a[40:43], v38 offset:0\n"
"v_mfma_f32_16x16x32_bf16 v[140:143], a[12:15], a[32:35], v[140:143]\n"
"ds_read_b128 a[44:47], v38 offset:2048\n"
"v_mfma_f32_16x16x32_bf16 v[144:147], a[12:15], a[36:39], v[144:147]\n"
"buffer_load_dwordx4 v40, s[84:87], s88 offen offset:1024 lds\n"
"v_mfma_f32_16x16x32_bf16 v[148:151], a[16:19], a[20:23], v[148:151]\n"
"ds_read_b64_tr_b16 a[12:13], v33 offset:16392\n"
"v_mfma_f32_16x16x32_bf16 v[152:155], a[16:19], a[24:27], v[152:155]\n"
"ds_read_b64_tr_b16 a[14:15], v33 offset:18440\n"
"v_mfma_f32_16x16x32_bf16 v[156:159], a[16:19], a[28:31], v[156:159]\n"
"ds_read_b128 a[48:51], v38 offset:4096\n"
"v_mfma_f32_16x16x32_bf16 v[160:163], a[16:19], a[32:35], v[160:163]\n"
"ds_read_b128 a[52:55], v38 offset:6144\n"
"v_mfma_f32_16x16x32_bf16 v[164:167], a[16:19], a[36:39], v[164:167]\n"
"s_waitcnt lgkmcnt(5)\n"
"v_mfma_f32_16x16x32_bf16 v[168:171], a[0:3], a[20:23], v[168:171]\n"
"ds_read_b64_tr_b16 a[16:17], v34 offset:16384\n"
"v_mfma_f32_16x16x32_bf16 v[172:175], a[0:3], a[24:27], v[172:175]\n"
"ds_read_b64_tr_b16 a[18:19], v34 offset:18432\n"
"v_mfma_f32_16x16x32_bf16 v[176:179], a[0:3], a[28:31], v[176:179]\n"
"ds_read_b128 a[56:59], v38 offset:8192\n"
"v_mfma_f32_16x16x32_bf16 v[180:183], a[0:3], a[32:35], v[180:183]\n"
"v_mfma_f32_16x16x32_bf16 v[184:187], a[0:3], a[36:39], v[184:187]\n"
"buffer_load_dwordx4 v41, s[84:87], s88 offen offset:2048 lds\n"
"v_mfma_f32_16x16x32_bf16 v[188:191], a[4:7], a[20:23], v[188:191]\n"
"ds_read_b64_tr_b16 a[0:1], v34 offset:16392\n"
"v_mfma_f32_16x16x32_bf16 v[192:195], a[4:7], a[24:27], v[192:195]\n"
"ds_read_b64_tr_b16 a[2:3], v34 offset:18440\n"
"v_mfma_f32_16x16x32_bf16 v[196:199], a[4:7], a[28:31], v[196:199]\n"
"v_mfma_f32_16x16x32_bf16 v[200:203], a[4:7], a[32:35], v[200:203]\n"
"buffer_load_dwordx4 v42, s[84:87], s88 offen offset:3072 lds\n"
"v_mfma_f32_16x16x32_bf16 v[204:207], a[4:7], a[36:39], v[204:207]\n"
"v_mfma_f32_16x16x32_bf16 v[48:51], a[8:11], a[40:43], v[48:51]\n"
"s_waitcnt lgkmcnt(3)\n"
"s_add_u32 s88, s88, 458752\n"
"ds_read_b64_tr_b16 a[4:5], v35 offset:16384\n"
"v_mfma_f32_16x16x32_bf16 v[52:55], a[8:11], a[44:47], v[52:55]\n"
"s_add_u32 m0, s91, 139264\n"
"ds_read_b64_tr_b16 a[6:7], v35 offset:18432\n"
"v_mfma_f32_16x16x32_bf16 v[56:59], a[8:11], a[48:51], v[56:59]\n"
"buffer_load_dwordx4 v43, s[80:83], s89 offen lds\n"
"v_mfma_f32_16x16x32_bf16 v[60:63], a[8:11], a[52:55], v[60:63]\n"
"s_waitcnt lgkmcnt(2)\n"
"v_mfma_f32_16x16x32_bf16 v[64:67], a[8:11], a[56:59], v[64:67]\n"
"buffer_load_dwordx4 v44, s[80:83], s89 offen offset:1024 lds\n"
"v_mfma_f32_16x16x32_bf16 v[68:71], a[12:15], a[40:43], v[68:71]\n"
"ds_read_b64_tr_b16 a[8:9], v35 offset:16392\n"
"v_mfma_f32_16x16x32_bf16 v[72:75], a[12:15], a[44:47], v[72:75]\n"
"ds_read_b64_tr_b16 a[10:11], v35 offset:18440\n"
"v_mfma_f32_16x16x32_bf16 v[76:79], a[12:15], a[48:51], v[76:79]\n"
"v_mfma_f32_16x16x32_bf16 v[80:83], a[12:15], a[52:55], v[80:83]\n"
"buffer_load_dwordx4 v45, s[80:83], s89 offen offset:2048 lds\n"
"v_mfma_f32_16x16x32_bf16 v[84:87], a[12:15], a[56:59], v[84:87]\n"
"v_mfma_f32_16x16x32_bf16 v[88:91], a[16:19], a[40:43], v[88:91]\n"
"ds_read_b64_tr_b16 a[12:13], v36 offset:16384\n"
"v_mfma_f32_16x16x32_bf16 v[92:95], a[16:19], a[44:47], v[92:95]\n"
"ds_read_b64_tr_b16 a[14:15], v36 offset:18432\n"
"v_mfma_f32_16x16x32_bf16 v[96:99], a[16:19], a[48:51], v[96:99]\n"
"buffer_load_dwordx4 v46, s[80:83], s89 offen offset:3072 lds\n"
"v_mfma_f32_16x16x32_bf16 v[100:103], a[16:19], a[52:55], v[100:103]\n"
"s_add_u32 m0, s91, 143360\n"
"v_mfma_f32_16x16x32_bf16 v[104:107], a[16:19], a[56:59], v[104:107]\n"
"buffer_load_dwordx4 v47, s[80:83], s89 offen lds\n"
"v_mfma_f32_16x16x32_bf16 v[108:111], a[0:3], a[40:43], v[108:111]\n"
"s_add_u32 s89, s89, 128\n"
"ds_read_b64_tr_b16 a[16:17], v36 offset:16392\n"
"v_mfma_f32_16x16x32_bf16 v[112:115], a[0:3], a[44:47], v[112:115]\n"
"ds_read_b64_tr_b16 a[18:19], v36 offset:18440\n"
"v_mfma_f32_16x16x32_bf16 v[116:119], a[0:3], a[48:51], v[116:119]\n"
"v_mfma_f32_16x16x32_bf16 v[120:123], a[0:3], a[52:55], v[120:123]\n"
"v_mfma_f32_16x16x32_bf16 v[124:127], a[0:3], a[56:59], v[124:127]\n"
"s_waitcnt lgkmcnt(2)\n"
"v_mfma_f32_16x16x32_bf16 v[128:131], a[4:7], a[40:43], v[128:131]\n"
"v_mfma_f32_16x16x32_bf16 v[132:135], a[4:7], a[44:47], v[132:135]\n"
"v_mfma_f32_16x16x32_bf16 v[136:139], a[4:7], a[48:51], v[136:139]\n"
"s_waitcnt vmcnt(13) lgkmcnt(0)\n"
"s_barrier\n"
"v_mfma_f32_16x16x32_bf16 v[140:143], a[4:7], a[52:55], v[140:143]\n"
"s_mov_b32 m0, s48\n"
"v_add_u32 v33, 53248, v33\n"
"v_add_u32 v34, 53248, v34\n"
"v_mfma_f32_16x16x32_bf16 v[144:147], a[4:7], a[56:59], v[144:147]\n"
"v_add_u32 v35, 53248, v35\n"
"v_add_u32 v36, 53248, v36\n"
"v_mfma_f32_16x16x32_bf16 v[148:151], a[8:11], a[40:43], v[148:151]\n"
"v_add_u32 v37, 53248, v37\n"
"v_add_u32 v38, 53248, v38\n"
"v_mfma_f32_16x16x32_bf16 v[152:155], a[8:11], a[44:47], v[152:155]\n"
"ds_read_b128 a[20:23], v37 offset:0\n"
"v_mfma_f32_16x16x32_bf16 v[156:159], a[8:11], a[48:51], v[156:159]\n"
"ds_read_b64_tr_b16 a[0:1], v33 offset:0\n"
"v_mfma_f32_16x16x32_bf16 v[160:163], a[8:11], a[52:55], v[160:163]\n"
"ds_read_b64_tr_b16 a[2:3], v33 offset:2048\n"
"v_mfma_f32_16x16x32_bf16 v[164:167], a[8:11], a[56:59], v[164:167]\n"
"ds_read_b128 a[24:27], v37 offset:2048\n"
"v_mfma_f32_16x16x32_bf16 v[168:171], a[12:15], a[40:43], v[168:171]\n"
"ds_read_b128 a[28:31], v37 offset:4096\n"
"v_mfma_f32_16x16x32_bf16 v[172:175], a[12:15], a[44:47], v[172:175]\n"
"ds_read_b128 a[32:35], v37 offset:6144\n"
"v_mfma_f32_16x16x32_bf16 v[176:179], a[12:15], a[48:51], v[176:179]\n"
"ds_read_b128 a[36:39], v37 offset:8192\n"
"v_mfma_f32_16x16x32_bf16 v[180:183], a[12:15], a[52:55], v[180:183]\n"
"ds_read_b64_tr_b16 a[4:5], v33 offset:8\n"
"v_mfma_f32_16x16x32_bf16 v[184:187], a[12:15], a[56:59], v[184:187]\n"
"ds_read_b64_tr_b16 a[6:7], v33 offset:2056\n"
"v_mfma_f32_16x16x32_bf16 v[188:191], a[16:19], a[40:43], v[188:191]\n"
"ds_read_b64_tr_b16 a[8:9], v34 offset:0\n"
"v_mfma_f32_16x16x32_bf16 v[192:195], a[16:19], a[44:47], v[192:195]\n"
"ds_read_b64_tr_b16 a[10:11], v34 offset:2048\n"
"v_mfma_f32_16x16x32_bf16 v[196:199], a[16:19], a[48:51], v[196:199]\n"
"ds_read_b64_tr_b16 a[12:13], v34 offset:8\n"
"v_mfma_f32_16x16x32_bf16 v[200:203], a[16:19], a[52:55], v[200:203]\n"
"ds_read_b64_tr_b16 a[14:15], v34 offset:2056\n"
"v_mfma_f32_16x16x32_bf16 v[204:207], a[16:19], a[56:59], v[204:207]\n"
"s_waitcnt lgkmcnt(6)\n"
"v_mfma_f32_16x16x32_bf16 v[48:51], a[0:3], a[20:23], v[48:51]\n"
"ds_read_b64_tr_b16 a[16:17], v35 offset:0\n"
"v_mfma_f32_16x16x32_bf16 v[52:55], a[0:3], a[24:27], v[52:55]\n"
"ds_read_b64_tr_b16 a[18:19], v35 offset:2048\n"
"v_mfma_f32_16x16x32_bf16 v[56:59], a[0:3], a[28:31], v[56:59]\n"
"buffer_load_dwordx4 v17, s[36:39], s44 offen nt lds\n"
"v_mfma_f32_16x16x32_bf16 v[60:63], a[0:3], a[32:35], v[60:63]\n"
"v_mfma_f32_16x16x32_bf16 v[64:67], a[0:3], a[36:39], v[64:67]\n"
"s_waitcnt lgkmcnt(4)\n"
"buffer_load_dwordx4 v18, s[36:39], s44 offen offset:1024 nt lds\n"
"v_mfma_f32_16x16x32_bf16 v[68:71], a[4:7], a[20:23], v[68:71]\n"
"ds_read_b64_tr_b16 a[0:1], v35 offset:8\n"
"v_mfma_f32_16x16x32_bf16 v[72:75], a[4:7], a[24:27], v[72:75]\n"
"ds_read_b64_tr_b16 a[2:3], v35 offset:2056\n"
"v_mfma_f32_16x16x32_bf16 v[76:79], a[4:7], a[28:31], v[76:79]\n"
"v_mfma_f32_16x16x32_bf16 v[80:83], a[4:7], a[32:35], v[80:83]\n"
"buffer_load_dwordx4 v19, s[36:39], s44 offen offset:2048 nt lds\n"
"v_mfma_f32_16x16x32_bf16 v[84:87], a[4:7], a[36:39], v[84:87]\n"
"v_mfma_f32_16x16x32_bf16 v[88:91], a[8:11], a[20:23], v[88:91]\n"
"ds_read_b64_tr_b16 a[4:5], v36 offset:0\n"
"v_mfma_f32_16x16x32_bf16 v[92:95], a[8:11], a[24:27], v[92:95]\n"
"ds_read_b64_tr_b16 a[6:7], v36 offset:2048\n"
"v_mfma_f32_16x16x32_bf16 v[96:99], a[8:11], a[28:31], v[96:99]\n"
"buffer_load_dwordx4 v20, s[36:39], s44 offen offset:3072 nt lds\n"
"v_mfma_f32_16x16x32_bf16 v[100:103], a[8:11], a[32:35], v[100:103]\n"
"s_add_u32 s44, s44, 32768\n"
"v_mfma_f32_16x16x32_bf16 v[104:107], a[8:11], a[36:39], v[104:107]\n"
"s_waitcnt lgkmcnt(2)\n"
"v_mfma_f32_16x16x32_bf16 v[108:111], a[12:15], a[20:23], v[108:111]\n"
"s_mov_b32 m0, s50\n"
"ds_read_b64_tr_b16 a[8:9], v36 offset:8\n"
"v_mfma_f32_16x16x32_bf16 v[112:115], a[12:15], a[24:27], v[112:115]\n"
"ds_read_b64_tr_b16 a[10:11], v36 offset:2056\n"
"v_mfma_f32_16x16x32_bf16 v[116:119], a[12:15], a[28:31], v[116:119]\n"
"v_mfma_f32_16x16x32_bf16 v[120:123], a[12:15], a[32:35], v[120:123]\n"
"buffer_load_dwordx4 v17, s[36:39], s45 offen nt lds\n"
"v_mfma_f32_16x16x32_bf16 v[124:127], a[12:15], a[36:39], v[124:127]\n"
"v_mfma_f32_16x16x32_bf16 v[128:131], a[16:19], a[20:23], v[128:131]\n"
"ds_read_b64_tr_b16 a[12:13], v33 offset:16384\n"
"v_mfma_f32_16x16x32_bf16 v[132:135], a[16:19], a[24:27], v[132:135]\n"
"ds_read_b64_tr_b16 a[14:15], v33 offset:18432\n"
"v_mfma_f32_16x16x32_bf16 v[136:139], a[16:19], a[28:31], v[136:139]\n"
"ds_read_b128 a[40:43], v38 offset:0\n"
"v_mfma_f32_16x16x32_bf16 v[140:143], a[16:19], a[32:35], v[140:143]\n"
"ds_read_b128 a[44:47], v38 offset:2048\n"
"v_mfma_f32_16x16x32_bf16 v[144:147], a[16:19], a[36:39], v[144:147]\n"
"buffer_load_dwordx4 v18, s[36:39], s45 offen offset:1024 nt lds\n"
"v_mfma_f32_16x16x32_bf16 v[148:151], a[0:3], a[20:23], v[148:151]\n"
"ds_read_b64_tr_b16 a[16:17], v33 offset:16392\n"
"v_mfma_f32_16x16x32_bf16 v[152:155], a[0:3], a[24:27], v[152:155]\n"
"ds_read_b64_tr_b16 a[18:19], v33 offset:18440\n"
"v_mfma_f32_16x16x32_bf16 v[156:159], a[0:3], a[28:31], v[156:159]\n"
"ds_read_b128 a[48:51], v38 offset:4096\n"
"v_mfma_f32_16x16x32_bf16 v[160:163], a[0:3], a[32:35], v[160:163]\n"
"ds_read_b128 a[52:55], v38 offset:6144\n"
"v_mfma_f32_16x16x32_bf16 v[164:167], a[0:3], a[36:39], v[164:167]\n"
"s_waitcnt lgkmcnt(5)\n"
"v_mfma_f32_16x16x32_bf16 v[168:171], a[4:7], a[20:23], v[168:171]\n"
"ds_read_b64_tr_b16 a[0:1], v34 offset:16384\n"
"v_mfma_f32_16x16x32_bf16 v[172:175], a[4:7], a[24:27], v[172:175]\n"
"ds_read_b64_tr_b16 a[2:3], v34 offset:18432\n"
"v_mfma_f32_16x16x32_bf16 v[176:179], a[4:7], a[28:31], v[176:179]\n"
"ds_read_b128 a[56:59], v38 offset:8192\n"
"v_mfma_f32_16x16x32_bf16 v[180:183], a[4:7], a[32:35], v[180:183]\n"
"v_mfma_f32_16x16x32_bf16 v[184:187], a[4:7], a[36:39], v[184:187]\n"
"buffer_load_dwordx4 v19, s[36:39], s45 offen offset:2048 nt lds\n"
"v_mfma_f32_16x16x32_bf16 v[188:191], a[8:11], a[20:23], v[188:191]\n"
"ds_read_b64_tr_b16 a[4:5], v34 offset:16392\n"
"v_mfma_f32_16x16x32_bf16 v[192:195], a[8:11], a[24:27], v[192:195]\n"
"ds_read_b64_tr_b16 a[6:7], v34 offset:18440\n"
"v_mfma_f32_16x16x32_bf16 v[196:199], a[8:11], a[28:31], v[196:199]\n"
"v_mfma_f32_16x16x32_bf16 v[200:203], a[8:11], a[32:35], v[200:203]\n"
"buffer_load_dwordx4 v20, s[36:39], s45 offen offset:3072 nt lds\n"
"v_mfma_f32_16x16x32_bf16 v[204:207], a[8:11], a[36:39], v[204:207]\n"
"v_mfma_f32_16x16x32_bf16 v[48:51], a[12:15], a[40:43], v[48:51]\n"
"s_waitcnt lgkmcnt(3)\n"
"s_add_u32 s45, s45, 32768\n"
"ds_read_b64_tr_b16 a[8:9], v35 offset:16384\n"
"v_mfma_f32_16x16x32_bf16 v[52:55], a[12:15], a[44:47], v[52:55]\n"
"s_mov_b32 m0, s49\n"
"ds_read_b64_tr_b16 a[10:11], v35 offset:18432\n"
"v_mfma_f32_16x16x32_bf16 v[56:59], a[12:15], a[48:51], v[56:59]\n"
"buffer_load_dwordx4 v17, s[36:39], s44 offen nt lds\n"
"v_mfma_f32_16x16x32_bf16 v[60:63], a[12:15], a[52:55], v[60:63]\n"
"s_waitcnt lgkmcnt(2)\n"
"v_mfma_f32_16x16x32_bf16 v[64:67], a[12:15], a[56:59], v[64:67]\n"
"buffer_load_dwordx4 v18, s[36:39], s44 offen offset:1024 nt lds\n"
"v_mfma_f32_16x16x32_bf16 v[68:71], a[16:19], a[40:43], v[68:71]\n"
"ds_read_b64_tr_b16 a[12:13], v35 offset:16392\n"
"v_mfma_f32_16x16x32_bf16 v[72:75], a[16:19], a[44:47], v[72:75]\n"
"ds_read_b64_tr_b16 a[14:15], v35 offset:18440\n"
"v_mfma_f32_16x16x32_bf16 v[76:79], a[16:19], a[48:51], v[76:79]\n"
"v_mfma_f32_16x16x32_bf16 v[80:83], a[16:19], a[52:55], v[80:83]\n"
"buffer_load_dwordx4 v19, s[36:39], s44 offen offset:2048 nt lds\n"
"v_mfma_f32_16x16x32_bf16 v[84:87], a[16:19], a[56:59], v[84:87]\n"
"v_mfma_f32_16x16x32_bf16 v[88:91], a[0:3], a[40:43], v[88:91]\n"
"ds_read_b64_tr_b16 a[16:17], v36 offset:16384\n"
"v_mfma_f32_16x16x32_bf16 v[92:95], a[0:3], a[44:47], v[92:95]\n"
"ds_read_b64_tr_b16 a[18:19], v36 offset:18432\n"
"v_mfma_f32_16x16x32_bf16 v[96:99], a[0:3], a[48:51], v[96:99]\n"
"buffer_load_dwordx4 v20, s[36:39], s44 offen offset:3072 nt lds\n"
"v_mfma_f32_16x16x32_bf16 v[100:103], a[0:3], a[52:55], v[100:103]\n"
"s_add_u32 s44, s44, 32768\n"
"v_mfma_f32_16x16x32_bf16 v[104:107], a[0:3], a[56:59], v[104:107]\n"
"v_mfma_f32_16x16x32_bf16 v[108:111], a[4:7], a[40:43], v[108:111]\n"
"ds_read_b64_tr_b16 a[0:1], v36 offset:16392\n"
"v_mfma_f32_16x16x32_bf16 v[112:115], a[4:7], a[44:47], v[112:115]\n"
"ds_read_b64_tr_b16 a[2:3], v36 offset:18440\n"
"v_mfma_f32_16x16x32_bf16 v[116:119], a[4:7], a[48:51], v[116:119]\n"
"v_mfma_f32_16x16x32_bf16 v[120:123], a[4:7], a[52:55], v[120:123]\n"
"v_mfma_f32_16x16x32_bf16 v[124:127], a[4:7], a[56:59], v[124:127]\n"
"s_waitcnt lgkmcnt(3)\n"
"v_mfma_f32_16x16x32_bf16 v[128:131], a[8:11], a[40:43], v[128:131]\n"
"v_mfma_f32_16x16x32_bf16 v[132:135], a[8:11], a[44:47], v[132:135]\n"
"v_mfma_f32_16x16x32_bf16 v[136:139], a[8:11], a[48:51], v[136:139]\n"
"s_waitcnt vmcnt(12) lgkmcnt(0)\n"
"s_barrier\n"
"v_mfma_f32_16x16x32_bf16 v[140:143], a[8:11], a[52:55], v[140:143]\n"
"s_mov_b32 m0, s52\n"
"v_add_u32 v33, 53248, v33\n"
"v_add_u32 v34, 53248, v34\n"
"v_mfma_f32_16x16x32_bf16 v[144:147], a[8:11], a[56:59], v[144:147]\n"
"v_add_u32 v35, 53248, v35\n"
"v_add_u32 v36, 53248, v36\n"
"v_mfma_f32_16x16x32_bf16 v[148:151], a[12:15], a[40:43], v[148:151]\n"
"v_add_u32 v37, 53248, v37\n"
"v_add_u32 v38, 53248, v38\n"
"v_mfma_f32_16x16x32_bf16 v[152:155], a[12:15], a[44:47], v[152:155]\n"
"ds_read_b128 a[20:23], v37 offset:0\n"
"v_mfma_f32_16x16x32_bf16 v[156:159], a[12:15], a[48:51], v[156:159]\n"
"ds_read_b64_tr_b16 a[4:5], v36 offset:0\n"
"v_mfma_f32_16x16x32_bf16 v[160:163], a[12:15], a[52:55], v[160:163]\n"
"ds_read_b64_tr_b16 a[6:7], v36 offset:2048\n"
"v_mfma_f32_16x16x32_bf16 v[164:167], a[12:15], a[56:59], v[164:167]\n"
"ds_read_b128 a[24:27], v37 offset:2048\n"
"v_mfma_f32_16x16x32_bf16 v[168:171], a[16:19], a[40:43], v[168:171]\n"
"ds_read_b128 a[28:31], v37 offset:4096\n"
"v_mfma_f32_16x16x32_bf16 v[172:175], a[16:19], a[44:47], v[172:175]\n"
"ds_read_b128 a[32:35], v37 offset:6144\n"
"v_mfma_f32_16x16x32_bf16 v[176:179], a[16:19], a[48:51], v[176:179]\n"
"ds_read_b128 a[36:39], v37 offset:8192\n"
"v_mfma_f32_16x16x32_bf16 v[180:183], a[16:19], a[52:55], v[180:183]\n"
"ds_read_b64_tr_b16 a[8:9], v36 offset:8\n"
"v_mfma_f32_16x16x32_bf16 v[184:187], a[16:19], a[56:59], v[184:187]\n"
"ds_read_b64_tr_b16 a[10:11], v36 offset:2056\n"
"v_mfma_f32_16x16x32_bf16 v[188:191], a[0:3], a[40:43], v[188:191]\n"
"ds_read_b64_tr_b16 a[12:13], v36 offset:16384\n"
"v_mfma_f32_16x16x32_bf16 v[192:195], a[0:3], a[44:47], v[192:195]\n"
"ds_read_b64_tr_b16 a[14:15], v36 offset:18432\n"
"v_mfma_f32_16x16x32_bf16 v[196:199], a[0:3], a[48:51], v[196:199]\n"
"ds_read_b64_tr_b16 a[16:17], v36 offset:16392\n"
"v_mfma_f32_16x16x32_bf16 v[200:203], a[0:3], a[52:55], v[200:203]\n"
"ds_read_b64_tr_b16 a[18:19], v36 offset:18440\n"
"v_mfma_f32_16x16x32_bf16 v[204:207], a[0:3], a[56:59], v[204:207]\n"
"s_waitcnt lgkmcnt(6)\n"
"v_mfma_f32_16x16x32_bf16 v[168:171], a[4:7], a[20:23], v[168:171]\n"
"ds_read_b64_tr_b16 a[0:1], v35 offset:0\n"
"v_mfma_f32_16x16x32_bf16 v[172:175], a[4:7], a[24:27], v[172:175]\n"
"ds_read_b64_tr_b16 a[2:3], v35 offset:2048\n"
"v_mfma_f32_16x16x32_bf16 v[176:179], a[4:7], a[28:31], v[176:179]\n"
"ds_read_b128 a[40:43], v38 offset:0\n"
"v_mfma_f32_16x16x32_bf16 v[180:183], a[4:7], a[32:35], v[180:183]\n"
"ds_read_b128 a[44:47], v38 offset:2048\n"
"v_mfma_f32_16x16x32_bf16 v[184:187], a[4:7], a[36:39], v[184:187]\n"
"s_waitcnt lgkmcnt(5)\n"
"ds_read_b128 a[48:51], v38 offset:4096\n"
"v_mfma_f32_16x16x32_bf16 v[188:191], a[8:11], a[20:23], v[188:191]\n"
"ds_read_b64_tr_b16 a[4:5], v35 offset:8\n"
"v_mfma_f32_16x16x32_bf16 v[192:195], a[8:11], a[24:27], v[192:195]\n"
"ds_read_b64_tr_b16 a[6:7], v35 offset:2056\n"
"v_mfma_f32_16x16x32_bf16 v[196:199], a[8:11], a[28:31], v[196:199]\n"
"ds_read_b128 a[52:55], v38 offset:6144\n"
"v_mfma_f32_16x16x32_bf16 v[200:203], a[8:11], a[32:35], v[200:203]\n"
"ds_read_b128 a[56:59], v38 offset:8192\n"
"v_mfma_f32_16x16x32_bf16 v[204:207], a[8:11], a[36:39], v[204:207]\n"
"s_waitcnt lgkmcnt(6)\n"
"v_mfma_f32_16x16x32_bf16 v[168:171], a[12:15], a[40:43], v[168:171]\n"
"s_waitcnt lgkmcnt(5)\n"
"ds_read_b64_tr_b16 a[8:9], v35 offset:16384\n"
"v_mfma_f32_16x16x32_bf16 v[172:175], a[12:15], a[44:47], v[172:175]\n"
"s_waitcnt lgkmcnt(5)\n"
"ds_read_b64_tr_b16 a[10:11], v35 offset:18432\n"
"v_mfma_f32_16x16x32_bf16 v[176:179], a[12:15], a[48:51], v[176:179]\n"
"s_waitcnt lgkmcnt(3)\n"
"buffer_load_dwordx4 v13, s[40:43], s46 offen nt lds\n"
"v_mfma_f32_16x16x32_bf16 v[180:183], a[12:15], a[52:55], v[180:183]\n"
"s_waitcnt lgkmcnt(2)\n"
"v_mfma_f32_16x16x32_bf16 v[184:187], a[12:15], a[56:59], v[184:187]\n"
"buffer_load_dwordx4 v14, s[40:43], s46 offen offset:1024 nt lds\n"
"v_mfma_f32_16x16x32_bf16 v[188:191], a[16:19], a[40:43], v[188:191]\n"
"ds_read_b64_tr_b16 a[12:13], v35 offset:16392\n"
"v_mfma_f32_16x16x32_bf16 v[192:195], a[16:19], a[44:47], v[192:195]\n"
"ds_read_b64_tr_b16 a[14:15], v35 offset:18440\n"
"v_mfma_f32_16x16x32_bf16 v[196:199], a[16:19], a[48:51], v[196:199]\n"
"v_mfma_f32_16x16x32_bf16 v[200:203], a[16:19], a[52:55], v[200:203]\n"
"buffer_load_dwordx4 v15, s[40:43], s46 offen offset:2048 nt lds\n"
"v_mfma_f32_16x16x32_bf16 v[204:207], a[16:19], a[56:59], v[204:207]\n"
"v_mfma_f32_16x16x32_bf16 v[128:131], a[0:3], a[20:23], v[128:131]\n"
"ds_read_b64_tr_b16 a[16:17], v34 offset:0\n"
"v_mfma_f32_16x16x32_bf16 v[132:135], a[0:3], a[24:27], v[132:135]\n"
"ds_read_b64_tr_b16 a[18:19], v34 offset:2048\n"
"v_mfma_f32_16x16x32_bf16 v[136:139], a[0:3], a[28:31], v[136:139]\n"
"buffer_load_dwordx4 v16, s[40:43], s46 offen offset:3072 nt lds\n"
"v_mfma_f32_16x16x32_bf16 v[140:143], a[0:3], a[32:35], v[140:143]\n"
"s_add_u32 s46, s46, 32768\n"
"v_mul_f32 v168, s72, v168\n"
"v_mul_f32 v169, s72, v169\n"
"v_mfma_f32_16x16x32_bf16 v[144:147], a[0:3], a[36:39], v[144:147]\n"
"v_mul_f32 v170, s72, v170\n"
"v_mul_f32 v171, s72, v171\n"
"v_mfma_f32_16x16x32_bf16 v[148:151], a[4:7], a[20:23], v[148:151]\n"
"s_mov_b32 m0, s51\n"
"ds_read_b64_tr_b16 a[0:1], v34 offset:8\n"
"v_mul_f32 v188, s72, v188\n"
"v_mfma_f32_16x16x32_bf16 v[152:155], a[4:7], a[24:27], v[152:155]\n"
"ds_read_b64_tr_b16 a[2:3], v34 offset:2056\n"
"v_mul_f32 v189, s72, v189\n"
"v_mfma_f32_16x16x32_bf16 v[156:159], a[4:7], a[28:31], v[156:159]\n"
"v_mul_f32 v190, s72, v190\n"
"v_mul_f32 v191, s72, v191\n"
"v_mfma_f32_16x16x32_bf16 v[160:163], a[4:7], a[32:35], v[160:163]\n"
"buffer_load_dwordx4 v17, s[36:39], s45 offen nt lds\n"
"v_cvt_pk_bf16_f32 v236, v168, v169\n"
"v_mfma_f32_16x16x32_bf16 v[164:167], a[4:7], a[36:39], v[164:167]\n"
"s_waitcnt lgkmcnt(2)\n"
"v_cvt_pk_bf16_f32 v237, v170, v171\n"
"v_cvt_pk_bf16_f32 v238, v188, v189\n"
"v_mfma_f32_16x16x32_bf16 v[128:131], a[8:11], a[40:43], v[128:131]\n"
"ds_read_b64_tr_b16 a[4:5], v34 offset:16384\n"
"v_cvt_pk_bf16_f32 v239, v190, v191\n"
"v_mfma_f32_16x16x32_bf16 v[132:135], a[8:11], a[44:47], v[132:135]\n"
"ds_read_b64_tr_b16 a[6:7], v34 offset:18432\n"
"v_mul_f32 v172, s72, v172\n"
"v_mfma_f32_16x16x32_bf16 v[136:139], a[8:11], a[48:51], v[136:139]\n"
"buffer_load_dwordx4 v18, s[36:39], s45 offen offset:1024 nt lds\n"
"v_mul_f32 v173, s72, v173\n"
"v_mfma_f32_16x16x32_bf16 v[140:143], a[8:11], a[52:55], v[140:143]\n"
"v_mul_f32 v174, s72, v174\n"
"v_mul_f32 v175, s72, v175\n"
"v_mfma_f32_16x16x32_bf16 v[144:147], a[8:11], a[56:59], v[144:147]\n"
"buffer_load_dwordx4 v19, s[36:39], s45 offen offset:2048 nt lds\n"
"v_mul_f32 v192, s72, v192\n"
"v_mfma_f32_16x16x32_bf16 v[148:151], a[12:15], a[40:43], v[148:151]\n"
"ds_read_b64_tr_b16 a[8:9], v34 offset:16392\n"
"v_mul_f32 v193, s72, v193\n"
"v_mfma_f32_16x16x32_bf16 v[152:155], a[12:15], a[44:47], v[152:155]\n"
"ds_read_b64_tr_b16 a[10:11], v34 offset:18440\n"
"v_mul_f32 v194, s72, v194\n"
"v_mfma_f32_16x16x32_bf16 v[156:159], a[12:15], a[48:51], v[156:159]\n"
"v_mul_f32 v195, s72, v195\n"
"v_cvt_pk_bf16_f32 v240, v172, v173\n"
"v_mfma_f32_16x16x32_bf16 v[160:163], a[12:15], a[52:55], v[160:163]\n"
"buffer_load_dwordx4 v20, s[36:39], s45 offen offset:3072 nt lds\n"
"v_cvt_pk_bf16_f32 v241, v174, v175\n"
"v_mfma_f32_16x16x32_bf16 v[164:167], a[12:15], a[56:59], v[164:167]\n"
"v_cvt_pk_bf16_f32 v242, v192, v193\n"
"v_cvt_pk_bf16_f32 v243, v194, v195\n"
"v_mfma_f32_16x16x32_bf16 v[88:91], a[16:19], a[20:23], v[88:91]\n"
"s_add_u32 s45, s45, 32768\n"
"ds_read_b64_tr_b16 a[12:13], v33 offset:0\n"
"v_mul_f32 v176, s72, v176\n"
"v_mfma_f32_16x16x32_bf16 v[92:95], a[16:19], a[24:27], v[92:95]\n"
"ds_read_b64_tr_b16 a[14:15], v33 offset:2048\n"
"v_mul_f32 v177, s72, v177\n"
"v_mfma_f32_16x16x32_bf16 v[96:99], a[16:19], a[28:31], v[96:99]\n"
"v_mul_f32 v178, s72, v178\n"
"v_mul_f32 v179, s72, v179\n"
"v_mfma_f32_16x16x32_bf16 v[100:103], a[16:19], a[32:35], v[100:103]\n"
"v_mul_f32 v196, s72, v196\n"
"v_mul_f32 v197, s72, v197\n"
"v_mfma_f32_16x16x32_bf16 v[104:107], a[16:19], a[36:39], v[104:107]\n"
"s_waitcnt lgkmcnt(2)\n"
"v_mul_f32 v198, s72, v198\n"
"v_mul_f32 v199, s72, v199\n"
"v_mfma_f32_16x16x32_bf16 v[108:111], a[0:3], a[20:23], v[108:111]\n"
"ds_read_b64_tr_b16 a[16:17], v33 offset:8\n"
"v_cvt_pk_bf16_f32 v244, v176, v177\n"
"v_mfma_f32_16x16x32_bf16 v[112:115], a[0:3], a[24:27], v[112:115]\n"
"ds_read_b64_tr_b16 a[18:19], v33 offset:2056\n"
"v_cvt_pk_bf16_f32 v245, v178, v179\n"
"v_mfma_f32_16x16x32_bf16 v[116:119], a[0:3], a[28:31], v[116:119]\n"
"v_cvt_pk_bf16_f32 v246, v196, v197\n"
"v_cvt_pk_bf16_f32 v247, v198, v199\n"
"v_mfma_f32_16x16x32_bf16 v[120:123], a[0:3], a[32:35], v[120:123]\n"
"v_mul_f32 v180, s72, v180\n"
"v_mul_f32 v181, s72, v181\n"
"v_mfma_f32_16x16x32_bf16 v[124:127], a[0:3], a[36:39], v[124:127]\n"
"v_mul_f32 v182, s72, v182\n"
"v_mul_f32 v183, s72, v183\n"
"v_mfma_f32_16x16x32_bf16 v[88:91], a[4:7], a[40:43], v[88:91]\n"
"ds_read_b64_tr_b16 a[0:1], v33 offset:16384\n"
"v_mul_f32 v200, s72, v200\n"
"v_mfma_f32_16x16x32_bf16 v[92:95], a[4:7], a[44:47], v[92:95]\n"
"ds_read_b64_tr_b16 a[2:3], v33 offset:18432\n"
"v_mul_f32 v201, s72, v201\n"
"v_mfma_f32_16x16x32_bf16 v[96:99], a[4:7], a[48:51], v[96:99]\n"
"v_mul_f32 v202, s72, v202\n"
"v_mul_f32 v203, s72, v203\n"
"v_mfma_f32_16x16x32_bf16 v[100:103], a[4:7], a[52:55], v[100:103]\n"
"v_cvt_pk_bf16_f32 v248, v180, v181\n"
"v_cvt_pk_bf16_f32 v249, v182, v183\n"
"v_mfma_f32_16x16x32_bf16 v[104:107], a[4:7], a[56:59], v[104:107]\n"
"v_cvt_pk_bf16_f32 v250, v200, v201\n"
"v_cvt_pk_bf16_f32 v251, v202, v203\n"
"v_mfma_f32_16x16x32_bf16 v[108:111], a[8:11], a[40:43], v[108:111]\n"
"ds_read_b64_tr_b16 a[4:5], v33 offset:16392\n"
"v_mul_f32 v184, s72, v184\n"
"v_mfma_f32_16x16x32_bf16 v[112:115], a[8:11], a[44:47], v[112:115]\n"
"ds_read_b64_tr_b16 a[6:7], v33 offset:18440\n"
"v_mul_f32 v185, s72, v185\n"
"v_mfma_f32_16x16x32_bf16 v[116:119], a[8:11], a[48:51], v[116:119]\n"
"v_mul_f32 v186, s72, v186\n"
"v_mul_f32 v187, s72, v187\n"
"v_mfma_f32_16x16x32_bf16 v[120:123], a[8:11], a[52:55], v[120:123]\n"
"v_mul_f32 v204, s72, v204\n"
"v_mul_f32 v205, s72, v205\n"
"v_mfma_f32_16x16x32_bf16 v[124:127], a[8:11], a[56:59], v[124:127]\n"
"s_waitcnt lgkmcnt(2)\n"
"v_mul_f32 v206, s72, v206\n"
"v_mul_f32 v207, s72, v207\n"
"v_mfma_f32_16x16x32_bf16 v[48:51], a[12:15], a[20:23], v[48:51]\n"
"v_cvt_pk_bf16_f32 v252, v184, v185\n"
"v_cvt_pk_bf16_f32 v253, v186, v187\n"
"v_mfma_f32_16x16x32_bf16 v[52:55], a[12:15], a[24:27], v[52:55]\n"
"v_cvt_pk_bf16_f32 v254, v204, v205\n"
"v_cvt_pk_bf16_f32 v255, v206, v207\n"
"v_mfma_f32_16x16x32_bf16 v[56:59], a[12:15], a[28:31], v[56:59]\n"
"s_waitcnt lgkmcnt(0)\n"
"s_barrier\n"
"v_mfma_f32_16x16x32_bf16 v[60:63], a[12:15], a[32:35], v[60:63]\n"
"ds_write_b128 v2, v[236:239] offset:15360\n"
"v_mfma_f32_16x16x32_bf16 v[64:67], a[12:15], a[36:39], v[64:67]\n"
"ds_write_b128 v2, v[240:243] offset:16384\n"
"v_mfma_f32_16x16x32_bf16 v[68:71], a[16:19], a[20:23], v[68:71]\n"
"ds_write_b128 v2, v[244:247] offset:17408\n"
"v_mfma_f32_16x16x32_bf16 v[72:75], a[16:19], a[24:27], v[72:75]\n"
"ds_write_b128 v2, v[248:251] offset:18432\n"
"v_mfma_f32_16x16x32_bf16 v[76:79], a[16:19], a[28:31], v[76:79]\n"
"ds_write_b128 v2, v[252:255] offset:19456\n"
"v_mfma_f32_16x16x32_bf16 v[80:83], a[16:19], a[32:35], v[80:83]\n"
"v_mul_f32 v128, s72, v128\n"
"v_mul_f32 v129, s72, v129\n"
"v_mfma_f32_16x16x32_bf16 v[84:87], a[16:19], a[36:39], v[84:87]\n"
"v_mul_f32 v130, s72, v130\n"
"v_mul_f32 v131, s72, v131\n"
"v_mfma_f32_16x16x32_bf16 v[48:51], a[0:3], a[40:43], v[48:51]\n"
"v_mul_f32 v148, s72, v148\n"
"v_mul_f32 v149, s72, v149\n"
"v_mfma_f32_16x16x32_bf16 v[52:55], a[0:3], a[44:47], v[52:55]\n"
"v_mul_f32 v150, s72, v150\n"
"v_mul_f32 v151, s72, v151\n"
"v_mfma_f32_16x16x32_bf16 v[56:59], a[0:3], a[48:51], v[56:59]\n"
"v_cvt_pk_bf16_f32 v216, v128, v129\n"
"v_cvt_pk_bf16_f32 v217, v130, v131\n"
"v_mfma_f32_16x16x32_bf16 v[60:63], a[0:3], a[52:55], v[60:63]\n"
"v_cvt_pk_bf16_f32 v218, v148, v149\n"
"v_cvt_pk_bf16_f32 v219, v150, v151\n"
"v_mfma_f32_16x16x32_bf16 v[64:67], a[0:3], a[56:59], v[64:67]\n"
"ds_write_b128 v2, v[216:219] offset:10240\n"
"v_mfma_f32_16x16x32_bf16 v[68:71], a[4:7], a[40:43], v[68:71]\n"
"v_mul_f32 v132, s72, v132\n"
"v_mul_f32 v133, s72, v133\n"
"v_mfma_f32_16x16x32_bf16 v[72:75], a[4:7], a[44:47], v[72:75]\n"
"v_mul_f32 v134, s72, v134\n"
"v_mul_f32 v135, s72, v135\n"
"v_mfma_f32_16x16x32_bf16 v[76:79], a[4:7], a[48:51], v[76:79]\n"
"v_mul_f32 v152, s72, v152\n"
"v_mul_f32 v153, s72, v153\n"
"v_mfma_f32_16x16x32_bf16 v[80:83], a[4:7], a[52:55], v[80:83]\n"
"v_mul_f32 v154, s72, v154\n"
"v_mul_f32 v155, s72, v155\n"
"v_mfma_f32_16x16x32_bf16 v[84:87], a[4:7], a[56:59], v[84:87]\n"
"v_cvt_pk_bf16_f32 v220, v132, v133\n"
"v_cvt_pk_bf16_f32 v221, v134, v135\n"
"s_nop 7\n"
"v_cvt_pk_bf16_f32 v222, v152, v153\n"
"v_cvt_pk_bf16_f32 v223, v154, v155\n"
"ds_write_b128 v2, v[220:223] offset:11264\n"
"v_mul_f32 v136, s72, v136\n"
"v_mul_f32 v137, s72, v137\n"
"v_mul_f32 v138, s72, v138\n"
"v_mul_f32 v139, s72, v139\n"
"v_mul_f32 v156, s72, v156\n"
"v_mul_f32 v157, s72, v157\n"
"v_mul_f32 v158, s72, v158\n"
"v_mul_f32 v159, s72, v159\n"
"v_cvt_pk_bf16_f32 v224, v136, v137\n"
"v_cvt_pk_bf16_f32 v225, v138, v139\n"
"v_cvt_pk_bf16_f32 v226, v156, v157\n"
"v_cvt_pk_bf16_f32 v227, v158, v159\n"
"ds_write_b128 v2, v[224:227] offset:12288\n"
"v_mul_f32 v140, s72, v140\n"
"v_mul_f32 v141, s72, v141\n"
"v_mul_f32 v142, s72, v142\n"
"v_mul_f32 v143, s72, v143\n"
"v_mul_f32 v160, s72, v160\n"
"v_mul_f32 v161, s72, v161\n"
"v_mul_f32 v162, s72, v162\n"
"v_mul_f32 v163, s72, v163\n"
"v_cvt_pk_bf16_f32 v228, v140, v141\n"
"v_cvt_pk_bf16_f32 v229, v142, v143\n"
"v_cvt_pk_bf16_f32 v230, v160, v161\n"
"v_cvt_pk_bf16_f32 v231, v162, v163\n"
"ds_write_b128 v2, v[228:231] offset:13312\n"
"v_mul_f32 v144, s72, v144\n"
"v_mul_f32 v145, s72, v145\n"
"v_mul_f32 v146, s72, v146\n"
"v_mul_f32 v147, s72, v147\n"
"v_mul_f32 v164, s72, v164\n"
"v_mul_f32 v165, s72, v165\n"
"v_mul_f32 v166, s72, v166\n"
"v_mul_f32 v167, s72, v167\n"
"v_cvt_pk_bf16_f32 v232, v144, v145\n"
"v_cvt_pk_bf16_f32 v233, v146, v147\n"
"v_cvt_pk_bf16_f32 v234, v164, v165\n"
"v_cvt_pk_bf16_f32 v235, v166, v167\n"
"ds_write_b128 v2, v[232:235] offset:14336\n"
"v_mul_f32 v88, s72, v88\n"
"v_mul_f32 v89, s72, v89\n"
"v_mul_f32 v90, s72, v90\n"
"v_mul_f32 v91, s72, v91\n"
"v_mul_f32 v108, s72, v108\n"
"v_mul_f32 v109, s72, v109\n"
"v_mul_f32 v110, s72, v110\n"
"v_mul_f32 v111, s72, v111\n"
"v_cvt_pk_bf16_f32 v196, v88, v89\n"
"v_cvt_pk_bf16_f32 v197, v90, v91\n"
"v_cvt_pk_bf16_f32 v198, v108, v109\n"
"v_cvt_pk_bf16_f32 v199, v110, v111\n"
"ds_write_b128 v2, v[196:199] offset:5120\n"
"v_mul_f32 v92, s72, v92\n"
"v_mul_f32 v93, s72, v93\n"
"v_mul_f32 v94, s72, v94\n"
"v_mul_f32 v95, s72, v95\n"
"v_mul_f32 v112, s72, v112\n"
"v_mul_f32 v113, s72, v113\n"
"v_mul_f32 v114, s72, v114\n"
"v_mul_f32 v115, s72, v115\n"
"v_cvt_pk_bf16_f32 v200, v92, v93\n"
"v_cvt_pk_bf16_f32 v201, v94, v95\n"
"v_cvt_pk_bf16_f32 v202, v112, v113\n"
"v_cvt_pk_bf16_f32 v203, v114, v115\n"
"ds_write_b128 v2, v[200:203] offset:6144\n"
"v_mul_f32 v96, s72, v96\n"
"v_mul_f32 v97, s72, v97\n"
"v_mul_f32 v98, s72, v98\n"
"v_mul_f32 v99, s72, v99\n"
"v_mul_f32 v116, s72, v116\n"
"v_mul_f32 v117, s72, v117\n"
"v_mul_f32 v118, s72, v118\n"
"v_mul_f32 v119, s72, v119\n"
"v_cvt_pk_bf16_f32 v204, v96, v97\n"
"v_cvt_pk_bf16_f32 v205, v98, v99\n"
"v_cvt_pk_bf16_f32 v206, v116, v117\n"
"v_cvt_pk_bf16_f32 v207, v118, v119\n"
"ds_write_b128 v2, v[204:207] offset:7168\n"
"v_mul_f32 v100, s72, v100\n"
"v_mul_f32 v101, s72, v101\n"
"v_mul_f32 v102, s72, v102\n"
"v_mul_f32 v103, s72, v103\n"
"v_mul_f32 v120, s72, v120\n"
"v_mul_f32 v121, s72, v121\n"
"v_mul_f32 v122, s72, v122\n"
"v_mul_f32 v123, s72, v123\n"
"v_cvt_pk_bf16_f32 v208, v100, v101\n"
"v_cvt_pk_bf16_f32 v209, v102, v103\n"
"v_cvt_pk_bf16_f32 v210, v120, v121\n"
"v_cvt_pk_bf16_f32 v211, v122, v123\n"
"ds_write_b128 v2, v[208:211] offset:8192\n"
"v_mul_f32 v104, s72, v104\n"
"v_mul_f32 v105, s72, v105\n"
"v_mul_f32 v106, s72, v106\n"
"v_mul_f32 v107, s72, v107\n"
"v_mul_f32 v124, s72, v124\n"
"v_mul_f32 v125, s72, v125\n"
"v_mul_f32 v126, s72, v126\n"
"v_mul_f32 v127, s72, v127\n"
"v_cvt_pk_bf16_f32 v212, v104, v105\n"
"v_cvt_pk_bf16_f32 v213, v106, v107\n"
"v_cvt_pk_bf16_f32 v214, v124, v125\n"
"v_cvt_pk_bf16_f32 v215, v126, v127\n"
"ds_write_b128 v2, v[212:215] offset:9216\n"
"v_pk_mul_f32 v[48:49], v[48:49], s[72:73]\n"
"v_pk_mul_f32 v[50:51], v[50:51], s[72:73]\n"
"v_pk_mul_f32 v[68:69], v[68:69], s[72:73]\n"
"v_pk_mul_f32 v[70:71], v[70:71], s[72:73]\n"
"v_cvt_pk_bf16_f32 v176, v48, v49\n"
"v_cvt_pk_bf16_f32 v177, v50, v51\n"
"v_cvt_pk_bf16_f32 v178, v68, v69\n"
"v_cvt_pk_bf16_f32 v179, v70, v71\n"
"ds_write_b128 v2, v[176:179] offset:0\n"
"v_pk_mul_f32 v[52:53], v[52:53], s[72:73]\n"
"v_pk_mul_f32 v[54:55], v[54:55], s[72:73]\n"
"v_pk_mul_f32 v[72:73], v[72:73], s[72:73]\n"
"v_pk_mul_f32 v[74:75], v[74:75], s[72:73]\n"
"v_cvt_pk_bf16_f32 v180, v52, v53\n"
"v_cvt_pk_bf16_f32 v181, v54, v55\n"
"v_cvt_pk_bf16_f32 v182, v72, v73\n"
"v_cvt_pk_bf16_f32 v183, v74, v75\n"
"ds_write_b128 v2, v[180:183] offset:1024\n"
"v_pk_mul_f32 v[56:57], v[56:57], s[72:73]\n"
"v_pk_mul_f32 v[58:59], v[58:59], s[72:73]\n"
"v_pk_mul_f32 v[76:77], v[76:77], s[72:73]\n"
"v_pk_mul_f32 v[78:79], v[78:79], s[72:73]\n"
"v_cvt_pk_bf16_f32 v184, v56, v57\n"
"v_cvt_pk_bf16_f32 v185, v58, v59\n"
"v_cvt_pk_bf16_f32 v186, v76, v77\n"
"v_cvt_pk_bf16_f32 v187, v78, v79\n"
"ds_write_b128 v2, v[184:187] offset:2048\n"
"v_pk_mul_f32 v[60:61], v[60:61], s[72:73]\n"
"v_pk_mul_f32 v[62:63], v[62:63], s[72:73]\n"
"v_pk_mul_f32 v[80:81], v[80:81], s[72:73]\n"
"v_pk_mul_f32 v[82:83], v[82:83], s[72:73]\n"
"v_cvt_pk_bf16_f32 v188, v60, v61\n"
"v_cvt_pk_bf16_f32 v189, v62, v63\n"
"v_cvt_pk_bf16_f32 v190, v80, v81\n"
"v_cvt_pk_bf16_f32 v191, v82, v83\n"
"ds_write_b128 v2, v[188:191] offset:3072\n"
"v_pk_mul_f32 v[64:65], v[64:65], s[72:73]\n"
"v_pk_mul_f32 v[66:67], v[66:67], s[72:73]\n"
"v_pk_mul_f32 v[84:85], v[84:85], s[72:73]\n"
"v_pk_mul_f32 v[86:87], v[86:87], s[72:73]\n"
"v_cvt_pk_bf16_f32 v192, v64, v65\n"
"v_cvt_pk_bf16_f32 v193, v66, v67\n"
"v_cvt_pk_bf16_f32 v194, v84, v85\n"
"v_cvt_pk_bf16_f32 v195, v86, v87\n"
"ds_write_b128 v2, v[192:195] offset:4096\n"
"s_waitcnt vmcnt(16) lgkmcnt(0)\n"
"s_barrier\n"
"ds_read_b128 a[240:243], v29 offset:0\n"
"ds_read_b128 a[244:247], v30 offset:0\n"
"ds_read_b128 a[248:251], v31 offset:0\n"
"ds_read_b128 a[252:255], v32 offset:0\n"
"s_waitcnt lgkmcnt(3)\n"
"v_mfma_f32_16x16x32_bf16 v[136:139], a[240:243], v[176:179], 0\n"
"ds_read_b128 a[160:163], v3 offset:0\n"
"v_mfma_f32_16x16x32_bf16 v[140:143], a[240:243], v[180:183], 0\n"
"ds_read_b128 a[164:167], v3 offset:1024\n"
"v_mfma_f32_16x16x32_bf16 v[144:147], a[240:243], v[184:187], 0\n"
"ds_read_b128 a[168:171], v3 offset:2048\n"
"v_mfma_f32_16x16x32_bf16 v[148:151], a[240:243], v[188:191], 0\n"
"ds_read_b128 a[172:175], v3 offset:3072\n"
"v_mfma_f32_16x16x32_bf16 v[152:155], a[240:243], v[192:195], 0\n"
"s_waitcnt lgkmcnt(6)\n"
"ds_read_b128 a[240:243], v29 offset:256\n"
"v_mfma_f32_16x16x32_bf16 v[136:139], a[244:247], v[196:199], v[136:139]\n"
"ds_read_b128 a[176:179], v3 offset:4096\n"
"v_mfma_f32_16x16x32_bf16 v[140:143], a[244:247], v[200:203], v[140:143]\n"
"ds_read_b128 a[180:183], v3 offset:5120\n"
"v_mfma_f32_16x16x32_bf16 v[144:147], a[244:247], v[204:207], v[144:147]\n"
"ds_read_b128 a[184:187], v3 offset:6144\n"
"v_mfma_f32_16x16x32_bf16 v[148:151], a[244:247], v[208:211], v[148:151]\n"
"ds_read_b128 a[188:191], v3 offset:7168\n"
"v_mfma_f32_16x16x32_bf16 v[152:155], a[244:247], v[212:215], v[152:155]\n"
"s_waitcnt lgkmcnt(6)\n"
"ds_read_b128 a[244:247], v30 offset:256\n"
"v_mfma_f32_16x16x32_bf16 v[136:139], a[248:251], v[216:219], v[136:139]\n"
"ds_read_b128 a[192:195], v3 offset:8192\n"
"v_mfma_f32_16x16x32_bf16 v[140:143], a[248:251], v[220:223], v[140:143]\n"
"ds_read_b128 a[196:199], v3 offset:9216\n"
"v_mfma_f32_16x16x32_bf16 v[144:147], a[248:251], v[224:227], v[144:147]\n"
"ds_read_b128 a[200:203], v3 offset:10240\n"
"v_mfma_f32_16x16x32_bf16 v[148:151], a[248:251], v[228:231], v[148:151]\n"
"ds_read_b128 a[204:207], v3 offset:11264\n"
"v_mfma_f32_16x16x32_bf16 v[152:155], a[248:251], v[232:235], v[152:155]\n"
"ds_read_b128 a[248:251], v31 offset:256\n"
"v_mfma_f32_16x16x32_bf16 v[136:139], a[252:255], v[236:239], v[136:139]\n"
"ds_read_b128 a[208:211], v3 offset:12288\n"
"v_mfma_f32_16x16x32_bf16 v[140:143], a[252:255], v[240:243], v[140:143]\n"
"ds_read_b128 a[212:215], v3 offset:13312\n"
"v_mfma_f32_16x16x32_bf16 v[144:147], a[252:255], v[244:247], v[144:147]\n"
"ds_read_b128 a[216:219], v3 offset:14336\n"
"v_mfma_f32_16x16x32_bf16 v[148:151], a[252:255], v[248:251], v[148:151]\n"
"ds_read_b128 a[220:223], v3 offset:15360\n"
"v_mfma_f32_16x16x32_bf16 v[152:155], a[252:255], v[252:255], v[152:155]\n"
"s_waitcnt lgkmcnt(6)\n"
"ds_read_b128 a[252:255], v32 offset:256\n"
"v_mfma_f32_16x16x32_bf16 v[136:139], a[240:243], a[160:163], v[136:139]\n"
"ds_read_b128 a[224:227], v3 offset:16384\n"
"v_mfma_f32_16x16x32_bf16 v[140:143], a[240:243], a[164:167], v[140:143]\n"
"ds_read_b128 a[228:231], v3 offset:17408\n"
"v_mfma_f32_16x16x32_bf16 v[144:147], a[240:243], a[168:171], v[144:147]\n"
"ds_read_b128 a[232:235], v3 offset:18432\n"
"v_mfma_f32_16x16x32_bf16 v[148:151], a[240:243], a[172:175], v[148:151]\n"
"ds_read_b128 a[236:239], v3 offset:19456\n"
"v_mfma_f32_16x16x32_bf16 v[152:155], a[240:243], a[176:179], v[152:155]\n"
"v_mfma_f32_16x16x32_bf16 v[136:139], a[244:247], a[180:183], v[136:139]\n"
"v_mfma_f32_16x16x32_bf16 v[140:143], a[244:247], a[184:187], v[140:143]\n"
"v_mfma_f32_16x16x32_bf16 v[144:147], a[244:247], a[188:191], v[144:147]\n"
"v_mfma_f32_16x16x32_bf16 v[148:151], a[244:247], a[192:195], v[148:151]\n"
"v_mfma_f32_16x16x32_bf16 v[152:155], a[244:247], a[196:199], v[152:155]\n"
"s_waitcnt vmcnt(12) lgkmcnt(0)\n"
"s_barrier\n"
"v_mfma_f32_16x16x32_bf16 v[136:139], a[248:251], a[200:203], v[136:139]\n"
"s_mov_b32 m0, s54\n"
"ds_read_b128 a[240:243], v29 offset:16384\n"
"v_mfma_f32_16x16x32_bf16 v[140:143], a[248:251], a[204:207], v[140:143]\n"
"ds_read_b128 a[244:247], v30 offset:16384\n"
"v_mfma_f32_16x16x32_bf16 v[144:147], a[248:251], a[208:211], v[144:147]\n"
"buffer_load_dwordx4 v13, s[40:43], s47 offen nt lds\n"
"v_mfma_f32_16x16x32_bf16 v[148:151], a[248:251], a[212:215], v[148:151]\n"
"v_mfma_f32_16x16x32_bf16 v[152:155], a[248:251], a[216:219], v[152:155]\n"
"buffer_load_dwordx4 v14, s[40:43], s47 offen offset:1024 nt lds\n"
"v_mfma_f32_16x16x32_bf16 v[136:139], a[252:255], a[220:223], v[136:139]\n"
"ds_read_b128 a[248:251], v31 offset:16384\n"
"v_mfma_f32_16x16x32_bf16 v[140:143], a[252:255], a[224:227], v[140:143]\n"
"buffer_load_dwordx4 v15, s[40:43], s47 offen offset:2048 nt lds\n"
"v_mfma_f32_16x16x32_bf16 v[144:147], a[252:255], a[228:231], v[144:147]\n"
"v_mfma_f32_16x16x32_bf16 v[148:151], a[252:255], a[232:235], v[148:151]\n"
"buffer_load_dwordx4 v16, s[40:43], s47 offen offset:3072 nt lds\n"
"v_mfma_f32_16x16x32_bf16 v[152:155], a[252:255], a[236:239], v[152:155]\n"
"s_waitcnt lgkmcnt(1)\n"
"s_add_u32 s47, s47, 32768\n"
"s_mov_b32 m0, s48\n"
"v_mfma_f32_16x16x32_bf16 v[156:159], a[240:243], v[176:179], 0\n"
"ds_read_b128 a[252:255], v32 offset:16384\n"
"v_mfma_f32_16x16x32_bf16 v[160:163], a[240:243], v[180:183], 0\n"
"v_mfma_f32_16x16x32_bf16 v[164:167], a[240:243], v[184:187], 0\n"
"buffer_load_dwordx4 v17, s[36:39], s44 offen nt lds\n"
"v_mfma_f32_16x16x32_bf16 v[168:171], a[240:243], v[188:191], 0\n"
"v_max3_f32 v43, v136, v137, v138\n"
"v_max_f32 v43, v43, v139\n"
"v_mov_b32 v2, v43\n"
"v_mfma_f32_16x16x32_bf16 v[172:175], a[240:243], v[192:195], 0\n"
"ds_read_b128 a[240:243], v29 offset:16640\n"
"v_mov_b32 v3, v43\n"
"s_nop 1\n"
"v_permlane16_swap_b32 v2, v3\n"
"v_mfma_f32_16x16x32_bf16 v[156:159], a[244:247], v[196:199], v[156:159]\n"
"v_max_f32 v43, v2, v3\n"
"v_mov_b32 v2, v43\n"
"v_mov_b32 v3, v43\n"
"v_mfma_f32_16x16x32_bf16 v[160:163], a[244:247], v[200:203], v[160:163]\n"
"buffer_load_dwordx4 v18, s[36:39], s44 offen offset:1024 nt lds\n"
"v_permlane32_swap_b32 v2, v3\n"
"v_max_f32 v43, v2, v3\n"
"v_mfma_f32_16x16x32_bf16 v[164:167], a[244:247], v[204:207], v[164:167]\n"
"ds_write_b32 v12, v43 offset:0\n"
"v_max3_f32 v44, v140, v141, v142\n"
"v_max_f32 v44, v44, v143\n"
"v_mfma_f32_16x16x32_bf16 v[168:171], a[244:247], v[208:211], v[168:171]\n"
"v_mov_b32 v2, v44\n"
"v_mov_b32 v3, v44\n"
"s_nop 1\n"
"v_permlane16_swap_b32 v2, v3\n"
"v_mfma_f32_16x16x32_bf16 v[172:175], a[244:247], v[212:215], v[172:175]\n"
"s_waitcnt lgkmcnt(1)\n"
"ds_read_b128 a[244:247], v30 offset:16640\n"
"v_max_f32 v44, v2, v3\n"
"v_mov_b32 v2, v44\n"
"v_mfma_f32_16x16x32_bf16 v[156:159], a[248:251], v[216:219], v[156:159]\n"
"v_mov_b32 v3, v44\n"
"s_nop 1\n"
"v_permlane32_swap_b32 v2, v3\n"
"v_max_f32 v44, v2, v3\n"
"v_mfma_f32_16x16x32_bf16 v[160:163], a[248:251], v[220:223], v[160:163]\n"
"buffer_load_dwordx4 v19, s[36:39], s44 offen offset:2048 nt lds\n"
"ds_write_b32 v12, v44 offset:1024\n"
"v_max3_f32 v45, v144, v145, v146\n"
"v_mfma_f32_16x16x32_bf16 v[164:167], a[248:251], v[224:227], v[164:167]\n"
"v_max_f32 v45, v45, v147\n"
"v_mov_b32 v2, v45\n"
"v_mov_b32 v3, v45\n"
"v_mfma_f32_16x16x32_bf16 v[168:171], a[248:251], v[228:231], v[168:171]\n"
"s_nop 0\n"
"v_permlane16_swap_b32 v2, v3\n"
"v_max_f32 v45, v2, v3\n"
"v_mov_b32 v2, v45\n"
"v_mfma_f32_16x16x32_bf16 v[172:175], a[248:251], v[232:235], v[172:175]\n"
"ds_read_b128 a[248:251], v31 offset:16640\n"
"v_mov_b32 v3, v45\n"
"s_nop 1\n"
"v_permlane32_swap_b32 v2, v3\n"
"v_mfma_f32_16x16x32_bf16 v[156:159], a[252:255], v[236:239], v[156:159]\n"
"v_max_f32 v45, v2, v3\n"
"ds_write_b32 v12, v45 offset:2048\n"
"v_max3_f32 v46, v148, v149, v150\n"
"v_mfma_f32_16x16x32_bf16 v[160:163], a[252:255], v[240:243], v[160:163]\n"
"buffer_load_dwordx4 v20, s[36:39], s44 offen offset:3072 nt lds\n"
"v_max_f32 v46, v46, v151\n"
"v_mov_b32 v2, v46\n"
"v_mfma_f32_16x16x32_bf16 v[164:167], a[252:255], v[244:247], v[164:167]\n"
"s_add_u32 s44, s44, 32768\n"
"s_mov_b32 m0, s53\n"
"v_mov_b32 v3, v46\n"
"s_nop 1\n"
"v_permlane16_swap_b32 v2, v3\n"
"v_max_f32 v46, v2, v3\n"
"v_mfma_f32_16x16x32_bf16 v[168:171], a[252:255], v[248:251], v[168:171]\n"
"v_mov_b32 v2, v46\n"
"v_mov_b32 v3, v46\n"
"s_nop 1\n"
"v_permlane32_swap_b32 v2, v3\n"
"v_mfma_f32_16x16x32_bf16 v[172:175], a[252:255], v[252:255], v[172:175]\n"
"ds_read_b128 a[252:255], v32 offset:16640\n"
"v_max_f32 v46, v2, v3\n"
"ds_write_b32 v12, v46 offset:3072\n"
"v_mfma_f32_16x16x32_bf16 v[156:159], a[240:243], a[160:163], v[156:159]\n"
"v_max3_f32 v47, v152, v153, v154\n"
"v_max_f32 v47, v47, v155\n"
"v_mov_b32 v2, v47\n"
"v_mfma_f32_16x16x32_bf16 v[160:163], a[240:243], a[164:167], v[160:163]\n"
"buffer_load_dwordx4 v13, s[40:43], s46 offen nt lds\n"
"v_mov_b32 v3, v47\n"
"s_nop 1\n"
"v_permlane16_swap_b32 v2, v3\n"
"v_mfma_f32_16x16x32_bf16 v[164:167], a[240:243], a[168:171], v[164:167]\n"
"v_max_f32 v47, v2, v3\n"
"v_mov_b32 v2, v47\n"
"v_mov_b32 v3, v47\n"
"v_mfma_f32_16x16x32_bf16 v[168:171], a[240:243], a[172:175], v[168:171]\n"
"s_nop 0\n"
"v_permlane32_swap_b32 v2, v3\n"
"v_max_f32 v47, v2, v3\n"
"ds_write_b32 v12, v47 offset:4096\n"
"v_mfma_f32_16x16x32_bf16 v[172:175], a[240:243], a[176:179], v[172:175]\n"
"s_waitcnt lgkmcnt(1)\n"
"v_mfma_f32_16x16x32_bf16 v[156:159], a[244:247], a[180:183], v[156:159]\n"
"buffer_load_dwordx4 v14, s[40:43], s46 offen offset:1024 nt lds\n"
"v_mfma_f32_16x16x32_bf16 v[160:163], a[244:247], a[184:187], v[160:163]\n"
"v_mfma_f32_16x16x32_bf16 v[164:167], a[244:247], a[188:191], v[164:167]\n"
"s_waitcnt lgkmcnt(0)\n"
"s_barrier\n"
"v_mfma_f32_16x16x32_bf16 v[168:171], a[244:247], a[192:195], v[168:171]\n"
"ds_read_b32 v48, v11 offset:0\n"
"ds_read_b32 v49, v11 offset:1024\n"
"ds_read_b32 v50, v11 offset:2048\n"
"v_mfma_f32_16x16x32_bf16 v[172:175], a[244:247], a[196:199], v[172:175]\n"
"ds_read_b32 v51, v11 offset:3072\n"
"ds_read_b32 v52, v11 offset:4096\n"
"buffer_load_dwordx4 v15, s[40:43], s46 offen offset:2048 nt lds\n"
"v_mfma_f32_16x16x32_bf16 v[156:159], a[248:251], a[200:203], v[156:159]\n"
"v_mov_b32 v33, 0\n"
"v_mov_b32 v34, 0\n"
"v_mov_b32 v35, 0\n"
"v_mfma_f32_16x16x32_bf16 v[160:163], a[248:251], a[204:207], v[160:163]\n"
"v_mov_b32 v36, 0\n"
"v_mov_b32 v37, 0\n"
"v_mov_b32 v1, 0\n"
"v_mfma_f32_16x16x32_bf16 v[164:167], a[248:251], a[208:211], v[164:167]\n"
"v_mfma_f32_16x16x32_bf16 v[168:171], a[248:251], a[212:215], v[168:171]\n"
"s_waitcnt lgkmcnt(2)\n"
"buffer_load_dwordx4 v16, s[40:43], s46 offen offset:3072 nt lds\n"
"v_max_f32 v43, v43, v48\n"
"v_max_f32 v44, v44, v49\n"
"v_mfma_f32_16x16x32_bf16 v[172:175], a[248:251], a[216:219], v[172:175]\n"
"s_waitcnt lgkmcnt(0)\n"
"s_add_u32 s46, s46, 32768\n"
"v_max_f32 v45, v45, v50\n"
"v_max_f32 v46, v46, v51\n"
"v_max_f32 v47, v47, v52\n"
"v_mfma_f32_16x16x32_bf16 v[156:159], a[252:255], a[220:223], v[156:159]\n"
"v_sub_f32 v136, v136, v43\n"
"v_sub_f32 v137, v137, v43\n"
"v_sub_f32 v138, v138, v43\n"
"v_mfma_f32_16x16x32_bf16 v[160:163], a[252:255], a[224:227], v[160:163]\n"
"v_sub_f32 v139, v139, v43\n"
"v_sub_f32 v140, v140, v44\n"
"v_sub_f32 v141, v141, v44\n"
"v_mfma_f32_16x16x32_bf16 v[164:167], a[252:255], a[228:231], v[164:167]\n"
"v_sub_f32 v142, v142, v44\n"
"v_sub_f32 v143, v143, v44\n"
"v_sub_f32 v144, v144, v45\n"
"v_mfma_f32_16x16x32_bf16 v[168:171], a[252:255], a[232:235], v[168:171]\n"
"v_sub_f32 v145, v145, v45\n"
"v_sub_f32 v146, v146, v45\n"
"v_sub_f32 v147, v147, v45\n"
"v_mfma_f32_16x16x32_bf16 v[172:175], a[252:255], a[236:239], v[172:175]\n"
"v_sub_f32 v148, v148, v46\n"
"v_sub_f32 v149, v149, v46\n"
"v_sub_f32 v150, v150, v46\n"
"v_sub_f32 v151, v151, v46\n"
"v_sub_f32 v152, v152, v47\n"
"v_sub_f32 v153, v153, v47\n"
"v_sub_f32 v154, v154, v47\n"
"v_sub_f32 v155, v155, v47\n"
"v_sub_f32 v76, 0, v43\n"
"v_sub_f32 v77, 0, v43\n"
"v_sub_f32 v78, 0, v43\n"
"v_sub_f32 v79, 0, v43\n"
"v_sub_f32 v80, 0, v44\n"
"v_sub_f32 v81, 0, v44\n"
"v_sub_f32 v82, 0, v44\n"
"v_sub_f32 v83, 0, v44\n"
"v_sub_f32 v84, 0, v45\n"
"v_sub_f32 v85, 0, v45\n"
"v_sub_f32 v86, 0, v45\n"
"v_sub_f32 v87, 0, v45\n"
"v_sub_f32 v88, 0, v46\n"
"v_sub_f32 v89, 0, v46\n"
"v_sub_f32 v90, 0, v46\n"
"v_sub_f32 v91, 0, v46\n"
"v_sub_f32 v92, 0, v47\n"
"v_sub_f32 v93, 0, v47\n"
"v_sub_f32 v94, 0, v47\n"
"v_sub_f32 v95, 0, v47\n"
"ds_write_b32 v8, v1 offset:1280\n"
"v_sub_f32 v156, v156, v43\n"
"v_sub_f32 v157, v157, v43\n"
"v_sub_f32 v158, v158, v43\n"
"v_sub_f32 v159, v159, v43\n"
"ds_write_b32 v8, v43 offset:0\n"
"v_sub_f32 v160, v160, v44\n"
"v_sub_f32 v161, v161, v44\n"
"v_sub_f32 v162, v162, v44\n"
"v_sub_f32 v163, v163, v44\n"
"ds_write_b32 v8, v44 offset:256\n"
"v_sub_f32 v164, v164, v45\n"
"v_sub_f32 v165, v165, v45\n"
"v_sub_f32 v166, v166, v45\n"
"v_sub_f32 v167, v167, v45\n"
"ds_write_b32 v8, v45 offset:512\n"
"v_sub_f32 v168, v168, v46\n"
"v_sub_f32 v169, v169, v46\n"
"v_sub_f32 v170, v170, v46\n"
"v_sub_f32 v171, v171, v46\n"
"ds_write_b32 v8, v46 offset:768\n"
"v_sub_f32 v172, v172, v47\n"
"v_sub_f32 v173, v173, v47\n"
"v_sub_f32 v174, v174, v47\n"
"v_sub_f32 v175, v175, v47\n"
"ds_write_b32 v8, v47 offset:1024\n"
"v_max3_f32 v10, v156, v157, v158\n"
"v_max3_f32 v10, v10, v159, v160\n"
"v_max3_f32 v10, v10, v161, v162\n"
"v_max3_f32 v10, v10, v163, v164\n"
"v_max3_f32 v10, v10, v165, v166\n"
"v_max3_f32 v10, v10, v167, v168\n"
"v_max3_f32 v10, v10, v169, v170\n"
"v_max3_f32 v10, v10, v171, v172\n"
"v_max3_f32 v10, v10, v173, v174\n"
"v_max_f32 v10, v10, v175\n"
"v_cmp_nge_f32 vcc, s62, v10\n"
"s_nop 0\n"
"s_cbranch_vccnz R1_0_%=\n"
"R1_0_r_%=:\n"
"s_waitcnt vmcnt(16) lgkmcnt(0)\n"
"s_barrier\n"
"ds_read_b128 a[240:243], v29 offset:32768\n"
"ds_read_b128 a[244:247], v30 offset:32768\n"
"ds_read_b128 a[248:251], v31 offset:32768\n"
"ds_read_b128 a[252:255], v32 offset:32768\n"
"ds_read_b128 v[68:71], v31 offset:33024\n"
"ds_read_b128 v[72:75], v32 offset:33024\n"
"ds_read_b128 v[60:63], v29 offset:33024\n"
"ds_read_b128 v[64:67], v30 offset:33024\n"
"v_exp_f32 v48, v136\n"
"v_exp_f32 v49, v137\n"
"v_exp_f32 v50, v138\n"
"v_exp_f32 v51, v139\n"
"v_cvt_pk_bf16_f32 v116, v48, v49\n"
"v_cvt_pk_bf16_f32 v117, v50, v51\n"
"v_add_f32 v33, v33, v48\n"
"v_add_f32 v33, v33, v49\n"
"v_add_f32 v33, v33, v50\n"
"v_add_f32 v33, v33, v51\n"
"v_exp_f32 v52, v140\n"
"v_exp_f32 v53, v141\n"
"v_exp_f32 v54, v142\n"
"v_exp_f32 v55, v143\n"
"v_cvt_pk_bf16_f32 v120, v52, v53\n"
"v_cvt_pk_bf16_f32 v121, v54, v55\n"
"v_add_f32 v34, v34, v52\n"
"v_add_f32 v34, v34, v53\n"
"v_add_f32 v34, v34, v54\n"
"v_add_f32 v34, v34, v55\n"
"v_exp_f32 v48, v144\n"
"v_exp_f32 v49, v145\n"
"v_exp_f32 v50, v146\n"
"v_exp_f32 v51, v147\n"
"v_cvt_pk_bf16_f32 v124, v48, v49\n"
"v_cvt_pk_bf16_f32 v125, v50, v51\n"
"v_add_f32 v35, v35, v48\n"
"v_add_f32 v35, v35, v49\n"
"v_add_f32 v35, v35, v50\n"
"v_add_f32 v35, v35, v51\n"
"v_exp_f32 v52, v148\n"
"v_exp_f32 v53, v149\n"
"v_exp_f32 v54, v150\n"
"v_exp_f32 v55, v151\n"
"v_cvt_pk_bf16_f32 v128, v52, v53\n"
"v_cvt_pk_bf16_f32 v129, v54, v55\n"
"v_add_f32 v36, v36, v52\n"
"v_add_f32 v36, v36, v53\n"
"v_add_f32 v36, v36, v54\n"
"v_add_f32 v36, v36, v55\n"
"v_exp_f32 v48, v152\n"
"v_exp_f32 v49, v153\n"
"v_exp_f32 v50, v154\n"
"v_exp_f32 v51, v155\n"
"v_cvt_pk_bf16_f32 v132, v48, v49\n"
"v_cvt_pk_bf16_f32 v133, v50, v51\n"
"v_add_f32 v37, v37, v48\n"
"v_add_f32 v37, v37, v49\n"
"v_add_f32 v37, v37, v50\n"
"v_add_f32 v37, v37, v51\n"
"v_exp_f32 v52, v156\n"
"v_exp_f32 v53, v157\n"
"v_exp_f32 v54, v158\n"
"v_exp_f32 v55, v159\n"
"v_cvt_pk_bf16_f32 v118, v52, v53\n"
"v_cvt_pk_bf16_f32 v119, v54, v55\n"
"v_add_f32 v33, v33, v52\n"
"v_add_f32 v33, v33, v53\n"
"v_add_f32 v33, v33, v54\n"
"v_add_f32 v33, v33, v55\n"
"v_exp_f32 v48, v160\n"
"v_exp_f32 v49, v161\n"
"v_exp_f32 v50, v162\n"
"v_exp_f32 v51, v163\n"
"v_cvt_pk_bf16_f32 v122, v48, v49\n"
"v_cvt_pk_bf16_f32 v123, v50, v51\n"
"v_add_f32 v34, v34, v48\n"
"v_add_f32 v34, v34, v49\n"
"v_add_f32 v34, v34, v50\n"
"v_add_f32 v34, v34, v51\n"
"s_cmp_lg_u32 s56, 0\n"
"s_cbranch_scc1 S1_0_%=\n"
"S1_0_r_%=:\n"
"s_waitcnt lgkmcnt(0)\n"
"v_mfma_f32_16x16x32_bf16 v[136:139], a[240:243], v[176:179], v[76:79]\n"
"s_mov_b32 m0, s50\n"
"v_exp_f32 v52, v164\n"
"v_mfma_f32_16x16x32_bf16 v[136:139], a[244:247], v[196:199], v[136:139]\n"
"buffer_load_dwordx4 v17, s[36:39], s45 offen nt lds\n"
"v_add_f32 v35, v35, v52\n"
"v_mfma_f32_16x16x32_bf16 v[136:139], a[248:251], v[216:219], v[136:139]\n"
"v_exp_f32 v53, v165\n"
"v_mfma_f32_16x16x32_bf16 v[136:139], a[252:255], v[236:239], v[136:139]\n"
"v_exp_f32 v54, v166\n"
"v_mfma_f32_16x16x32_bf16 v[136:139], v[68:71], a[200:203], v[136:139]\n"
"ds_read_b64_tr_b16 v[56:57], v21 offset:0\n"
"v_cvt_pk_bf16_f32 v126, v52, v53\n"
"v_mfma_f32_16x16x32_bf16 v[136:139], v[72:75], a[220:223], v[136:139]\n"
"ds_read_b64_tr_b16 v[58:59], v21 offset:4096\n"
"v_add_f32 v35, v35, v53\n"
"v_mfma_f32_16x16x32_bf16 v[136:139], v[60:63], a[160:163], v[136:139]\n"
"buffer_load_dwordx4 v18, s[36:39], s45 offen offset:1024 nt lds\n"
"v_add_f32 v35, v35, v54\n"
"v_mfma_f32_16x16x32_bf16 v[136:139], v[64:67], a[180:183], v[136:139]\n"
"v_exp_f32 v55, v167\n"
"v_mfma_f32_16x16x32_bf16 v[140:143], a[240:243], v[180:183], v[80:83]\n"
"v_exp_f32 v48, v168\n"
"v_mfma_f32_16x16x32_bf16 v[140:143], a[244:247], v[200:203], v[140:143]\n"
"v_exp_f32 v49, v169\n"
"v_mfma_f32_16x16x32_bf16 v[140:143], a[248:251], v[220:223], v[140:143]\n"
"v_exp_f32 v50, v170\n"
"v_mfma_f32_16x16x32_bf16 v[140:143], a[252:255], v[240:243], v[140:143]\n"
"buffer_load_dwordx4 v19, s[36:39], s45 offen offset:2048 nt lds\n"
"v_cvt_pk_bf16_f32 v127, v54, v55\n"
"v_mfma_f32_16x16x32_bf16 v[140:143], v[60:63], a[164:167], v[140:143]\n"
"v_exp_f32 v51, v171\n"
"v_mfma_f32_16x16x32_bf16 v[140:143], v[64:67], a[184:187], v[140:143]\n"
"v_exp_f32 v52, v172\n"
"v_mfma_f32_16x16x32_bf16 v[140:143], v[68:71], a[204:207], v[140:143]\n"
"v_exp_f32 v53, v173\n"
"v_mfma_f32_16x16x32_bf16 v[140:143], v[72:75], a[224:227], v[140:143]\n"
"v_exp_f32 v54, v174\n"
"v_mfma_f32_16x16x32_bf16 v[144:147], a[240:243], v[184:187], v[84:87]\n"
"buffer_load_dwordx4 v20, s[36:39], s45 offen offset:3072 nt lds\n"
"v_cvt_pk_bf16_f32 v130, v48, v49\n"
"v_mfma_f32_16x16x32_bf16 v[144:147], a[244:247], v[204:207], v[144:147]\n"
"s_add_u32 s45, s45, 32768\n"
"v_cvt_pk_bf16_f32 v131, v50, v51\n"
"v_cvt_pk_bf16_f32 v134, v52, v53\n"
"v_mfma_f32_16x16x32_bf16 v[144:147], a[248:251], v[224:227], v[144:147]\n"
"v_add_f32 v35, v35, v55\n"
"v_add_f32 v36, v36, v48\n"
"v_mfma_f32_16x16x32_bf16 v[144:147], a[252:255], v[244:247], v[144:147]\n"
"v_exp_f32 v55, v175\n"
"v_mfma_f32_16x16x32_bf16 v[144:147], v[60:63], a[168:171], v[144:147]\n"
"ds_write_b128 v12, v[116:119] offset:0\n"
"v_mfma_f32_16x16x32_bf16 v[144:147], v[64:67], a[188:191], v[144:147]\n"
"v_cvt_pk_bf16_f32 v135, v54, v55\n"
"v_add_f32 v36, v36, v49\n"
"v_mfma_f32_16x16x32_bf16 v[144:147], v[68:71], a[208:211], v[144:147]\n"
"ds_write_b128 v12, v[120:123] offset:1024\n"
"v_mfma_f32_16x16x32_bf16 v[144:147], v[72:75], a[228:231], v[144:147]\n"
"v_add_f32 v36, v36, v50\n"
"v_add_f32 v36, v36, v51\n"
"v_mfma_f32_16x16x32_bf16 v[148:151], a[240:243], v[188:191], v[88:91]\n"
"ds_write_b128 v12, v[124:127] offset:2048\n"
"v_mfma_f32_16x16x32_bf16 v[148:151], a[244:247], v[208:211], v[148:151]\n"
"v_add_f32 v37, v37, v52\n"
"v_add_f32 v37, v37, v53\n"
"v_mfma_f32_16x16x32_bf16 v[148:151], a[248:251], v[228:231], v[148:151]\n"
"v_add_f32 v37, v37, v54\n"
"v_add_f32 v37, v37, v55\n"
"v_mfma_f32_16x16x32_bf16 v[148:151], a[252:255], v[248:251], v[148:151]\n"
"ds_write_b128 v12, v[128:131] offset:3072\n"
"v_mfma_f32_16x16x32_bf16 v[148:151], v[60:63], a[172:175], v[148:151]\n"
"v_mfma_f32_16x16x32_bf16 v[148:151], v[64:67], a[192:195], v[148:151]\n"
"ds_write_b128 v12, v[132:135] offset:4096\n"
"v_mfma_f32_16x16x32_bf16 v[148:151], v[68:71], a[212:215], v[148:151]\n"
"v_mfma_f32_16x16x32_bf16 v[148:151], v[72:75], a[232:235], v[148:151]\n"
"v_mfma_f32_16x16x32_bf16 v[152:155], v[60:63], a[176:179], v[92:95]\n"
"ds_read_b64_tr_b16 v[60:61], v22 offset:0\n"
"v_mfma_f32_16x16x32_bf16 v[152:155], v[64:67], a[196:199], v[152:155]\n"
"ds_read_b64_tr_b16 v[62:63], v22 offset:4096\n"
"v_mfma_f32_16x16x32_bf16 v[152:155], v[68:71], a[216:219], v[152:155]\n"
"ds_read_b64_tr_b16 v[64:65], v23 offset:0\n"
"v_mfma_f32_16x16x32_bf16 v[152:155], v[72:75], a[236:239], v[152:155]\n"
"ds_read_b64_tr_b16 v[66:67], v23 offset:4096\n"
"v_mfma_f32_16x16x32_bf16 v[152:155], a[240:243], v[192:195], v[152:155]\n"
"ds_read_b64_tr_b16 v[68:69], v24 offset:0\n"
"v_mfma_f32_16x16x32_bf16 v[152:155], a[244:247], v[212:215], v[152:155]\n"
"ds_read_b64_tr_b16 v[70:71], v24 offset:4096\n"
"v_mfma_f32_16x16x32_bf16 v[152:155], a[248:251], v[232:235], v[152:155]\n"
"s_cmp_lg_u32 s56, 0\n"
"ds_read_b64_tr_b16 v[72:73], v25 offset:0\n"
"v_mfma_f32_16x16x32_bf16 v[152:155], a[252:255], v[252:255], v[152:155]\n"
"s_waitcnt lgkmcnt(6)\n"
"s_cbranch_scc1 S2_1_%=\n"
"S2_1_r_%=:\n"
"ds_read_b64_tr_b16 v[74:75], v25 offset:4096\n"
"v_mfma_f32_16x16x32_bf16 a[0:3], v[56:59], v[116:119], 0\n"
"s_mov_b32 m0, s55\n"
"v_max3_f32 v10, v136, v137, v138\n"
"v_max3_f32 v10, v10, v139, v140\n"
"v_mfma_f32_16x16x32_bf16 a[4:7], v[56:59], v[120:123], 0\n"
"buffer_load_dwordx4 v13, s[40:43], s47 offen nt lds\n"
"v_max3_f32 v10, v10, v141, v142\n"
"v_mfma_f32_16x16x32_bf16 a[8:11], v[56:59], v[124:127], 0\n"
"v_max3_f32 v10, v10, v143, v144\n"
"v_max3_f32 v10, v10, v145, v146\n"
"v_mfma_f32_16x16x32_bf16 a[12:15], v[56:59], v[128:131], 0\n"
"v_max3_f32 v10, v10, v147, v148\n"
"v_max3_f32 v10, v10, v149, v150\n"
"v_mfma_f32_16x16x32_bf16 a[16:19], v[56:59], v[132:135], 0\n"
"s_waitcnt lgkmcnt(0)\n"
"ds_read_b64_tr_b16 v[56:57], v26 offset:0\n"
"v_max3_f32 v10, v10, v151, v152\n"
"v_mfma_f32_16x16x32_bf16 a[20:23], v[60:63], v[116:119], 0\n"
"ds_read_b64_tr_b16 v[58:59], v26 offset:4096\n"
"v_max3_f32 v10, v10, v153, v154\n"
"v_mfma_f32_16x16x32_bf16 a[24:27], v[60:63], v[120:123], 0\n"
"buffer_load_dwordx4 v14, s[40:43], s47 offen offset:1024 nt lds\n"
"v_max_f32 v10, v10, v155\n"
"v_mfma_f32_16x16x32_bf16 a[28:31], v[60:63], v[124:127], 0\n"
"v_cmp_nge_f32 vcc, s62, v10\n"
"v_mfma_f32_16x16x32_bf16 a[32:35], v[60:63], v[128:131], 0\n"
"v_mfma_f32_16x16x32_bf16 a[36:39], v[60:63], v[132:135], 0\n"
"ds_read_b64_tr_b16 v[60:61], v27 offset:0\n"
"v_mfma_f32_16x16x32_bf16 a[40:43], v[64:67], v[116:119], 0\n"
"ds_read_b64_tr_b16 v[62:63], v27 offset:4096\n"
"v_mfma_f32_16x16x32_bf16 a[44:47], v[64:67], v[120:123], 0\n"
"buffer_load_dwordx4 v15, s[40:43], s47 offen offset:2048 nt lds\n"
"v_mfma_f32_16x16x32_bf16 a[48:51], v[64:67], v[124:127], 0\n"
"v_mfma_f32_16x16x32_bf16 a[52:55], v[64:67], v[128:131], 0\n"
"v_mfma_f32_16x16x32_bf16 a[56:59], v[64:67], v[132:135], 0\n"
"s_cbranch_vccnz R0_1_%=\n"
"R0_1_r_%=:\n"
"ds_read_b64_tr_b16 v[64:65], v28 offset:0\n"
"v_mfma_f32_16x16x32_bf16 a[60:63], v[68:71], v[116:119], 0\n"
"ds_read_b64_tr_b16 v[66:67], v28 offset:4096\n"
"v_mfma_f32_16x16x32_bf16 a[64:67], v[68:71], v[120:123], 0\n"
"buffer_load_dwordx4 v16, s[40:43], s47 offen offset:3072 nt lds\n"
"v_mfma_f32_16x16x32_bf16 a[68:71], v[68:71], v[124:127], 0\n"
"s_add_u32 s47, s47, 32768\n"
"v_exp_f32 v48, v136\n"
"v_mfma_f32_16x16x32_bf16 a[72:75], v[68:71], v[128:131], 0\n"
"v_exp_f32 v49, v137\n"
"v_mfma_f32_16x16x32_bf16 a[76:79], v[68:71], v[132:135], 0\n"
"v_exp_f32 v50, v138\n"
"v_mfma_f32_16x16x32_bf16 a[80:83], v[72:75], v[116:119], 0\n"
"v_exp_f32 v51, v139\n"
"v_mfma_f32_16x16x32_bf16 a[84:87], v[72:75], v[120:123], 0\n"
"v_exp_f32 v52, v140\n"
"v_mfma_f32_16x16x32_bf16 a[88:91], v[72:75], v[124:127], 0\n"
"v_exp_f32 v53, v141\n"
"v_mfma_f32_16x16x32_bf16 a[92:95], v[72:75], v[128:131], 0\n"
"v_exp_f32 v54, v142\n"
"v_mfma_f32_16x16x32_bf16 a[96:99], v[72:75], v[132:135], 0\n"
"s_waitcnt vmcnt(16) lgkmcnt(0)\n"
"s_barrier\n"
"v_mfma_f32_16x16x32_bf16 a[100:103], v[56:59], v[116:119], 0\n"
"ds_read_b128 a[240:243], v29 offset:49152\n"
"v_add_f32 v33, v33, v48\n"
"v_mfma_f32_16x16x32_bf16 a[104:107], v[56:59], v[120:123], 0\n"
"s_mov_b32 m0, s49\n"
"ds_read_b128 a[244:247], v30 offset:49152\n"
"v_add_f32 v33, v33, v49\n"
"v_mfma_f32_16x16x32_bf16 a[108:111], v[56:59], v[124:127], 0\n"
"buffer_load_dwordx4 v17, s[36:39], s44 offen nt lds\n"
"v_add_f32 v33, v33, v50\n"
"v_mfma_f32_16x16x32_bf16 a[112:115], v[56:59], v[128:131], 0\n"
"ds_read_b128 a[248:251], v31 offset:49152\n"
"v_add_f32 v33, v33, v51\n"
"v_mfma_f32_16x16x32_bf16 a[116:119], v[56:59], v[132:135], 0\n"
"ds_read_b128 a[252:255], v32 offset:49152\n"
"v_add_f32 v34, v34, v52\n"
"v_mfma_f32_16x16x32_bf16 a[120:123], v[60:63], v[116:119], 0\n"
"ds_read_b128 v[68:71], v31 offset:49408\n"
"v_add_f32 v34, v34, v53\n"
"v_mfma_f32_16x16x32_bf16 a[124:127], v[60:63], v[120:123], 0\n"
"ds_read_b128 v[72:75], v32 offset:49408\n"
"v_add_f32 v34, v34, v54\n"
"v_mfma_f32_16x16x32_bf16 a[128:131], v[60:63], v[124:127], 0\n"
"buffer_load_dwordx4 v18, s[36:39], s44 offen offset:1024 nt lds\n"
"v_mfma_f32_16x16x32_bf16 a[132:135], v[60:63], v[128:131], 0\n"
"v_exp_f32 v55, v143\n"
"v_mfma_f32_16x16x32_bf16 a[136:139], v[60:63], v[132:135], 0\n"
"v_add_f32 v34, v34, v55\n"
"v_mfma_f32_16x16x32_bf16 a[140:143], v[64:67], v[116:119], 0\n"
"ds_read_b128 v[60:63], v29 offset:49408\n"
"v_cvt_pk_bf16_f32 v116, v48, v49\n"
"v_mfma_f32_16x16x32_bf16 a[144:147], v[64:67], v[120:123], 0\n"
"v_exp_f32 v48, v144\n"
"v_mfma_f32_16x16x32_bf16 a[148:151], v[64:67], v[124:127], 0\n"
"buffer_load_dwordx4 v19, s[36:39], s44 offen offset:2048 nt lds\n"
"v_cvt_pk_bf16_f32 v117, v50, v51\n"
"v_mfma_f32_16x16x32_bf16 a[152:155], v[64:67], v[128:131], 0\n"
"ds_read_b32 v9, v7 offset:1280\n"
"v_cvt_pk_bf16_f32 v120, v52, v53\n"
"v_mfma_f32_16x16x32_bf16 a[156:159], v[64:67], v[132:135], 0\n"
"s_waitcnt lgkmcnt(2)\n"
"ds_read_b128 v[64:67], v30 offset:49408\n"
"v_cvt_pk_bf16_f32 v121, v54, v55\n"
"v_mfma_f32_16x16x32_bf16 v[156:159], a[240:243], v[176:179], v[76:79]\n"
"ds_read_b128 v[96:99], v11 offset:0\n"
"v_add_f32 v35, v35, v48\n"
"v_mfma_f32_16x16x32_bf16 v[156:159], a[244:247], v[196:199], v[156:159]\n"
"ds_read_b128 v[100:103], v11 offset:1024\n"
"v_mfma_f32_16x16x32_bf16 v[156:159], a[248:251], v[216:219], v[156:159]\n"
"ds_read_b128 v[104:107], v11 offset:2048\n"
"v_mfma_f32_16x16x32_bf16 v[156:159], a[252:255], v[236:239], v[156:159]\n"
"buffer_load_dwordx4 v20, s[36:39], s44 offen offset:3072 nt lds\n"
"v_mfma_f32_16x16x32_bf16 v[156:159], v[68:71], a[200:203], v[156:159]\n"
"s_add_u32 s44, s44, 32768\n"
"ds_read_b64_tr_b16 v[56:57], v21 offset:32768\n"
"v_mfma_f32_16x16x32_bf16 v[156:159], v[72:75], a[220:223], v[156:159]\n"
"s_waitcnt lgkmcnt(4)\n"
"ds_read_b64_tr_b16 v[58:59], v21 offset:36864\n"
"v_mfma_f32_16x16x32_bf16 v[156:159], v[60:63], a[160:163], v[156:159]\n"
"ds_read_b128 v[108:111], v11 offset:3072\n"
"v_mfma_f32_16x16x32_bf16 v[156:159], v[64:67], a[180:183], v[156:159]\n"
"ds_read_b128 v[112:115], v11 offset:4096\n"
"v_mfma_f32_16x16x32_bf16 v[160:163], a[240:243], v[180:183], v[80:83]\n"
"v_exp_f32 v49, v145\n"
"v_mfma_f32_16x16x32_bf16 v[160:163], a[244:247], v[200:203], v[160:163]\n"
"v_exp_f32 v50, v146\n"
"v_mfma_f32_16x16x32_bf16 v[160:163], a[248:251], v[220:223], v[160:163]\n"
"s_mov_b32 m0, s52\n"
"v_readfirstlane_b32 s59, v9\n"
"v_cvt_pk_bf16_f32 v124, v48, v49\n"
"v_mfma_f32_16x16x32_bf16 v[160:163], a[252:255], v[240:243], v[160:163]\n"
"buffer_load_dwordx4 v13, s[40:43], s46 offen nt lds\n"
"v_add_f32 v35, v35, v49\n"
"v_mfma_f32_16x16x32_bf16 v[160:163], v[60:63], a[164:167], v[160:163]\n"
"s_cmp_lg_u32 s59, s58\n"
"v_exp_f32 v51, v147\n"
"v_mfma_f32_16x16x32_bf16 v[160:163], v[64:67], a[184:187], v[160:163]\n"
"s_cselect_b32 s60, 3, 0\n"
"v_exp_f32 v52, v148\n"
"v_mfma_f32_16x16x32_bf16 v[160:163], v[68:71], a[204:207], v[160:163]\n"
"v_exp_f32 v53, v149\n"
"v_mfma_f32_16x16x32_bf16 v[160:163], v[72:75], a[224:227], v[160:163]\n"
"s_max_u32 s56, s56, s60\n"
"v_exp_f32 v54, v150\n"
"v_mfma_f32_16x16x32_bf16 v[164:167], a[240:243], v[184:187], v[84:87]\n"
"s_mov_b32 s58, s59\n"
"v_exp_f32 v55, v151\n"
"v_mfma_f32_16x16x32_bf16 v[164:167], a[244:247], v[204:207], v[164:167]\n"
"v_exp_f32 v48, v152\n"
"v_mfma_f32_16x16x32_bf16 v[164:167], a[248:251], v[224:227], v[164:167]\n"
"buffer_load_dwordx4 v14, s[40:43], s46 offen offset:1024 nt lds\n"
"v_cvt_pk_bf16_f32 v125, v50, v51\n"
"v_mfma_f32_16x16x32_bf16 v[164:167], a[252:255], v[244:247], v[164:167]\n"
"v_exp_f32 v49, v153\n"
"v_mfma_f32_16x16x32_bf16 v[164:167], v[60:63], a[168:171], v[164:167]\n"
"v_cvt_pk_bf16_f32 v128, v52, v53\n"
"v_cvt_pk_bf16_f32 v129, v54, v55\n"
"v_mfma_f32_16x16x32_bf16 v[164:167], v[64:67], a[188:191], v[164:167]\n"
"v_cvt_pk_bf16_f32 v132, v48, v49\n"
"v_add_f32 v35, v35, v50\n"
"v_mfma_f32_16x16x32_bf16 v[164:167], v[68:71], a[208:211], v[164:167]\n"
"v_exp_f32 v50, v154\n"
"v_mfma_f32_16x16x32_bf16 v[164:167], v[72:75], a[228:231], v[164:167]\n"
"v_add_f32 v35, v35, v51\n"
"v_add_f32 v36, v36, v52\n"
"v_mfma_f32_16x16x32_bf16 v[168:171], a[240:243], v[188:191], v[88:91]\n"
"buffer_load_dwordx4 v15, s[40:43], s46 offen offset:2048 nt lds\n"
"v_add_f32 v36, v36, v53\n"
"v_mfma_f32_16x16x32_bf16 v[168:171], a[244:247], v[208:211], v[168:171]\n"
"v_exp_f32 v51, v155\n"
"v_mfma_f32_16x16x32_bf16 v[168:171], a[248:251], v[228:231], v[168:171]\n"
"v_cvt_pk_bf16_f32 v133, v50, v51\n"
"v_add_f32 v36, v36, v54\n"
"v_mfma_f32_16x16x32_bf16 v[168:171], a[252:255], v[248:251], v[168:171]\n"
"v_add_f32 v36, v36, v55\n"
"v_add_f32 v37, v37, v48\n"
"v_mfma_f32_16x16x32_bf16 v[168:171], v[60:63], a[172:175], v[168:171]\n"
"v_add_f32 v37, v37, v49\n"
"v_add_f32 v37, v37, v50\n"
"v_mfma_f32_16x16x32_bf16 v[168:171], v[64:67], a[192:195], v[168:171]\n"
"v_add_f32 v37, v37, v51\n"
"v_mfma_f32_16x16x32_bf16 v[168:171], v[68:71], a[212:215], v[168:171]\n"
"buffer_load_dwordx4 v16, s[40:43], s46 offen offset:3072 nt lds\n"
"v_mfma_f32_16x16x32_bf16 v[168:171], v[72:75], a[232:235], v[168:171]\n"
"s_add_u32 s46, s46, 32768\n"
"v_mfma_f32_16x16x32_bf16 v[172:175], v[60:63], a[176:179], v[92:95]\n"
"ds_read_b64_tr_b16 v[60:61], v22 offset:32768\n"
"v_mfma_f32_16x16x32_bf16 v[172:175], v[64:67], a[196:199], v[172:175]\n"
"ds_read_b64_tr_b16 v[62:63], v22 offset:36864\n"
"v_mfma_f32_16x16x32_bf16 v[172:175], v[68:71], a[216:219], v[172:175]\n"
"ds_read_b64_tr_b16 v[64:65], v23 offset:32768\n"
"v_mfma_f32_16x16x32_bf16 v[172:175], v[72:75], a[236:239], v[172:175]\n"
"ds_read_b64_tr_b16 v[66:67], v23 offset:36864\n"
"v_mfma_f32_16x16x32_bf16 v[172:175], a[240:243], v[192:195], v[172:175]\n"
"ds_read_b64_tr_b16 v[68:69], v24 offset:32768\n"
"v_mfma_f32_16x16x32_bf16 v[172:175], a[244:247], v[212:215], v[172:175]\n"
"ds_read_b64_tr_b16 v[70:71], v24 offset:36864\n"
"v_mfma_f32_16x16x32_bf16 v[172:175], a[248:251], v[232:235], v[172:175]\n"
"s_cmp_lg_u32 s56, 0\n"
"ds_read_b64_tr_b16 v[72:73], v25 offset:32768\n"
"v_mfma_f32_16x16x32_bf16 v[172:175], a[252:255], v[252:255], v[172:175]\n"
"s_waitcnt lgkmcnt(6)\n"
"s_cbranch_scc1 S3_1_%=\n"
"S3_1_r_%=:\n"
"ds_read_b64_tr_b16 v[74:75], v25 offset:36864\n"
"v_mfma_f32_16x16x32_bf16 a[0:3], v[56:59], v[96:99], a[0:3]\n"
"v_max3_f32 v10, v156, v157, v158\n"
"v_max3_f32 v10, v10, v159, v160\n"
"v_mfma_f32_16x16x32_bf16 a[4:7], v[56:59], v[100:103], a[4:7]\n"
"v_max3_f32 v10, v10, v161, v162\n"
"v_max3_f32 v10, v10, v163, v164\n"
"v_mfma_f32_16x16x32_bf16 a[8:11], v[56:59], v[104:107], a[8:11]\n"
"v_max3_f32 v10, v10, v165, v166\n"
"v_max3_f32 v10, v10, v167, v168\n"
"v_mfma_f32_16x16x32_bf16 a[12:15], v[56:59], v[108:111], a[12:15]\n"
"v_max3_f32 v10, v10, v169, v170\n"
"v_max3_f32 v10, v10, v171, v172\n"
"v_mfma_f32_16x16x32_bf16 a[16:19], v[56:59], v[112:115], a[16:19]\n"
"s_waitcnt lgkmcnt(0)\n"
"ds_read_b64_tr_b16 v[56:57], v26 offset:32768\n"
"v_max3_f32 v10, v10, v173, v174\n"
"v_mfma_f32_16x16x32_bf16 a[20:23], v[60:63], v[96:99], a[20:23]\n"
"ds_read_b64_tr_b16 v[58:59], v26 offset:36864\n"
"v_max_f32 v10, v10, v175\n"
"v_mfma_f32_16x16x32_bf16 a[24:27], v[60:63], v[100:103], a[24:27]\n"
"v_cmp_nge_f32 vcc, s62, v10\n"
"v_mfma_f32_16x16x32_bf16 a[28:31], v[60:63], v[104:107], a[28:31]\n"
"v_mfma_f32_16x16x32_bf16 a[32:35], v[60:63], v[108:111], a[32:35]\n"
"v_mfma_f32_16x16x32_bf16 a[36:39], v[60:63], v[112:115], a[36:39]\n"
"s_cbranch_vccnz R1_1_%=\n"
"R1_1_r_%=:\n"
"ds_read_b64_tr_b16 v[60:61], v27 offset:32768\n"
"v_mfma_f32_16x16x32_bf16 a[40:43], v[64:67], v[96:99], a[40:43]\n"
"ds_read_b64_tr_b16 v[62:63], v27 offset:36864\n"
"v_mfma_f32_16x16x32_bf16 a[44:47], v[64:67], v[100:103], a[44:47]\n"
"v_exp_f32 v52, v156\n"
"v_mfma_f32_16x16x32_bf16 a[48:51], v[64:67], v[104:107], a[48:51]\n"
"v_exp_f32 v53, v157\n"
"v_mfma_f32_16x16x32_bf16 a[52:55], v[64:67], v[108:111], a[52:55]\n"
"v_exp_f32 v54, v158\n"
"v_mfma_f32_16x16x32_bf16 a[56:59], v[64:67], v[112:115], a[56:59]\n"
"ds_read_b64_tr_b16 v[64:65], v28 offset:32768\n"
"v_cvt_pk_bf16_f32 v118, v52, v53\n"
"v_mfma_f32_16x16x32_bf16 a[60:63], v[68:71], v[96:99], a[60:63]\n"
"ds_read_b64_tr_b16 v[66:67], v28 offset:36864\n"
"v_add_f32 v33, v33, v52\n"
"v_mfma_f32_16x16x32_bf16 a[64:67], v[68:71], v[100:103], a[64:67]\n"
"v_exp_f32 v55, v159\n"
"v_mfma_f32_16x16x32_bf16 a[68:71], v[68:71], v[104:107], a[68:71]\n"
"v_exp_f32 v48, v160\n"
"v_mfma_f32_16x16x32_bf16 a[72:75], v[68:71], v[108:111], a[72:75]\n"
"v_exp_f32 v49, v161\n"
"v_mfma_f32_16x16x32_bf16 a[76:79], v[68:71], v[112:115], a[76:79]\n"
"v_exp_f32 v50, v162\n"
"v_mfma_f32_16x16x32_bf16 a[80:83], v[72:75], v[96:99], a[80:83]\n"
"v_exp_f32 v51, v163\n"
"v_mfma_f32_16x16x32_bf16 a[84:87], v[72:75], v[100:103], a[84:87]\n"
"v_exp_f32 v52, v164\n"
"v_mfma_f32_16x16x32_bf16 a[88:91], v[72:75], v[104:107], a[88:91]\n"
"v_cvt_pk_bf16_f32 v119, v54, v55\n"
"v_cvt_pk_bf16_f32 v122, v48, v49\n"
"v_mfma_f32_16x16x32_bf16 a[92:95], v[72:75], v[108:111], a[92:95]\n"
"v_cvt_pk_bf16_f32 v123, v50, v51\n"
"v_add_f32 v33, v33, v53\n"
"v_mfma_f32_16x16x32_bf16 a[96:99], v[72:75], v[112:115], a[96:99]\n"
"s_waitcnt vmcnt(16) lgkmcnt(0)\n"
"s_barrier\n"
"v_mfma_f32_16x16x32_bf16 a[100:103], v[56:59], v[96:99], a[100:103]\n"
"ds_read_b128 a[240:243], v29 offset:0\n"
"v_add_f32 v33, v33, v54\n"
"v_mfma_f32_16x16x32_bf16 a[104:107], v[56:59], v[100:103], a[104:107]\n"
"ds_read_b128 a[244:247], v30 offset:0\n"
"v_add_f32 v33, v33, v55\n"
"v_mfma_f32_16x16x32_bf16 a[108:111], v[56:59], v[104:107], a[108:111]\n"
"ds_read_b128 a[248:251], v31 offset:0\n"
"v_add_f32 v34, v34, v48\n"
"v_mfma_f32_16x16x32_bf16 a[112:115], v[56:59], v[108:111], a[112:115]\n"
"ds_read_b128 a[252:255], v32 offset:0\n"
"v_add_f32 v34, v34, v49\n"
"v_mfma_f32_16x16x32_bf16 a[116:119], v[56:59], v[112:115], a[116:119]\n"
"ds_read_b128 v[68:71], v31 offset:256\n"
"v_add_f32 v34, v34, v50\n"
"v_mfma_f32_16x16x32_bf16 a[120:123], v[60:63], v[96:99], a[120:123]\n"
"ds_read_b128 v[72:75], v32 offset:256\n"
"v_add_f32 v34, v34, v51\n"
"v_mfma_f32_16x16x32_bf16 a[124:127], v[60:63], v[100:103], a[124:127]\n"
"v_exp_f32 v53, v165\n"
"v_mfma_f32_16x16x32_bf16 a[128:131], v[60:63], v[104:107], a[128:131]\n"
"v_exp_f32 v54, v166\n"
"v_mfma_f32_16x16x32_bf16 a[132:135], v[60:63], v[108:111], a[132:135]\n"
"v_exp_f32 v55, v167\n"
"v_mfma_f32_16x16x32_bf16 a[136:139], v[60:63], v[112:115], a[136:139]\n"
"v_exp_f32 v48, v168\n"
"v_mfma_f32_16x16x32_bf16 a[140:143], v[64:67], v[96:99], a[140:143]\n"
"ds_read_b128 v[60:63], v29 offset:256\n"
"v_cvt_pk_bf16_f32 v126, v52, v53\n"
"v_mfma_f32_16x16x32_bf16 a[144:147], v[64:67], v[100:103], a[144:147]\n"
"v_exp_f32 v49, v169\n"
"v_mfma_f32_16x16x32_bf16 a[148:151], v[64:67], v[104:107], a[148:151]\n"
"v_exp_f32 v50, v170\n"
"v_mfma_f32_16x16x32_bf16 a[152:155], v[64:67], v[108:111], a[152:155]\n"
"s_cmp_lg_u32 s56, 0\n"
"v_exp_f32 v51, v171\n"
"v_mfma_f32_16x16x32_bf16 a[156:159], v[64:67], v[112:115], a[156:159]\n"
"s_cbranch_scc1 S1_1_%=\n"
"S1_1_r_%=:\n"
"ds_read_b128 v[64:67], v30 offset:256\n"
"s_waitcnt lgkmcnt(0)\n"
"s_mov_b32 s61, 6\n"
"LOOP_%=:\n"
"s_waitcnt lgkmcnt(1)\n"
"v_mfma_f32_16x16x32_bf16 v[136:139], a[240:243], v[176:179], v[76:79]\n"
"s_mov_b32 m0, s51\n"
"v_cvt_pk_bf16_f32 v127, v54, v55\n"
"v_cvt_pk_bf16_f32 v130, v48, v49\n"
"v_mfma_f32_16x16x32_bf16 v[136:139], a[244:247], v[196:199], v[136:139]\n"
"buffer_load_dwordx4 v17, s[36:39], s45 offen nt lds\n"
"v_cvt_pk_bf16_f32 v131, v50, v51\n"
"v_mfma_f32_16x16x32_bf16 v[136:139], a[248:251], v[216:219], v[136:139]\n"
"v_add_f32 v35, v35, v52\n"
"v_add_f32 v35, v35, v53\n"
"v_mfma_f32_16x16x32_bf16 v[136:139], a[252:255], v[236:239], v[136:139]\n"
"v_exp_f32 v52, v172\n"
"v_mfma_f32_16x16x32_bf16 v[136:139], v[68:71], a[200:203], v[136:139]\n"
"ds_read_b64_tr_b16 v[56:57], v21 offset:16384\n"
"v_add_f32 v35, v35, v54\n"
"v_mfma_f32_16x16x32_bf16 v[136:139], v[72:75], a[220:223], v[136:139]\n"
"ds_read_b64_tr_b16 v[58:59], v21 offset:20480\n"
"v_add_f32 v35, v35, v55\n"
"v_mfma_f32_16x16x32_bf16 v[136:139], v[60:63], a[160:163], v[136:139]\n"
"s_waitcnt lgkmcnt(2)\n"
"buffer_load_dwordx4 v18, s[36:39], s45 offen offset:1024 nt lds\n"
"v_add_f32 v36, v36, v48\n"
"v_mfma_f32_16x16x32_bf16 v[136:139], v[64:67], a[180:183], v[136:139]\n"
"v_exp_f32 v53, v173\n"
"v_mfma_f32_16x16x32_bf16 v[140:143], a[240:243], v[180:183], v[80:83]\n"
"v_exp_f32 v54, v174\n"
"v_mfma_f32_16x16x32_bf16 v[140:143], a[244:247], v[200:203], v[140:143]\n"
"v_exp_f32 v55, v175\n"
"v_mfma_f32_16x16x32_bf16 v[140:143], a[248:251], v[220:223], v[140:143]\n"
"v_cvt_pk_bf16_f32 v134, v52, v53\n"
"v_cvt_pk_bf16_f32 v135, v54, v55\n"
"v_mfma_f32_16x16x32_bf16 v[140:143], a[252:255], v[240:243], v[140:143]\n"
"buffer_load_dwordx4 v19, s[36:39], s45 offen offset:2048 nt lds\n"
"v_add_f32 v36, v36, v49\n"
"v_mfma_f32_16x16x32_bf16 v[140:143], v[60:63], a[164:167], v[140:143]\n"
"v_add_f32 v36, v36, v50\n"
"v_add_f32 v36, v36, v51\n"
"v_mfma_f32_16x16x32_bf16 v[140:143], v[64:67], a[184:187], v[140:143]\n"
"v_add_f32 v37, v37, v52\n"
"v_add_f32 v37, v37, v53\n"
"v_mfma_f32_16x16x32_bf16 v[140:143], v[68:71], a[204:207], v[140:143]\n"
"v_add_f32 v37, v37, v54\n"
"v_add_f32 v37, v37, v55\n"
"v_mfma_f32_16x16x32_bf16 v[140:143], v[72:75], a[224:227], v[140:143]\n"
"v_mfma_f32_16x16x32_bf16 v[144:147], a[240:243], v[184:187], v[84:87]\n"
"buffer_load_dwordx4 v20, s[36:39], s45 offen offset:3072 nt lds\n"
"v_mfma_f32_16x16x32_bf16 v[144:147], a[244:247], v[204:207], v[144:147]\n"
"s_add_u32 s45, s45, 32768\n"
"v_mfma_f32_16x16x32_bf16 v[144:147], a[248:251], v[224:227], v[144:147]\n"
"v_mfma_f32_16x16x32_bf16 v[144:147], a[252:255], v[244:247], v[144:147]\n"
"v_mfma_f32_16x16x32_bf16 v[144:147], v[60:63], a[168:171], v[144:147]\n"
"ds_write_b128 v12, v[116:119] offset:0\n"
"v_mfma_f32_16x16x32_bf16 v[144:147], v[64:67], a[188:191], v[144:147]\n"
"v_mfma_f32_16x16x32_bf16 v[144:147], v[68:71], a[208:211], v[144:147]\n"
"ds_write_b128 v12, v[120:123] offset:1024\n"
"v_mfma_f32_16x16x32_bf16 v[144:147], v[72:75], a[228:231], v[144:147]\n"
"v_mfma_f32_16x16x32_bf16 v[148:151], a[240:243], v[188:191], v[88:91]\n"
"ds_write_b128 v12, v[124:127] offset:2048\n"
"v_mfma_f32_16x16x32_bf16 v[148:151], a[244:247], v[208:211], v[148:151]\n"
"v_mfma_f32_16x16x32_bf16 v[148:151], a[248:251], v[228:231], v[148:151]\n"
"v_mfma_f32_16x16x32_bf16 v[148:151], a[252:255], v[248:251], v[148:151]\n"
"ds_write_b128 v12, v[128:131] offset:3072\n"
"v_mfma_f32_16x16x32_bf16 v[148:151], v[60:63], a[172:175], v[148:151]\n"
"v_mfma_f32_16x16x32_bf16 v[148:151], v[64:67], a[192:195], v[148:151]\n"
"ds_write_b128 v12, v[132:135] offset:4096\n"
"v_mfma_f32_16x16x32_bf16 v[148:151], v[68:71], a[212:215], v[148:151]\n"
"v_mfma_f32_16x16x32_bf16 v[148:151], v[72:75], a[232:235], v[148:151]\n"
"v_mfma_f32_16x16x32_bf16 v[152:155], v[60:63], a[176:179], v[92:95]\n"
"ds_read_b64_tr_b16 v[60:61], v22 offset:16384\n"
"v_mfma_f32_16x16x32_bf16 v[152:155], v[64:67], a[196:199], v[152:155]\n"
"ds_read_b64_tr_b16 v[62:63], v22 offset:20480\n"
"v_mfma_f32_16x16x32_bf16 v[152:155], v[68:71], a[216:219], v[152:155]\n"
"ds_read_b64_tr_b16 v[64:65], v23 offset:16384\n"
"v_mfma_f32_16x16x32_bf16 v[152:155], v[72:75], a[236:239], v[152:155]\n"
"ds_read_b64_tr_b16 v[66:67], v23 offset:20480\n"
"v_mfma_f32_16x16x32_bf16 v[152:155], a[240:243], v[192:195], v[152:155]\n"
"ds_read_b64_tr_b16 v[68:69], v24 offset:16384\n"
"v_mfma_f32_16x16x32_bf16 v[152:155], a[244:247], v[212:215], v[152:155]\n"
"ds_read_b64_tr_b16 v[70:71], v24 offset:20480\n"
"v_mfma_f32_16x16x32_bf16 v[152:155], a[248:251], v[232:235], v[152:155]\n"
"s_cmp_lg_u32 s56, 0\n"
"ds_read_b64_tr_b16 v[72:73], v25 offset:16384\n"
"v_mfma_f32_16x16x32_bf16 v[152:155], a[252:255], v[252:255], v[152:155]\n"
"s_waitcnt lgkmcnt(6)\n"
"s_cbranch_scc1 S2_2v0_%=\n"
"S2_2v0_r_%=:\n"
"ds_read_b64_tr_b16 v[74:75], v25 offset:20480\n"
"v_mfma_f32_16x16x32_bf16 a[0:3], v[56:59], v[116:119], a[0:3]\n"
"s_mov_b32 m0, s54\n"
"v_max3_f32 v10, v136, v137, v138\n"
"v_max3_f32 v10, v10, v139, v140\n"
"v_mfma_f32_16x16x32_bf16 a[4:7], v[56:59], v[120:123], a[4:7]\n"
"buffer_load_dwordx4 v13, s[40:43], s47 offen nt lds\n"
"v_max3_f32 v10, v10, v141, v142\n"
"v_mfma_f32_16x16x32_bf16 a[8:11], v[56:59], v[124:127], a[8:11]\n"
"v_max3_f32 v10, v10, v143, v144\n"
"v_max3_f32 v10, v10, v145, v146\n"
"v_mfma_f32_16x16x32_bf16 a[12:15], v[56:59], v[128:131], a[12:15]\n"
"v_max3_f32 v10, v10, v147, v148\n"
"v_max3_f32 v10, v10, v149, v150\n"
"v_mfma_f32_16x16x32_bf16 a[16:19], v[56:59], v[132:135], a[16:19]\n"
"s_waitcnt lgkmcnt(0)\n"
"ds_read_b64_tr_b16 v[56:57], v26 offset:16384\n"
"v_max3_f32 v10, v10, v151, v152\n"
"v_mfma_f32_16x16x32_bf16 a[20:23], v[60:63], v[116:119], a[20:23]\n"
"ds_read_b64_tr_b16 v[58:59], v26 offset:20480\n"
"v_max3_f32 v10, v10, v153, v154\n"
"v_mfma_f32_16x16x32_bf16 a[24:27], v[60:63], v[120:123], a[24:27]\n"
"buffer_load_dwordx4 v14, s[40:43], s47 offen offset:1024 nt lds\n"
"v_max_f32 v10, v10, v155\n"
"v_mfma_f32_16x16x32_bf16 a[28:31], v[60:63], v[124:127], a[28:31]\n"
"v_cmp_nge_f32 vcc, s62, v10\n"
"v_mfma_f32_16x16x32_bf16 a[32:35], v[60:63], v[128:131], a[32:35]\n"
"v_mfma_f32_16x16x32_bf16 a[36:39], v[60:63], v[132:135], a[36:39]\n"
"ds_read_b64_tr_b16 v[60:61], v27 offset:16384\n"
"v_mfma_f32_16x16x32_bf16 a[40:43], v[64:67], v[116:119], a[40:43]\n"
"ds_read_b64_tr_b16 v[62:63], v27 offset:20480\n"
"v_mfma_f32_16x16x32_bf16 a[44:47], v[64:67], v[120:123], a[44:47]\n"
"buffer_load_dwordx4 v15, s[40:43], s47 offen offset:2048 nt lds\n"
"v_mfma_f32_16x16x32_bf16 a[48:51], v[64:67], v[124:127], a[48:51]\n"
"v_mfma_f32_16x16x32_bf16 a[52:55], v[64:67], v[128:131], a[52:55]\n"
"v_mfma_f32_16x16x32_bf16 a[56:59], v[64:67], v[132:135], a[56:59]\n"
"s_cbranch_vccnz R0_2v0_%=\n"
"R0_2v0_r_%=:\n"
"ds_read_b64_tr_b16 v[64:65], v28 offset:16384\n"
"v_mfma_f32_16x16x32_bf16 a[60:63], v[68:71], v[116:119], a[60:63]\n"
"ds_read_b64_tr_b16 v[66:67], v28 offset:20480\n"
"v_mfma_f32_16x16x32_bf16 a[64:67], v[68:71], v[120:123], a[64:67]\n"
"buffer_load_dwordx4 v16, s[40:43], s47 offen offset:3072 nt lds\n"
"v_mfma_f32_16x16x32_bf16 a[68:71], v[68:71], v[124:127], a[68:71]\n"
"s_add_u32 s47, s47, 32768\n"
"v_exp_f32 v48, v136\n"
"v_mfma_f32_16x16x32_bf16 a[72:75], v[68:71], v[128:131], a[72:75]\n"
"v_exp_f32 v49, v137\n"
"v_mfma_f32_16x16x32_bf16 a[76:79], v[68:71], v[132:135], a[76:79]\n"
"v_exp_f32 v50, v138\n"
"v_mfma_f32_16x16x32_bf16 a[80:83], v[72:75], v[116:119], a[80:83]\n"
"v_exp_f32 v51, v139\n"
"v_mfma_f32_16x16x32_bf16 a[84:87], v[72:75], v[120:123], a[84:87]\n"
"v_exp_f32 v52, v140\n"
"v_mfma_f32_16x16x32_bf16 a[88:91], v[72:75], v[124:127], a[88:91]\n"
"v_exp_f32 v53, v141\n"
"v_mfma_f32_16x16x32_bf16 a[92:95], v[72:75], v[128:131], a[92:95]\n"
"v_exp_f32 v54, v142\n"
"v_mfma_f32_16x16x32_bf16 a[96:99], v[72:75], v[132:135], a[96:99]\n"
"s_waitcnt vmcnt(16) lgkmcnt(0)\n"
"s_barrier\n"
"v_mfma_f32_16x16x32_bf16 a[100:103], v[56:59], v[116:119], a[100:103]\n"
"ds_read_b128 a[240:243], v29 offset:16384\n"
"v_add_f32 v33, v33, v48\n"
"v_mfma_f32_16x16x32_bf16 a[104:107], v[56:59], v[120:123], a[104:107]\n"
"s_mov_b32 m0, s48\n"
"ds_read_b128 a[244:247], v30 offset:16384\n"
"v_add_f32 v33, v33, v49\n"
"v_mfma_f32_16x16x32_bf16 a[108:111], v[56:59], v[124:127], a[108:111]\n"
"buffer_load_dwordx4 v17, s[36:39], s44 offen nt lds\n"
"v_add_f32 v33, v33, v50\n"
"v_mfma_f32_16x16x32_bf16 a[112:115], v[56:59], v[128:131], a[112:115]\n"
"ds_read_b128 a[248:251], v31 offset:16384\n"
"v_add_f32 v33, v33, v51\n"
"v_mfma_f32_16x16x32_bf16 a[116:119], v[56:59], v[132:135], a[116:119]\n"
"ds_read_b128 a[252:255], v32 offset:16384\n"
"v_add_f32 v34, v34, v52\n"
"v_mfma_f32_16x16x32_bf16 a[120:123], v[60:63], v[116:119], a[120:123]\n"
"ds_read_b128 v[68:71], v31 offset:16640\n"
"v_add_f32 v34, v34, v53\n"
"v_mfma_f32_16x16x32_bf16 a[124:127], v[60:63], v[120:123], a[124:127]\n"
"ds_read_b128 v[72:75], v32 offset:16640\n"
"v_add_f32 v34, v34, v54\n"
"v_mfma_f32_16x16x32_bf16 a[128:131], v[60:63], v[124:127], a[128:131]\n"
"buffer_load_dwordx4 v18, s[36:39], s44 offen offset:1024 nt lds\n"
"v_mfma_f32_16x16x32_bf16 a[132:135], v[60:63], v[128:131], a[132:135]\n"
"v_exp_f32 v55, v143\n"
"v_mfma_f32_16x16x32_bf16 a[136:139], v[60:63], v[132:135], a[136:139]\n"
"v_add_f32 v34, v34, v55\n"
"v_mfma_f32_16x16x32_bf16 a[140:143], v[64:67], v[116:119], a[140:143]\n"
"ds_read_b128 v[60:63], v29 offset:16640\n"
"v_cvt_pk_bf16_f32 v116, v48, v49\n"
"v_mfma_f32_16x16x32_bf16 a[144:147], v[64:67], v[120:123], a[144:147]\n"
"v_exp_f32 v48, v144\n"
"v_mfma_f32_16x16x32_bf16 a[148:151], v[64:67], v[124:127], a[148:151]\n"
"buffer_load_dwordx4 v19, s[36:39], s44 offen offset:2048 nt lds\n"
"v_cvt_pk_bf16_f32 v117, v50, v51\n"
"v_mfma_f32_16x16x32_bf16 a[152:155], v[64:67], v[128:131], a[152:155]\n"
"ds_read_b32 v9, v7 offset:1280\n"
"v_cvt_pk_bf16_f32 v120, v52, v53\n"
"v_mfma_f32_16x16x32_bf16 a[156:159], v[64:67], v[132:135], a[156:159]\n"
"s_waitcnt lgkmcnt(2)\n"
"ds_read_b128 v[64:67], v30 offset:16640\n"
"v_cvt_pk_bf16_f32 v121, v54, v55\n"
"v_mfma_f32_16x16x32_bf16 v[156:159], a[240:243], v[176:179], v[76:79]\n"
"ds_read_b128 v[96:99], v11 offset:0\n"
"v_add_f32 v35, v35, v48\n"
"v_mfma_f32_16x16x32_bf16 v[156:159], a[244:247], v[196:199], v[156:159]\n"
"ds_read_b128 v[100:103], v11 offset:1024\n"
"v_mfma_f32_16x16x32_bf16 v[156:159], a[248:251], v[216:219], v[156:159]\n"
"ds_read_b128 v[104:107], v11 offset:2048\n"
"v_mfma_f32_16x16x32_bf16 v[156:159], a[252:255], v[236:239], v[156:159]\n"
"buffer_load_dwordx4 v20, s[36:39], s44 offen offset:3072 nt lds\n"
"v_mfma_f32_16x16x32_bf16 v[156:159], v[68:71], a[200:203], v[156:159]\n"
"s_add_u32 s44, s44, 32768\n"
"ds_read_b64_tr_b16 v[56:57], v21 offset:49152\n"
"v_mfma_f32_16x16x32_bf16 v[156:159], v[72:75], a[220:223], v[156:159]\n"
"s_waitcnt lgkmcnt(4)\n"
"ds_read_b64_tr_b16 v[58:59], v21 offset:53248\n"
"v_mfma_f32_16x16x32_bf16 v[156:159], v[60:63], a[160:163], v[156:159]\n"
"ds_read_b128 v[108:111], v11 offset:3072\n"
"v_mfma_f32_16x16x32_bf16 v[156:159], v[64:67], a[180:183], v[156:159]\n"
"ds_read_b128 v[112:115], v11 offset:4096\n"
"v_mfma_f32_16x16x32_bf16 v[160:163], a[240:243], v[180:183], v[80:83]\n"
"v_exp_f32 v49, v145\n"
"v_mfma_f32_16x16x32_bf16 v[160:163], a[244:247], v[200:203], v[160:163]\n"
"v_exp_f32 v50, v146\n"
"v_mfma_f32_16x16x32_bf16 v[160:163], a[248:251], v[220:223], v[160:163]\n"
"s_mov_b32 m0, s53\n"
"v_readfirstlane_b32 s59, v9\n"
"v_cvt_pk_bf16_f32 v124, v48, v49\n"
"v_mfma_f32_16x16x32_bf16 v[160:163], a[252:255], v[240:243], v[160:163]\n"
"buffer_load_dwordx4 v13, s[40:43], s46 offen nt lds\n"
"v_add_f32 v35, v35, v49\n"
"v_mfma_f32_16x16x32_bf16 v[160:163], v[60:63], a[164:167], v[160:163]\n"
"s_cmp_lg_u32 s59, s58\n"
"v_exp_f32 v51, v147\n"
"v_mfma_f32_16x16x32_bf16 v[160:163], v[64:67], a[184:187], v[160:163]\n"
"s_cselect_b32 s60, 3, 0\n"
"v_exp_f32 v52, v148\n"
"v_mfma_f32_16x16x32_bf16 v[160:163], v[68:71], a[204:207], v[160:163]\n"
"v_exp_f32 v53, v149\n"
"v_mfma_f32_16x16x32_bf16 v[160:163], v[72:75], a[224:227], v[160:163]\n"
"s_max_u32 s56, s56, s60\n"
"v_exp_f32 v54, v150\n"
"v_mfma_f32_16x16x32_bf16 v[164:167], a[240:243], v[184:187], v[84:87]\n"
"s_mov_b32 s58, s59\n"
"v_exp_f32 v55, v151\n"
"v_mfma_f32_16x16x32_bf16 v[164:167], a[244:247], v[204:207], v[164:167]\n"
"v_exp_f32 v48, v152\n"
"v_mfma_f32_16x16x32_bf16 v[164:167], a[248:251], v[224:227], v[164:167]\n"
"buffer_load_dwordx4 v14, s[40:43], s46 offen offset:1024 nt lds\n"
"v_cvt_pk_bf16_f32 v125, v50, v51\n"
"v_mfma_f32_16x16x32_bf16 v[164:167], a[252:255], v[244:247], v[164:167]\n"
"v_exp_f32 v49, v153\n"
"v_mfma_f32_16x16x32_bf16 v[164:167], v[60:63], a[168:171], v[164:167]\n"
"v_cvt_pk_bf16_f32 v128, v52, v53\n"
"v_cvt_pk_bf16_f32 v129, v54, v55\n"
"v_mfma_f32_16x16x32_bf16 v[164:167], v[64:67], a[188:191], v[164:167]\n"
"v_cvt_pk_bf16_f32 v132, v48, v49\n"
"v_add_f32 v35, v35, v50\n"
"v_mfma_f32_16x16x32_bf16 v[164:167], v[68:71], a[208:211], v[164:167]\n"
"v_exp_f32 v50, v154\n"
"v_mfma_f32_16x16x32_bf16 v[164:167], v[72:75], a[228:231], v[164:167]\n"
"v_add_f32 v35, v35, v51\n"
"v_add_f32 v36, v36, v52\n"
"v_mfma_f32_16x16x32_bf16 v[168:171], a[240:243], v[188:191], v[88:91]\n"
"buffer_load_dwordx4 v15, s[40:43], s46 offen offset:2048 nt lds\n"
"v_add_f32 v36, v36, v53\n"
"v_mfma_f32_16x16x32_bf16 v[168:171], a[244:247], v[208:211], v[168:171]\n"
"v_exp_f32 v51, v155\n"
"v_mfma_f32_16x16x32_bf16 v[168:171], a[248:251], v[228:231], v[168:171]\n"
"v_cvt_pk_bf16_f32 v133, v50, v51\n"
"v_add_f32 v36, v36, v54\n"
"v_mfma_f32_16x16x32_bf16 v[168:171], a[252:255], v[248:251], v[168:171]\n"
"v_add_f32 v36, v36, v55\n"
"v_add_f32 v37, v37, v48\n"
"v_mfma_f32_16x16x32_bf16 v[168:171], v[60:63], a[172:175], v[168:171]\n"
"v_add_f32 v37, v37, v49\n"
"v_add_f32 v37, v37, v50\n"
"v_mfma_f32_16x16x32_bf16 v[168:171], v[64:67], a[192:195], v[168:171]\n"
"v_add_f32 v37, v37, v51\n"
"v_mfma_f32_16x16x32_bf16 v[168:171], v[68:71], a[212:215], v[168:171]\n"
"buffer_load_dwordx4 v16, s[40:43], s46 offen offset:3072 nt lds\n"
"v_mfma_f32_16x16x32_bf16 v[168:171], v[72:75], a[232:235], v[168:171]\n"
"s_add_u32 s46, s46, 32768\n"
"v_mfma_f32_16x16x32_bf16 v[172:175], v[60:63], a[176:179], v[92:95]\n"
"ds_read_b64_tr_b16 v[60:61], v22 offset:49152\n"
"v_mfma_f32_16x16x32_bf16 v[172:175], v[64:67], a[196:199], v[172:175]\n"
"ds_read_b64_tr_b16 v[62:63], v22 offset:53248\n"
"v_mfma_f32_16x16x32_bf16 v[172:175], v[68:71], a[216:219], v[172:175]\n"
"ds_read_b64_tr_b16 v[64:65], v23 offset:49152\n"
"v_mfma_f32_16x16x32_bf16 v[172:175], v[72:75], a[236:239], v[172:175]\n"
"ds_read_b64_tr_b16 v[66:67], v23 offset:53248\n"
"v_mfma_f32_16x16x32_bf16 v[172:175], a[240:243], v[192:195], v[172:175]\n"
"ds_read_b64_tr_b16 v[68:69], v24 offset:49152\n"
"v_mfma_f32_16x16x32_bf16 v[172:175], a[244:247], v[212:215], v[172:175]\n"
"ds_read_b64_tr_b16 v[70:71], v24 offset:53248\n"
"v_mfma_f32_16x16x32_bf16 v[172:175], a[248:251], v[232:235], v[172:175]\n"
"s_cmp_lg_u32 s56, 0\n"
"ds_read_b64_tr_b16 v[72:73], v25 offset:49152\n"
"v_mfma_f32_16x16x32_bf16 v[172:175], a[252:255], v[252:255], v[172:175]\n"
"s_waitcnt lgkmcnt(6)\n"
"s_cbranch_scc1 S3_2v0_%=\n"
"S3_2v0_r_%=:\n"
"ds_read_b64_tr_b16 v[74:75], v25 offset:53248\n"
"v_mfma_f32_16x16x32_bf16 a[0:3], v[56:59], v[96:99], a[0:3]\n"
"v_max3_f32 v10, v156, v157, v158\n"
"v_max3_f32 v10, v10, v159, v160\n"
"v_mfma_f32_16x16x32_bf16 a[4:7], v[56:59], v[100:103], a[4:7]\n"
"v_max3_f32 v10, v10, v161, v162\n"
"v_max3_f32 v10, v10, v163, v164\n"
"v_mfma_f32_16x16x32_bf16 a[8:11], v[56:59], v[104:107], a[8:11]\n"
"v_max3_f32 v10, v10, v165, v166\n"
"v_max3_f32 v10, v10, v167, v168\n"
"v_mfma_f32_16x16x32_bf16 a[12:15], v[56:59], v[108:111], a[12:15]\n"
"v_max3_f32 v10, v10, v169, v170\n"
"v_max3_f32 v10, v10, v171, v172\n"
"v_mfma_f32_16x16x32_bf16 a[16:19], v[56:59], v[112:115], a[16:19]\n"
"s_waitcnt lgkmcnt(0)\n"
"ds_read_b64_tr_b16 v[56:57], v26 offset:49152\n"
"v_max3_f32 v10, v10, v173, v174\n"
"v_mfma_f32_16x16x32_bf16 a[20:23], v[60:63], v[96:99], a[20:23]\n"
"ds_read_b64_tr_b16 v[58:59], v26 offset:53248\n"
"v_max_f32 v10, v10, v175\n"
"v_mfma_f32_16x16x32_bf16 a[24:27], v[60:63], v[100:103], a[24:27]\n"
"v_cmp_nge_f32 vcc, s62, v10\n"
"v_mfma_f32_16x16x32_bf16 a[28:31], v[60:63], v[104:107], a[28:31]\n"
"v_mfma_f32_16x16x32_bf16 a[32:35], v[60:63], v[108:111], a[32:35]\n"
"v_mfma_f32_16x16x32_bf16 a[36:39], v[60:63], v[112:115], a[36:39]\n"
"s_cbranch_vccnz R1_2v0_%=\n"
"R1_2v0_r_%=:\n"
"ds_read_b64_tr_b16 v[60:61], v27 offset:49152\n"
"v_mfma_f32_16x16x32_bf16 a[40:43], v[64:67], v[96:99], a[40:43]\n"
"ds_read_b64_tr_b16 v[62:63], v27 offset:53248\n"
"v_mfma_f32_16x16x32_bf16 a[44:47], v[64:67], v[100:103], a[44:47]\n"
"v_exp_f32 v52, v156\n"
"v_mfma_f32_16x16x32_bf16 a[48:51], v[64:67], v[104:107], a[48:51]\n"
"v_exp_f32 v53, v157\n"
"v_mfma_f32_16x16x32_bf16 a[52:55], v[64:67], v[108:111], a[52:55]\n"
"v_exp_f32 v54, v158\n"
"v_mfma_f32_16x16x32_bf16 a[56:59], v[64:67], v[112:115], a[56:59]\n"
"ds_read_b64_tr_b16 v[64:65], v28 offset:49152\n"
"v_cvt_pk_bf16_f32 v118, v52, v53\n"
"v_mfma_f32_16x16x32_bf16 a[60:63], v[68:71], v[96:99], a[60:63]\n"
"ds_read_b64_tr_b16 v[66:67], v28 offset:53248\n"
"v_add_f32 v33, v33, v52\n"
"v_mfma_f32_16x16x32_bf16 a[64:67], v[68:71], v[100:103], a[64:67]\n"
"v_exp_f32 v55, v159\n"
"v_mfma_f32_16x16x32_bf16 a[68:71], v[68:71], v[104:107], a[68:71]\n"
"v_exp_f32 v48, v160\n"
"v_mfma_f32_16x16x32_bf16 a[72:75], v[68:71], v[108:111], a[72:75]\n"
"v_exp_f32 v49, v161\n"
"v_mfma_f32_16x16x32_bf16 a[76:79], v[68:71], v[112:115], a[76:79]\n"
"v_exp_f32 v50, v162\n"
"v_mfma_f32_16x16x32_bf16 a[80:83], v[72:75], v[96:99], a[80:83]\n"
"v_exp_f32 v51, v163\n"
"v_mfma_f32_16x16x32_bf16 a[84:87], v[72:75], v[100:103], a[84:87]\n"
"v_exp_f32 v52, v164\n"
"v_mfma_f32_16x16x32_bf16 a[88:91], v[72:75], v[104:107], a[88:91]\n"
"v_cvt_pk_bf16_f32 v119, v54, v55\n"
"v_cvt_pk_bf16_f32 v122, v48, v49\n"
"v_mfma_f32_16x16x32_bf16 a[92:95], v[72:75], v[108:111], a[92:95]\n"
"v_cvt_pk_bf16_f32 v123, v50, v51\n"
"v_add_f32 v33, v33, v53\n"
"v_mfma_f32_16x16x32_bf16 a[96:99], v[72:75], v[112:115], a[96:99]\n"
"s_waitcnt vmcnt(16) lgkmcnt(0)\n"
"s_barrier\n"
"v_mfma_f32_16x16x32_bf16 a[100:103], v[56:59], v[96:99], a[100:103]\n"
"ds_read_b128 a[240:243], v29 offset:32768\n"
"v_add_f32 v33, v33, v54\n"
"v_mfma_f32_16x16x32_bf16 a[104:107], v[56:59], v[100:103], a[104:107]\n"
"ds_read_b128 a[244:247], v30 offset:32768\n"
"v_add_f32 v33, v33, v55\n"
"v_mfma_f32_16x16x32_bf16 a[108:111], v[56:59], v[104:107], a[108:111]\n"
"ds_read_b128 a[248:251], v31 offset:32768\n"
"v_add_f32 v34, v34, v48\n"
"v_mfma_f32_16x16x32_bf16 a[112:115], v[56:59], v[108:111], a[112:115]\n"
"ds_read_b128 a[252:255], v32 offset:32768\n"
"v_add_f32 v34, v34, v49\n"
"v_mfma_f32_16x16x32_bf16 a[116:119], v[56:59], v[112:115], a[116:119]\n"
"ds_read_b128 v[68:71], v31 offset:33024\n"
"v_add_f32 v34, v34, v50\n"
"v_mfma_f32_16x16x32_bf16 a[120:123], v[60:63], v[96:99], a[120:123]\n"
"ds_read_b128 v[72:75], v32 offset:33024\n"
"v_add_f32 v34, v34, v51\n"
"v_mfma_f32_16x16x32_bf16 a[124:127], v[60:63], v[100:103], a[124:127]\n"
"v_exp_f32 v53, v165\n"
"v_mfma_f32_16x16x32_bf16 a[128:131], v[60:63], v[104:107], a[128:131]\n"
"v_exp_f32 v54, v166\n"
"v_mfma_f32_16x16x32_bf16 a[132:135], v[60:63], v[108:111], a[132:135]\n"
"v_exp_f32 v55, v167\n"
"v_mfma_f32_16x16x32_bf16 a[136:139], v[60:63], v[112:115], a[136:139]\n"
"v_exp_f32 v48, v168\n"
"v_mfma_f32_16x16x32_bf16 a[140:143], v[64:67], v[96:99], a[140:143]\n"
"ds_read_b128 v[60:63], v29 offset:33024\n"
"v_cvt_pk_bf16_f32 v126, v52, v53\n"
"v_mfma_f32_16x16x32_bf16 a[144:147], v[64:67], v[100:103], a[144:147]\n"
"v_exp_f32 v49, v169\n"
"v_mfma_f32_16x16x32_bf16 a[148:151], v[64:67], v[104:107], a[148:151]\n"
"v_exp_f32 v50, v170\n"
"v_mfma_f32_16x16x32_bf16 a[152:155], v[64:67], v[108:111], a[152:155]\n"
"s_cmp_lg_u32 s56, 0\n"
"v_exp_f32 v51, v171\n"
"v_mfma_f32_16x16x32_bf16 a[156:159], v[64:67], v[112:115], a[156:159]\n"
"s_cbranch_scc1 S1_2v0_%=\n"
"S1_2v0_r_%=:\n"
"ds_read_b128 v[64:67], v30 offset:33024\n"
"s_waitcnt lgkmcnt(1)\n"
"v_mfma_f32_16x16x32_bf16 v[136:139], a[240:243], v[176:179], v[76:79]\n"
"s_mov_b32 m0, s50\n"
"v_cvt_pk_bf16_f32 v127, v54, v55\n"
"v_cvt_pk_bf16_f32 v130, v48, v49\n"
"v_mfma_f32_16x16x32_bf16 v[136:139], a[244:247], v[196:199], v[136:139]\n"
"buffer_load_dwordx4 v17, s[36:39], s45 offen nt lds\n"
"v_cvt_pk_bf16_f32 v131, v50, v51\n"
"v_mfma_f32_16x16x32_bf16 v[136:139], a[248:251], v[216:219], v[136:139]\n"
"v_add_f32 v35, v35, v52\n"
"v_add_f32 v35, v35, v53\n"
"v_mfma_f32_16x16x32_bf16 v[136:139], a[252:255], v[236:239], v[136:139]\n"
"v_exp_f32 v52, v172\n"
"v_mfma_f32_16x16x32_bf16 v[136:139], v[68:71], a[200:203], v[136:139]\n"
"ds_read_b64_tr_b16 v[56:57], v21 offset:0\n"
"v_add_f32 v35, v35, v54\n"
"v_mfma_f32_16x16x32_bf16 v[136:139], v[72:75], a[220:223], v[136:139]\n"
"ds_read_b64_tr_b16 v[58:59], v21 offset:4096\n"
"v_add_f32 v35, v35, v55\n"
"v_mfma_f32_16x16x32_bf16 v[136:139], v[60:63], a[160:163], v[136:139]\n"
"s_waitcnt lgkmcnt(2)\n"
"buffer_load_dwordx4 v18, s[36:39], s45 offen offset:1024 nt lds\n"
"v_add_f32 v36, v36, v48\n"
"v_mfma_f32_16x16x32_bf16 v[136:139], v[64:67], a[180:183], v[136:139]\n"
"v_exp_f32 v53, v173\n"
"v_mfma_f32_16x16x32_bf16 v[140:143], a[240:243], v[180:183], v[80:83]\n"
"v_exp_f32 v54, v174\n"
"v_mfma_f32_16x16x32_bf16 v[140:143], a[244:247], v[200:203], v[140:143]\n"
"v_exp_f32 v55, v175\n"
"v_mfma_f32_16x16x32_bf16 v[140:143], a[248:251], v[220:223], v[140:143]\n"
"v_cvt_pk_bf16_f32 v134, v52, v53\n"
"v_cvt_pk_bf16_f32 v135, v54, v55\n"
"v_mfma_f32_16x16x32_bf16 v[140:143], a[252:255], v[240:243], v[140:143]\n"
"buffer_load_dwordx4 v19, s[36:39], s45 offen offset:2048 nt lds\n"
"v_add_f32 v36, v36, v49\n"
"v_mfma_f32_16x16x32_bf16 v[140:143], v[60:63], a[164:167], v[140:143]\n"
"v_add_f32 v36, v36, v50\n"
"v_add_f32 v36, v36, v51\n"
"v_mfma_f32_16x16x32_bf16 v[140:143], v[64:67], a[184:187], v[140:143]\n"
"v_add_f32 v37, v37, v52\n"
"v_add_f32 v37, v37, v53\n"
"v_mfma_f32_16x16x32_bf16 v[140:143], v[68:71], a[204:207], v[140:143]\n"
"v_add_f32 v37, v37, v54\n"
"v_add_f32 v37, v37, v55\n"
"v_mfma_f32_16x16x32_bf16 v[140:143], v[72:75], a[224:227], v[140:143]\n"
"v_mfma_f32_16x16x32_bf16 v[144:147], a[240:243], v[184:187], v[84:87]\n"
"buffer_load_dwordx4 v20, s[36:39], s45 offen offset:3072 nt lds\n"
"v_mfma_f32_16x16x32_bf16 v[144:147], a[244:247], v[204:207], v[144:147]\n"
"s_add_u32 s45, s45, 32768\n"
"v_mfma_f32_16x16x32_bf16 v[144:147], a[248:251], v[224:227], v[144:147]\n"
"v_mfma_f32_16x16x32_bf16 v[144:147], a[252:255], v[244:247], v[144:147]\n"
"v_mfma_f32_16x16x32_bf16 v[144:147], v[60:63], a[168:171], v[144:147]\n"
"ds_write_b128 v12, v[116:119] offset:0\n"
"v_mfma_f32_16x16x32_bf16 v[144:147], v[64:67], a[188:191], v[144:147]\n"
"v_mfma_f32_16x16x32_bf16 v[144:147], v[68:71], a[208:211], v[144:147]\n"
"ds_write_b128 v12, v[120:123] offset:1024\n"
"v_mfma_f32_16x16x32_bf16 v[144:147], v[72:75], a[228:231], v[144:147]\n"
"v_mfma_f32_16x16x32_bf16 v[148:151], a[240:243], v[188:191], v[88:91]\n"
"ds_write_b128 v12, v[124:127] offset:2048\n"
"v_mfma_f32_16x16x32_bf16 v[148:151], a[244:247], v[208:211], v[148:151]\n"
"v_mfma_f32_16x16x32_bf16 v[148:151], a[248:251], v[228:231], v[148:151]\n"
"v_mfma_f32_16x16x32_bf16 v[148:151], a[252:255], v[248:251], v[148:151]\n"
"ds_write_b128 v12, v[128:131] offset:3072\n"
"v_mfma_f32_16x16x32_bf16 v[148:151], v[60:63], a[172:175], v[148:151]\n"
"v_mfma_f32_16x16x32_bf16 v[148:151], v[64:67], a[192:195], v[148:151]\n"
"ds_write_b128 v12, v[132:135] offset:4096\n"
"v_mfma_f32_16x16x32_bf16 v[148:151], v[68:71], a[212:215], v[148:151]\n"
"v_mfma_f32_16x16x32_bf16 v[148:151], v[72:75], a[232:235], v[148:151]\n"
"v_mfma_f32_16x16x32_bf16 v[152:155], v[60:63], a[176:179], v[92:95]\n"
"ds_read_b64_tr_b16 v[60:61], v22 offset:0\n"
"v_mfma_f32_16x16x32_bf16 v[152:155], v[64:67], a[196:199], v[152:155]\n"
"ds_read_b64_tr_b16 v[62:63], v22 offset:4096\n"
"v_mfma_f32_16x16x32_bf16 v[152:155], v[68:71], a[216:219], v[152:155]\n"
"ds_read_b64_tr_b16 v[64:65], v23 offset:0\n"
"v_mfma_f32_16x16x32_bf16 v[152:155], v[72:75], a[236:239], v[152:155]\n"
"ds_read_b64_tr_b16 v[66:67], v23 offset:4096\n"
"v_mfma_f32_16x16x32_bf16 v[152:155], a[240:243], v[192:195], v[152:155]\n"
"ds_read_b64_tr_b16 v[68:69], v24 offset:0\n"
"v_mfma_f32_16x16x32_bf16 v[152:155], a[244:247], v[212:215], v[152:155]\n"
"ds_read_b64_tr_b16 v[70:71], v24 offset:4096\n"
"v_mfma_f32_16x16x32_bf16 v[152:155], a[248:251], v[232:235], v[152:155]\n"
"s_cmp_lg_u32 s56, 0\n"
"ds_read_b64_tr_b16 v[72:73], v25 offset:0\n"
"v_mfma_f32_16x16x32_bf16 v[152:155], a[252:255], v[252:255], v[152:155]\n"
"s_waitcnt lgkmcnt(6)\n"
"s_cbranch_scc1 S2_3v0_%=\n"
"S2_3v0_r_%=:\n"
"ds_read_b64_tr_b16 v[74:75], v25 offset:4096\n"
"v_mfma_f32_16x16x32_bf16 a[0:3], v[56:59], v[116:119], a[0:3]\n"
"s_mov_b32 m0, s55\n"
"v_max3_f32 v10, v136, v137, v138\n"
"v_max3_f32 v10, v10, v139, v140\n"
"v_mfma_f32_16x16x32_bf16 a[4:7], v[56:59], v[120:123], a[4:7]\n"
"buffer_load_dwordx4 v13, s[40:43], s47 offen nt lds\n"
"v_max3_f32 v10, v10, v141, v142\n"
"v_mfma_f32_16x16x32_bf16 a[8:11], v[56:59], v[124:127], a[8:11]\n"
"v_max3_f32 v10, v10, v143, v144\n"
"v_max3_f32 v10, v10, v145, v146\n"
"v_mfma_f32_16x16x32_bf16 a[12:15], v[56:59], v[128:131], a[12:15]\n"
"v_max3_f32 v10, v10, v147, v148\n"
"v_max3_f32 v10, v10, v149, v150\n"
"v_mfma_f32_16x16x32_bf16 a[16:19], v[56:59], v[132:135], a[16:19]\n"
"s_waitcnt lgkmcnt(0)\n"
"ds_read_b64_tr_b16 v[56:57], v26 offset:0\n"
"v_max3_f32 v10, v10, v151, v152\n"
"v_mfma_f32_16x16x32_bf16 a[20:23], v[60:63], v[116:119], a[20:23]\n"
"ds_read_b64_tr_b16 v[58:59], v26 offset:4096\n"
"v_max3_f32 v10, v10, v153, v154\n"
"v_mfma_f32_16x16x32_bf16 a[24:27], v[60:63], v[120:123], a[24:27]\n"
"buffer_load_dwordx4 v14, s[40:43], s47 offen offset:1024 nt lds\n"
"v_max_f32 v10, v10, v155\n"
"v_mfma_f32_16x16x32_bf16 a[28:31], v[60:63], v[124:127], a[28:31]\n"
"v_cmp_nge_f32 vcc, s62, v10\n"
"v_mfma_f32_16x16x32_bf16 a[32:35], v[60:63], v[128:131], a[32:35]\n"
"v_mfma_f32_16x16x32_bf16 a[36:39], v[60:63], v[132:135], a[36:39]\n"
"ds_read_b64_tr_b16 v[60:61], v27 offset:0\n"
"v_mfma_f32_16x16x32_bf16 a[40:43], v[64:67], v[116:119], a[40:43]\n"
"ds_read_b64_tr_b16 v[62:63], v27 offset:4096\n"
"v_mfma_f32_16x16x32_bf16 a[44:47], v[64:67], v[120:123], a[44:47]\n"
"buffer_load_dwordx4 v15, s[40:43], s47 offen offset:2048 nt lds\n"
"v_mfma_f32_16x16x32_bf16 a[48:51], v[64:67], v[124:127], a[48:51]\n"
"v_mfma_f32_16x16x32_bf16 a[52:55], v[64:67], v[128:131], a[52:55]\n"
"v_mfma_f32_16x16x32_bf16 a[56:59], v[64:67], v[132:135], a[56:59]\n"
"s_cbranch_vccnz R0_3v0_%=\n"
"R0_3v0_r_%=:\n"
"ds_read_b64_tr_b16 v[64:65], v28 offset:0\n"
"v_mfma_f32_16x16x32_bf16 a[60:63], v[68:71], v[116:119], a[60:63]\n"
"ds_read_b64_tr_b16 v[66:67], v28 offset:4096\n"
"v_mfma_f32_16x16x32_bf16 a[64:67], v[68:71], v[120:123], a[64:67]\n"
"buffer_load_dwordx4 v16, s[40:43], s47 offen offset:3072 nt lds\n"
"v_mfma_f32_16x16x32_bf16 a[68:71], v[68:71], v[124:127], a[68:71]\n"
"s_add_u32 s47, s47, 32768\n"
"v_exp_f32 v48, v136\n"
"v_mfma_f32_16x16x32_bf16 a[72:75], v[68:71], v[128:131], a[72:75]\n"
"v_exp_f32 v49, v137\n"
"v_mfma_f32_16x16x32_bf16 a[76:79], v[68:71], v[132:135], a[76:79]\n"
"v_exp_f32 v50, v138\n"
"v_mfma_f32_16x16x32_bf16 a[80:83], v[72:75], v[116:119], a[80:83]\n"
"v_exp_f32 v51, v139\n"
"v_mfma_f32_16x16x32_bf16 a[84:87], v[72:75], v[120:123], a[84:87]\n"
"v_exp_f32 v52, v140\n"
"v_mfma_f32_16x16x32_bf16 a[88:91], v[72:75], v[124:127], a[88:91]\n"
"v_exp_f32 v53, v141\n"
"v_mfma_f32_16x16x32_bf16 a[92:95], v[72:75], v[128:131], a[92:95]\n"
"v_exp_f32 v54, v142\n"
"v_mfma_f32_16x16x32_bf16 a[96:99], v[72:75], v[132:135], a[96:99]\n"
"s_waitcnt vmcnt(16) lgkmcnt(0)\n"
"s_barrier\n"
"v_mfma_f32_16x16x32_bf16 a[100:103], v[56:59], v[116:119], a[100:103]\n"
"ds_read_b128 a[240:243], v29 offset:49152\n"
"v_add_f32 v33, v33, v48\n"
"v_mfma_f32_16x16x32_bf16 a[104:107], v[56:59], v[120:123], a[104:107]\n"
"s_mov_b32 m0, s49\n"
"ds_read_b128 a[244:247], v30 offset:49152\n"
"v_add_f32 v33, v33, v49\n"
"v_mfma_f32_16x16x32_bf16 a[108:111], v[56:59], v[124:127], a[108:111]\n"
"buffer_load_dwordx4 v17, s[36:39], s44 offen nt lds\n"
"v_add_f32 v33, v33, v50\n"
"v_mfma_f32_16x16x32_bf16 a[112:115], v[56:59], v[128:131], a[112:115]\n"
"ds_read_b128 a[248:251], v31 offset:49152\n"
"v_add_f32 v33, v33, v51\n"
"v_mfma_f32_16x16x32_bf16 a[116:119], v[56:59], v[132:135], a[116:119]\n"
"ds_read_b128 a[252:255], v32 offset:49152\n"
"v_add_f32 v34, v34, v52\n"
"v_mfma_f32_16x16x32_bf16 a[120:123], v[60:63], v[116:119], a[120:123]\n"
"ds_read_b128 v[68:71], v31 offset:49408\n"
"v_add_f32 v34, v34, v53\n"
"v_mfma_f32_16x16x32_bf16 a[124:127], v[60:63], v[120:123], a[124:127]\n"
"ds_read_b128 v[72:75], v32 offset:49408\n"
"v_add_f32 v34, v34, v54\n"
"v_mfma_f32_16x16x32_bf16 a[128:131], v[60:63], v[124:127], a[128:131]\n"
"buffer_load_dwordx4 v18, s[36:39], s44 offen offset:1024 nt lds\n"
"v_mfma_f32_16x16x32_bf16 a[132:135], v[60:63], v[128:131], a[132:135]\n"
"v_exp_f32 v55, v143\n"
"v_mfma_f32_16x16x32_bf16 a[136:139], v[60:63], v[132:135], a[136:139]\n"
"v_add_f32 v34, v34, v55\n"
"v_mfma_f32_16x16x32_bf16 a[140:143], v[64:67], v[116:119], a[140:143]\n"
"ds_read_b128 v[60:63], v29 offset:49408\n"
"v_cvt_pk_bf16_f32 v116, v48, v49\n"
"v_mfma_f32_16x16x32_bf16 a[144:147], v[64:67], v[120:123], a[144:147]\n"
"v_exp_f32 v48, v144\n"
"v_mfma_f32_16x16x32_bf16 a[148:151], v[64:67], v[124:127], a[148:151]\n"
"buffer_load_dwordx4 v19, s[36:39], s44 offen offset:2048 nt lds\n"
"v_cvt_pk_bf16_f32 v117, v50, v51\n"
"v_mfma_f32_16x16x32_bf16 a[152:155], v[64:67], v[128:131], a[152:155]\n"
"ds_read_b32 v9, v7 offset:1280\n"
"v_cvt_pk_bf16_f32 v120, v52, v53\n"
"v_mfma_f32_16x16x32_bf16 a[156:159], v[64:67], v[132:135], a[156:159]\n"
"s_waitcnt lgkmcnt(2)\n"
"ds_read_b128 v[64:67], v30 offset:49408\n"
"v_cvt_pk_bf16_f32 v121, v54, v55\n"
"v_mfma_f32_16x16x32_bf16 v[156:159], a[240:243], v[176:179], v[76:79]\n"
"ds_read_b128 v[96:99], v11 offset:0\n"
"v_add_f32 v35, v35, v48\n"
"v_mfma_f32_16x16x32_bf16 v[156:159], a[244:247], v[196:199], v[156:159]\n"
"ds_read_b128 v[100:103], v11 offset:1024\n"
"v_mfma_f32_16x16x32_bf16 v[156:159], a[248:251], v[216:219], v[156:159]\n"
"ds_read_b128 v[104:107], v11 offset:2048\n"
"v_mfma_f32_16x16x32_bf16 v[156:159], a[252:255], v[236:239], v[156:159]\n"
"buffer_load_dwordx4 v20, s[36:39], s44 offen offset:3072 nt lds\n"
"v_mfma_f32_16x16x32_bf16 v[156:159], v[68:71], a[200:203], v[156:159]\n"
"s_add_u32 s44, s44, 32768\n"
"ds_read_b64_tr_b16 v[56:57], v21 offset:32768\n"
"v_mfma_f32_16x16x32_bf16 v[156:159], v[72:75], a[220:223], v[156:159]\n"
"s_waitcnt lgkmcnt(4)\n"
"ds_read_b64_tr_b16 v[58:59], v21 offset:36864\n"
"v_mfma_f32_16x16x32_bf16 v[156:159], v[60:63], a[160:163], v[156:159]\n"
"ds_read_b128 v[108:111], v11 offset:3072\n"
"v_mfma_f32_16x16x32_bf16 v[156:159], v[64:67], a[180:183], v[156:159]\n"
"ds_read_b128 v[112:115], v11 offset:4096\n"
"v_mfma_f32_16x16x32_bf16 v[160:163], a[240:243], v[180:183], v[80:83]\n"
"v_exp_f32 v49, v145\n"
"v_mfma_f32_16x16x32_bf16 v[160:163], a[244:247], v[200:203], v[160:163]\n"
"v_exp_f32 v50, v146\n"
"v_mfma_f32_16x16x32_bf16 v[160:163], a[248:251], v[220:223], v[160:163]\n"
"s_mov_b32 m0, s52\n"
"v_readfirstlane_b32 s59, v9\n"
"v_cvt_pk_bf16_f32 v124, v48, v49\n"
"v_mfma_f32_16x16x32_bf16 v[160:163], a[252:255], v[240:243], v[160:163]\n"
"buffer_load_dwordx4 v13, s[40:43], s46 offen nt lds\n"
"v_add_f32 v35, v35, v49\n"
"v_mfma_f32_16x16x32_bf16 v[160:163], v[60:63], a[164:167], v[160:163]\n"
"s_cmp_lg_u32 s59, s58\n"
"v_exp_f32 v51, v147\n"
"v_mfma_f32_16x16x32_bf16 v[160:163], v[64:67], a[184:187], v[160:163]\n"
"s_cselect_b32 s60, 3, 0\n"
"v_exp_f32 v52, v148\n"
"v_mfma_f32_16x16x32_bf16 v[160:163], v[68:71], a[204:207], v[160:163]\n"
"v_exp_f32 v53, v149\n"
"v_mfma_f32_16x16x32_bf16 v[160:163], v[72:75], a[224:227], v[160:163]\n"
"s_max_u32 s56, s56, s60\n"
"v_exp_f32 v54, v150\n"
"v_mfma_f32_16x16x32_bf16 v[164:167], a[240:243], v[184:187], v[84:87]\n"
"s_mov_b32 s58, s59\n"
"v_exp_f32 v55, v151\n"
"v_mfma_f32_16x16x32_bf16 v[164:167], a[244:247], v[204:207], v[164:167]\n"
"v_exp_f32 v48, v152\n"
"v_mfma_f32_16x16x32_bf16 v[164:167], a[248:251], v[224:227], v[164:167]\n"
"buffer_load_dwordx4 v14, s[40:43], s46 offen offset:1024 nt lds\n"
"v_cvt_pk_bf16_f32 v125, v50, v51\n"
"v_mfma_f32_16x16x32_bf16 v[164:167], a[252:255], v[244:247], v[164:167]\n"
"v_exp_f32 v49, v153\n"
"v_mfma_f32_16x16x32_bf16 v[164:167], v[60:63], a[168:171], v[164:167]\n"
"v_cvt_pk_bf16_f32 v128, v52, v53\n"
"v_cvt_pk_bf16_f32 v129, v54, v55\n"
"v_mfma_f32_16x16x32_bf16 v[164:167], v[64:67], a[188:191], v[164:167]\n"
"v_cvt_pk_bf16_f32 v132, v48, v49\n"
"v_add_f32 v35, v35, v50\n"
"v_mfma_f32_16x16x32_bf16 v[164:167], v[68:71], a[208:211], v[164:167]\n"
"v_exp_f32 v50, v154\n"
"v_mfma_f32_16x16x32_bf16 v[164:167], v[72:75], a[228:231], v[164:167]\n"
"v_add_f32 v35, v35, v51\n"
"v_add_f32 v36, v36, v52\n"
"v_mfma_f32_16x16x32_bf16 v[168:171], a[240:243], v[188:191], v[88:91]\n"
"buffer_load_dwordx4 v15, s[40:43], s46 offen offset:2048 nt lds\n"
"v_add_f32 v36, v36, v53\n"
"v_mfma_f32_16x16x32_bf16 v[168:171], a[244:247], v[208:211], v[168:171]\n"
"v_exp_f32 v51, v155\n"
"v_mfma_f32_16x16x32_bf16 v[168:171], a[248:251], v[228:231], v[168:171]\n"
"v_cvt_pk_bf16_f32 v133, v50, v51\n"
"v_add_f32 v36, v36, v54\n"
"v_mfma_f32_16x16x32_bf16 v[168:171], a[252:255], v[248:251], v[168:171]\n"
"v_add_f32 v36, v36, v55\n"
"v_add_f32 v37, v37, v48\n"
"v_mfma_f32_16x16x32_bf16 v[168:171], v[60:63], a[172:175], v[168:171]\n"
"v_add_f32 v37, v37, v49\n"
"v_add_f32 v37, v37, v50\n"
"v_mfma_f32_16x16x32_bf16 v[168:171], v[64:67], a[192:195], v[168:171]\n"
"v_add_f32 v37, v37, v51\n"
"v_mfma_f32_16x16x32_bf16 v[168:171], v[68:71], a[212:215], v[168:171]\n"
"buffer_load_dwordx4 v16, s[40:43], s46 offen offset:3072 nt lds\n"
"v_mfma_f32_16x16x32_bf16 v[168:171], v[72:75], a[232:235], v[168:171]\n"
"s_add_u32 s46, s46, 32768\n"
"v_mfma_f32_16x16x32_bf16 v[172:175], v[60:63], a[176:179], v[92:95]\n"
"ds_read_b64_tr_b16 v[60:61], v22 offset:32768\n"
"v_mfma_f32_16x16x32_bf16 v[172:175], v[64:67], a[196:199], v[172:175]\n"
"ds_read_b64_tr_b16 v[62:63], v22 offset:36864\n"
"v_mfma_f32_16x16x32_bf16 v[172:175], v[68:71], a[216:219], v[172:175]\n"
"ds_read_b64_tr_b16 v[64:65], v23 offset:32768\n"
"v_mfma_f32_16x16x32_bf16 v[172:175], v[72:75], a[236:239], v[172:175]\n"
"ds_read_b64_tr_b16 v[66:67], v23 offset:36864\n"
"v_mfma_f32_16x16x32_bf16 v[172:175], a[240:243], v[192:195], v[172:175]\n"
"ds_read_b64_tr_b16 v[68:69], v24 offset:32768\n"
"v_mfma_f32_16x16x32_bf16 v[172:175], a[244:247], v[212:215], v[172:175]\n"
"ds_read_b64_tr_b16 v[70:71], v24 offset:36864\n"
"v_mfma_f32_16x16x32_bf16 v[172:175], a[248:251], v[232:235], v[172:175]\n"
"s_cmp_lg_u32 s56, 0\n"
"ds_read_b64_tr_b16 v[72:73], v25 offset:32768\n"
"v_mfma_f32_16x16x32_bf16 v[172:175], a[252:255], v[252:255], v[172:175]\n"
"s_waitcnt lgkmcnt(6)\n"
"s_cbranch_scc1 S3_3v0_%=\n"
"S3_3v0_r_%=:\n"
"ds_read_b64_tr_b16 v[74:75], v25 offset:36864\n"
"v_mfma_f32_16x16x32_bf16 a[0:3], v[56:59], v[96:99], a[0:3]\n"
"v_max3_f32 v10, v156, v157, v158\n"
"v_max3_f32 v10, v10, v159, v160\n"
"v_mfma_f32_16x16x32_bf16 a[4:7], v[56:59], v[100:103], a[4:7]\n"
"v_max3_f32 v10, v10, v161, v162\n"
"v_max3_f32 v10, v10, v163, v164\n"
"v_mfma_f32_16x16x32_bf16 a[8:11], v[56:59], v[104:107], a[8:11]\n"
"v_max3_f32 v10, v10, v165, v166\n"
"v_max3_f32 v10, v10, v167, v168\n"
"v_mfma_f32_16x16x32_bf16 a[12:15], v[56:59], v[108:111], a[12:15]\n"
"v_max3_f32 v10, v10, v169, v170\n"
"v_max3_f32 v10, v10, v171, v172\n"
"v_mfma_f32_16x16x32_bf16 a[16:19], v[56:59], v[112:115], a[16:19]\n"
"s_waitcnt lgkmcnt(0)\n"
"ds_read_b64_tr_b16 v[56:57], v26 offset:32768\n"
"v_max3_f32 v10, v10, v173, v174\n"
"v_mfma_f32_16x16x32_bf16 a[20:23], v[60:63], v[96:99], a[20:23]\n"
"ds_read_b64_tr_b16 v[58:59], v26 offset:36864\n"
"v_max_f32 v10, v10, v175\n"
"v_mfma_f32_16x16x32_bf16 a[24:27], v[60:63], v[100:103], a[24:27]\n"
"v_cmp_nge_f32 vcc, s62, v10\n"
"v_mfma_f32_16x16x32_bf16 a[28:31], v[60:63], v[104:107], a[28:31]\n"
"v_mfma_f32_16x16x32_bf16 a[32:35], v[60:63], v[108:111], a[32:35]\n"
"v_mfma_f32_16x16x32_bf16 a[36:39], v[60:63], v[112:115], a[36:39]\n"
"s_cbranch_vccnz R1_3v0_%=\n"
"R1_3v0_r_%=:\n"
"ds_read_b64_tr_b16 v[60:61], v27 offset:32768\n"
"v_mfma_f32_16x16x32_bf16 a[40:43], v[64:67], v[96:99], a[40:43]\n"
"ds_read_b64_tr_b16 v[62:63], v27 offset:36864\n"
"v_mfma_f32_16x16x32_bf16 a[44:47], v[64:67], v[100:103], a[44:47]\n"
"v_exp_f32 v52, v156\n"
"v_mfma_f32_16x16x32_bf16 a[48:51], v[64:67], v[104:107], a[48:51]\n"
"v_exp_f32 v53, v157\n"
"v_mfma_f32_16x16x32_bf16 a[52:55], v[64:67], v[108:111], a[52:55]\n"
"v_exp_f32 v54, v158\n"
"v_mfma_f32_16x16x32_bf16 a[56:59], v[64:67], v[112:115], a[56:59]\n"
"ds_read_b64_tr_b16 v[64:65], v28 offset:32768\n"
"v_cvt_pk_bf16_f32 v118, v52, v53\n"
"v_mfma_f32_16x16x32_bf16 a[60:63], v[68:71], v[96:99], a[60:63]\n"
"ds_read_b64_tr_b16 v[66:67], v28 offset:36864\n"
"v_add_f32 v33, v33, v52\n"
"v_mfma_f32_16x16x32_bf16 a[64:67], v[68:71], v[100:103], a[64:67]\n"
"v_exp_f32 v55, v159\n"
"v_mfma_f32_16x16x32_bf16 a[68:71], v[68:71], v[104:107], a[68:71]\n"
"v_exp_f32 v48, v160\n"
"v_mfma_f32_16x16x32_bf16 a[72:75], v[68:71], v[108:111], a[72:75]\n"
"v_exp_f32 v49, v161\n"
"v_mfma_f32_16x16x32_bf16 a[76:79], v[68:71], v[112:115], a[76:79]\n"
"v_exp_f32 v50, v162\n"
"v_mfma_f32_16x16x32_bf16 a[80:83], v[72:75], v[96:99], a[80:83]\n"
"v_exp_f32 v51, v163\n"
"v_mfma_f32_16x16x32_bf16 a[84:87], v[72:75], v[100:103], a[84:87]\n"
"v_exp_f32 v52, v164\n"
"v_mfma_f32_16x16x32_bf16 a[88:91], v[72:75], v[104:107], a[88:91]\n"
"v_cvt_pk_bf16_f32 v119, v54, v55\n"
"v_cvt_pk_bf16_f32 v122, v48, v49\n"
"v_mfma_f32_16x16x32_bf16 a[92:95], v[72:75], v[108:111], a[92:95]\n"
"v_cvt_pk_bf16_f32 v123, v50, v51\n"
"v_add_f32 v33, v33, v53\n"
"v_mfma_f32_16x16x32_bf16 a[96:99], v[72:75], v[112:115], a[96:99]\n"
"s_waitcnt vmcnt(16) lgkmcnt(0)\n"
"s_barrier\n"
"v_mfma_f32_16x16x32_bf16 a[100:103], v[56:59], v[96:99], a[100:103]\n"
"ds_read_b128 a[240:243], v29 offset:0\n"
"v_add_f32 v33, v33, v54\n"
"v_mfma_f32_16x16x32_bf16 a[104:107], v[56:59], v[100:103], a[104:107]\n"
"ds_read_b128 a[244:247], v30 offset:0\n"
"v_add_f32 v33, v33, v55\n"
"v_mfma_f32_16x16x32_bf16 a[108:111], v[56:59], v[104:107], a[108:111]\n"
"ds_read_b128 a[248:251], v31 offset:0\n"
"v_add_f32 v34, v34, v48\n"
"v_mfma_f32_16x16x32_bf16 a[112:115], v[56:59], v[108:111], a[112:115]\n"
"ds_read_b128 a[252:255], v32 offset:0\n"
"v_add_f32 v34, v34, v49\n"
"v_mfma_f32_16x16x32_bf16 a[116:119], v[56:59], v[112:115], a[116:119]\n"
"ds_read_b128 v[68:71], v31 offset:256\n"
"v_add_f32 v34, v34, v50\n"
"v_mfma_f32_16x16x32_bf16 a[120:123], v[60:63], v[96:99], a[120:123]\n"
"ds_read_b128 v[72:75], v32 offset:256\n"
"v_add_f32 v34, v34, v51\n"
"v_mfma_f32_16x16x32_bf16 a[124:127], v[60:63], v[100:103], a[124:127]\n"
"v_exp_f32 v53, v165\n"
"v_mfma_f32_16x16x32_bf16 a[128:131], v[60:63], v[104:107], a[128:131]\n"
"v_exp_f32 v54, v166\n"
"v_mfma_f32_16x16x32_bf16 a[132:135], v[60:63], v[108:111], a[132:135]\n"
"v_exp_f32 v55, v167\n"
"v_mfma_f32_16x16x32_bf16 a[136:139], v[60:63], v[112:115], a[136:139]\n"
"v_exp_f32 v48, v168\n"
"v_mfma_f32_16x16x32_bf16 a[140:143], v[64:67], v[96:99], a[140:143]\n"
"ds_read_b128 v[60:63], v29 offset:256\n"
"v_cvt_pk_bf16_f32 v126, v52, v53\n"
"v_mfma_f32_16x16x32_bf16 a[144:147], v[64:67], v[100:103], a[144:147]\n"
"v_exp_f32 v49, v169\n"
"v_mfma_f32_16x16x32_bf16 a[148:151], v[64:67], v[104:107], a[148:151]\n"
"v_exp_f32 v50, v170\n"
"v_mfma_f32_16x16x32_bf16 a[152:155], v[64:67], v[108:111], a[152:155]\n"
"s_cmp_lg_u32 s56, 0\n"
"v_exp_f32 v51, v171\n"
"v_mfma_f32_16x16x32_bf16 a[156:159], v[64:67], v[112:115], a[156:159]\n"
"s_cbranch_scc1 S1_3v0_%=\n"
"S1_3v0_r_%=:\n"
"ds_read_b128 v[64:67], v30 offset:256\n"
"s_sub_u32 s61, s61, 1\n"
"s_cmp_lg_u32 s61, 0\n"
"s_cbranch_scc1 LOOP_%=\n"
"LOOPX_%=:\n"
"s_waitcnt lgkmcnt(0)\n"
"v_mfma_f32_16x16x32_bf16 v[136:139], a[240:243], v[176:179], v[76:79]\n"
"s_mov_b32 m0, s51\n"
"v_cvt_pk_bf16_f32 v127, v54, v55\n"
"v_cvt_pk_bf16_f32 v130, v48, v49\n"
"v_mfma_f32_16x16x32_bf16 v[136:139], a[244:247], v[196:199], v[136:139]\n"
"buffer_load_dwordx4 v17, s[36:39], s45 offen nt lds\n"
"v_cvt_pk_bf16_f32 v131, v50, v51\n"
"v_mfma_f32_16x16x32_bf16 v[136:139], a[248:251], v[216:219], v[136:139]\n"
"v_add_f32 v35, v35, v52\n"
"v_add_f32 v35, v35, v53\n"
"v_mfma_f32_16x16x32_bf16 v[136:139], a[252:255], v[236:239], v[136:139]\n"
"v_exp_f32 v52, v172\n"
"v_mfma_f32_16x16x32_bf16 v[136:139], v[68:71], a[200:203], v[136:139]\n"
"ds_read_b64_tr_b16 v[56:57], v21 offset:16384\n"
"v_add_f32 v35, v35, v54\n"
"v_mfma_f32_16x16x32_bf16 v[136:139], v[72:75], a[220:223], v[136:139]\n"
"ds_read_b64_tr_b16 v[58:59], v21 offset:20480\n"
"v_add_f32 v35, v35, v55\n"
"v_mfma_f32_16x16x32_bf16 v[136:139], v[60:63], a[160:163], v[136:139]\n"
"buffer_load_dwordx4 v18, s[36:39], s45 offen offset:1024 nt lds\n"
"v_add_f32 v36, v36, v48\n"
"v_mfma_f32_16x16x32_bf16 v[136:139], v[64:67], a[180:183], v[136:139]\n"
"v_exp_f32 v53, v173\n"
"v_mfma_f32_16x16x32_bf16 v[140:143], a[240:243], v[180:183], v[80:83]\n"
"v_exp_f32 v54, v174\n"
"v_mfma_f32_16x16x32_bf16 v[140:143], a[244:247], v[200:203], v[140:143]\n"
"v_exp_f32 v55, v175\n"
"v_mfma_f32_16x16x32_bf16 v[140:143], a[248:251], v[220:223], v[140:143]\n"
"v_cvt_pk_bf16_f32 v134, v52, v53\n"
"v_cvt_pk_bf16_f32 v135, v54, v55\n"
"v_mfma_f32_16x16x32_bf16 v[140:143], a[252:255], v[240:243], v[140:143]\n"
"buffer_load_dwordx4 v19, s[36:39], s45 offen offset:2048 nt lds\n"
"v_add_f32 v36, v36, v49\n"
"v_mfma_f32_16x16x32_bf16 v[140:143], v[60:63], a[164:167], v[140:143]\n"
"v_add_f32 v36, v36, v50\n"
"v_add_f32 v36, v36, v51\n"
"v_mfma_f32_16x16x32_bf16 v[140:143], v[64:67], a[184:187], v[140:143]\n"
"v_add_f32 v37, v37, v52\n"
"v_add_f32 v37, v37, v53\n"
"v_mfma_f32_16x16x32_bf16 v[140:143], v[68:71], a[204:207], v[140:143]\n"
"v_add_f32 v37, v37, v54\n"
"v_add_f32 v37, v37, v55\n"
"v_mfma_f32_16x16x32_bf16 v[140:143], v[72:75], a[224:227], v[140:143]\n"
"v_mfma_f32_16x16x32_bf16 v[144:147], a[240:243], v[184:187], v[84:87]\n"
"buffer_load_dwordx4 v20, s[36:39], s45 offen offset:3072 nt lds\n"
"v_mfma_f32_16x16x32_bf16 v[144:147], a[244:247], v[204:207], v[144:147]\n"
"s_add_u32 s45, s45, 32768\n"
"v_mfma_f32_16x16x32_bf16 v[144:147], a[248:251], v[224:227], v[144:147]\n"
"v_mfma_f32_16x16x32_bf16 v[144:147], a[252:255], v[244:247], v[144:147]\n"
"v_mfma_f32_16x16x32_bf16 v[144:147], v[60:63], a[168:171], v[144:147]\n"
"ds_write_b128 v12, v[116:119] offset:0\n"
"v_mfma_f32_16x16x32_bf16 v[144:147], v[64:67], a[188:191], v[144:147]\n"
"v_mfma_f32_16x16x32_bf16 v[144:147], v[68:71], a[208:211], v[144:147]\n"
"ds_write_b128 v12, v[120:123] offset:1024\n"
"v_mfma_f32_16x16x32_bf16 v[144:147], v[72:75], a[228:231], v[144:147]\n"
"v_mfma_f32_16x16x32_bf16 v[148:151], a[240:243], v[188:191], v[88:91]\n"
"ds_write_b128 v12, v[124:127] offset:2048\n"
"v_mfma_f32_16x16x32_bf16 v[148:151], a[244:247], v[208:211], v[148:151]\n"
"v_mfma_f32_16x16x32_bf16 v[148:151], a[248:251], v[228:231], v[148:151]\n"
"v_mfma_f32_16x16x32_bf16 v[148:151], a[252:255], v[248:251], v[148:151]\n"
"ds_write_b128 v12, v[128:131] offset:3072\n"
"v_mfma_f32_16x16x32_bf16 v[148:151], v[60:63], a[172:175], v[148:151]\n"
"v_mfma_f32_16x16x32_bf16 v[148:151], v[64:67], a[192:195], v[148:151]\n"
"ds_write_b128 v12, v[132:135] offset:4096\n"
"v_mfma_f32_16x16x32_bf16 v[148:151], v[68:71], a[212:215], v[148:151]\n"
"v_mfma_f32_16x16x32_bf16 v[148:151], v[72:75], a[232:235], v[148:151]\n"
"v_mfma_f32_16x16x32_bf16 v[152:155], v[60:63], a[176:179], v[92:95]\n"
"ds_read_b64_tr_b16 v[60:61], v22 offset:16384\n"
"v_mfma_f32_16x16x32_bf16 v[152:155], v[64:67], a[196:199], v[152:155]\n"
"ds_read_b64_tr_b16 v[62:63], v22 offset:20480\n"
"v_mfma_f32_16x16x32_bf16 v[152:155], v[68:71], a[216:219], v[152:155]\n"
"ds_read_b64_tr_b16 v[64:65], v23 offset:16384\n"
"v_mfma_f32_16x16x32_bf16 v[152:155], v[72:75], a[236:239], v[152:155]\n"
"ds_read_b64_tr_b16 v[66:67], v23 offset:20480\n"
"v_mfma_f32_16x16x32_bf16 v[152:155], a[240:243], v[192:195], v[152:155]\n"
"ds_read_b64_tr_b16 v[68:69], v24 offset:16384\n"
"v_mfma_f32_16x16x32_bf16 v[152:155], a[244:247], v[212:215], v[152:155]\n"
"ds_read_b64_tr_b16 v[70:71], v24 offset:20480\n"
"v_mfma_f32_16x16x32_bf16 v[152:155], a[248:251], v[232:235], v[152:155]\n"
"s_cmp_lg_u32 s56, 0\n"
"ds_read_b64_tr_b16 v[72:73], v25 offset:16384\n"
"v_mfma_f32_16x16x32_bf16 v[152:155], a[252:255], v[252:255], v[152:155]\n"
"s_waitcnt lgkmcnt(6)\n"
"s_cbranch_scc1 S2_14_%=\n"
"S2_14_r_%=:\n"
"ds_read_b64_tr_b16 v[74:75], v25 offset:20480\n"
"v_mfma_f32_16x16x32_bf16 a[0:3], v[56:59], v[116:119], a[0:3]\n"
"s_mov_b32 m0, s54\n"
"v_max3_f32 v10, v136, v137, v138\n"
"v_max3_f32 v10, v10, v139, v140\n"
"v_mfma_f32_16x16x32_bf16 a[4:7], v[56:59], v[120:123], a[4:7]\n"
"buffer_load_dwordx4 v13, s[40:43], s47 offen nt lds\n"
"v_max3_f32 v10, v10, v141, v142\n"
"v_mfma_f32_16x16x32_bf16 a[8:11], v[56:59], v[124:127], a[8:11]\n"
"v_max3_f32 v10, v10, v143, v144\n"
"v_max3_f32 v10, v10, v145, v146\n"
"v_mfma_f32_16x16x32_bf16 a[12:15], v[56:59], v[128:131], a[12:15]\n"
"v_max3_f32 v10, v10, v147, v148\n"
"v_max3_f32 v10, v10, v149, v150\n"
"v_mfma_f32_16x16x32_bf16 a[16:19], v[56:59], v[132:135], a[16:19]\n"
"s_waitcnt lgkmcnt(0)\n"
"ds_read_b64_tr_b16 v[56:57], v26 offset:16384\n"
"v_max3_f32 v10, v10, v151, v152\n"
"v_mfma_f32_16x16x32_bf16 a[20:23], v[60:63], v[116:119], a[20:23]\n"
"ds_read_b64_tr_b16 v[58:59], v26 offset:20480\n"
"v_max3_f32 v10, v10, v153, v154\n"
"v_mfma_f32_16x16x32_bf16 a[24:27], v[60:63], v[120:123], a[24:27]\n"
"s_mov_b64 s[64:65], s[20:21]\n"
"s_mul_i32 s60, s70, 3584\n"
"s_add_u32 s60, s60, s97\n"
"s_add_u32 s64, s64, s60\n"
"s_addc_u32 s65, s65, 0\n"
"v_min_u32 v6, 27, v0\n"
"buffer_load_dwordx4 v14, s[40:43], s47 offen offset:1024 nt lds\n"
"v_max_f32 v10, v10, v155\n"
"v_mfma_f32_16x16x32_bf16 a[28:31], v[60:63], v[124:127], a[28:31]\n"
"v_lshlrev_b32 v6, 7, v6\n"
"v_cmp_nge_f32 vcc, s62, v10\n"
"v_mfma_f32_16x16x32_bf16 a[32:35], v[60:63], v[128:131], a[32:35]\n"
"global_load_ubyte v6, v6, s[64:65]\n"
"v_mfma_f32_16x16x32_bf16 a[36:39], v[60:63], v[132:135], a[36:39]\n"
"ds_read_b64_tr_b16 v[60:61], v27 offset:16384\n"
"v_mfma_f32_16x16x32_bf16 a[40:43], v[64:67], v[116:119], a[40:43]\n"
"ds_read_b64_tr_b16 v[62:63], v27 offset:20480\n"
"v_mfma_f32_16x16x32_bf16 a[44:47], v[64:67], v[120:123], a[44:47]\n"
"buffer_load_dwordx4 v15, s[40:43], s47 offen offset:2048 nt lds\n"
"v_mfma_f32_16x16x32_bf16 a[48:51], v[64:67], v[124:127], a[48:51]\n"
"v_mfma_f32_16x16x32_bf16 a[52:55], v[64:67], v[128:131], a[52:55]\n"
"v_mfma_f32_16x16x32_bf16 a[56:59], v[64:67], v[132:135], a[56:59]\n"
"s_cbranch_vccnz R0_14_%=\n"
"R0_14_r_%=:\n"
"ds_read_b64_tr_b16 v[64:65], v28 offset:16384\n"
"v_mfma_f32_16x16x32_bf16 a[60:63], v[68:71], v[116:119], a[60:63]\n"
"ds_read_b64_tr_b16 v[66:67], v28 offset:20480\n"
"v_mfma_f32_16x16x32_bf16 a[64:67], v[68:71], v[120:123], a[64:67]\n"
"buffer_load_dwordx4 v16, s[40:43], s47 offen offset:3072 nt lds\n"
"v_mfma_f32_16x16x32_bf16 a[68:71], v[68:71], v[124:127], a[68:71]\n"
"s_add_u32 s47, s47, 32768\n"
"v_exp_f32 v48, v136\n"
"v_mfma_f32_16x16x32_bf16 a[72:75], v[68:71], v[128:131], a[72:75]\n"
"v_exp_f32 v49, v137\n"
"v_mfma_f32_16x16x32_bf16 a[76:79], v[68:71], v[132:135], a[76:79]\n"
"v_exp_f32 v50, v138\n"
"v_mfma_f32_16x16x32_bf16 a[80:83], v[72:75], v[116:119], a[80:83]\n"
"v_exp_f32 v51, v139\n"
"v_mfma_f32_16x16x32_bf16 a[84:87], v[72:75], v[120:123], a[84:87]\n"
"v_exp_f32 v52, v140\n"
"v_mfma_f32_16x16x32_bf16 a[88:91], v[72:75], v[124:127], a[88:91]\n"
"v_exp_f32 v53, v141\n"
"v_mfma_f32_16x16x32_bf16 a[92:95], v[72:75], v[128:131], a[92:95]\n"
"v_exp_f32 v54, v142\n"
"v_mfma_f32_16x16x32_bf16 a[96:99], v[72:75], v[132:135], a[96:99]\n"
"s_waitcnt vmcnt(17) lgkmcnt(0)\n"
"s_barrier\n"
"v_mfma_f32_16x16x32_bf16 a[100:103], v[56:59], v[116:119], a[100:103]\n"
"ds_read_b128 a[240:243], v29 offset:16384\n"
"v_add_f32 v33, v33, v48\n"
"v_mfma_f32_16x16x32_bf16 a[104:107], v[56:59], v[120:123], a[104:107]\n"
"s_mov_b32 m0, s53\n"
"ds_read_b128 a[244:247], v30 offset:16384\n"
"v_add_f32 v33, v33, v49\n"
"v_mfma_f32_16x16x32_bf16 a[108:111], v[56:59], v[124:127], a[108:111]\n"
"buffer_load_dwordx4 v13, s[40:43], s46 offen nt lds\n"
"v_add_f32 v33, v33, v50\n"
"v_mfma_f32_16x16x32_bf16 a[112:115], v[56:59], v[128:131], a[112:115]\n"
"ds_read_b128 a[248:251], v31 offset:16384\n"
"v_add_f32 v33, v33, v51\n"
"v_mfma_f32_16x16x32_bf16 a[116:119], v[56:59], v[132:135], a[116:119]\n"
"ds_read_b128 a[252:255], v32 offset:16384\n"
"v_add_f32 v34, v34, v52\n"
"v_mfma_f32_16x16x32_bf16 a[120:123], v[60:63], v[116:119], a[120:123]\n"
"ds_read_b128 v[68:71], v31 offset:16640\n"
"v_add_f32 v34, v34, v53\n"
"v_mfma_f32_16x16x32_bf16 a[124:127], v[60:63], v[120:123], a[124:127]\n"
"ds_read_b128 v[72:75], v32 offset:16640\n"
"v_add_f32 v34, v34, v54\n"
"v_mfma_f32_16x16x32_bf16 a[128:131], v[60:63], v[124:127], a[128:131]\n"
"buffer_load_dwordx4 v14, s[40:43], s46 offen offset:1024 nt lds\n"
"v_mfma_f32_16x16x32_bf16 a[132:135], v[60:63], v[128:131], a[132:135]\n"
"v_exp_f32 v55, v143\n"
"v_mfma_f32_16x16x32_bf16 a[136:139], v[60:63], v[132:135], a[136:139]\n"
"v_add_f32 v34, v34, v55\n"
"v_mfma_f32_16x16x32_bf16 a[140:143], v[64:67], v[116:119], a[140:143]\n"
"ds_read_b128 v[60:63], v29 offset:16640\n"
"v_cvt_pk_bf16_f32 v116, v48, v49\n"
"v_mfma_f32_16x16x32_bf16 a[144:147], v[64:67], v[120:123], a[144:147]\n"
"v_exp_f32 v48, v144\n"
"v_mfma_f32_16x16x32_bf16 a[148:151], v[64:67], v[124:127], a[148:151]\n"
"buffer_load_dwordx4 v15, s[40:43], s46 offen offset:2048 nt lds\n"
"v_cvt_pk_bf16_f32 v117, v50, v51\n"
"v_mfma_f32_16x16x32_bf16 a[152:155], v[64:67], v[128:131], a[152:155]\n"
"ds_read_b32 v9, v7 offset:1280\n"
"v_cvt_pk_bf16_f32 v120, v52, v53\n"
"v_mfma_f32_16x16x32_bf16 a[156:159], v[64:67], v[132:135], a[156:159]\n"
"s_waitcnt lgkmcnt(2)\n"
"ds_read_b128 v[64:67], v30 offset:16640\n"
"v_cvt_pk_bf16_f32 v121, v54, v55\n"
"v_mfma_f32_16x16x32_bf16 v[156:159], a[240:243], v[176:179], v[76:79]\n"
"ds_read_b128 v[96:99], v11 offset:0\n"
"v_add_f32 v35, v35, v48\n"
"v_mfma_f32_16x16x32_bf16 v[156:159], a[244:247], v[196:199], v[156:159]\n"
"ds_read_b128 v[100:103], v11 offset:1024\n"
"v_mfma_f32_16x16x32_bf16 v[156:159], a[248:251], v[216:219], v[156:159]\n"
"ds_read_b128 v[104:107], v11 offset:2048\n"
"v_mfma_f32_16x16x32_bf16 v[156:159], a[252:255], v[236:239], v[156:159]\n"
"buffer_load_dwordx4 v16, s[40:43], s46 offen offset:3072 nt lds\n"
"v_mfma_f32_16x16x32_bf16 v[156:159], v[68:71], a[200:203], v[156:159]\n"
"s_add_u32 s46, s46, 32768\n"
"ds_read_b64_tr_b16 v[56:57], v21 offset:49152\n"
"v_mfma_f32_16x16x32_bf16 v[156:159], v[72:75], a[220:223], v[156:159]\n"
"s_waitcnt lgkmcnt(4)\n"
"ds_read_b64_tr_b16 v[58:59], v21 offset:53248\n"
"v_mfma_f32_16x16x32_bf16 v[156:159], v[60:63], a[160:163], v[156:159]\n"
"ds_read_b128 v[108:111], v11 offset:3072\n"
"v_mfma_f32_16x16x32_bf16 v[156:159], v[64:67], a[180:183], v[156:159]\n"
"ds_read_b128 v[112:115], v11 offset:4096\n"
"v_mfma_f32_16x16x32_bf16 v[160:163], a[240:243], v[180:183], v[80:83]\n"
"v_exp_f32 v49, v145\n"
"v_mfma_f32_16x16x32_bf16 v[160:163], a[244:247], v[200:203], v[160:163]\n"
"v_exp_f32 v50, v146\n"
"v_mfma_f32_16x16x32_bf16 v[160:163], a[248:251], v[220:223], v[160:163]\n"
"v_readfirstlane_b32 s59, v9\n"
"v_cvt_pk_bf16_f32 v124, v48, v49\n"
"v_mfma_f32_16x16x32_bf16 v[160:163], a[252:255], v[240:243], v[160:163]\n"
"v_exp_f32 v51, v147\n"
"v_mfma_f32_16x16x32_bf16 v[160:163], v[60:63], a[164:167], v[160:163]\n"
"s_cmp_lg_u32 s59, s58\n"
"v_exp_f32 v52, v148\n"
"v_mfma_f32_16x16x32_bf16 v[160:163], v[64:67], a[184:187], v[160:163]\n"
"s_cselect_b32 s60, 3, 0\n"
"v_exp_f32 v53, v149\n"
"v_mfma_f32_16x16x32_bf16 v[160:163], v[68:71], a[204:207], v[160:163]\n"
"v_exp_f32 v54, v150\n"
"v_mfma_f32_16x16x32_bf16 v[160:163], v[72:75], a[224:227], v[160:163]\n"
"s_max_u32 s56, s56, s60\n"
"v_exp_f32 v55, v151\n"
"v_mfma_f32_16x16x32_bf16 v[164:167], a[240:243], v[184:187], v[84:87]\n"
"s_mov_b32 s58, s59\n"
"v_exp_f32 v48, v152\n"
"v_mfma_f32_16x16x32_bf16 v[164:167], a[244:247], v[204:207], v[164:167]\n"
"v_cvt_pk_bf16_f32 v125, v50, v51\n"
"v_cvt_pk_bf16_f32 v128, v52, v53\n"
"v_mfma_f32_16x16x32_bf16 v[164:167], a[248:251], v[224:227], v[164:167]\n"
"v_cvt_pk_bf16_f32 v129, v54, v55\n"
"v_add_f32 v35, v35, v49\n"
"v_mfma_f32_16x16x32_bf16 v[164:167], a[252:255], v[244:247], v[164:167]\n"
"v_exp_f32 v49, v153\n"
"v_mfma_f32_16x16x32_bf16 v[164:167], v[60:63], a[168:171], v[164:167]\n"
"v_cvt_pk_bf16_f32 v132, v48, v49\n"
"v_add_f32 v35, v35, v50\n"
"v_mfma_f32_16x16x32_bf16 v[164:167], v[64:67], a[188:191], v[164:167]\n"
"v_exp_f32 v50, v154\n"
"v_mfma_f32_16x16x32_bf16 v[164:167], v[68:71], a[208:211], v[164:167]\n"
"v_add_f32 v35, v35, v51\n"
"v_add_f32 v36, v36, v52\n"
"v_mfma_f32_16x16x32_bf16 v[164:167], v[72:75], a[228:231], v[164:167]\n"
"v_exp_f32 v51, v155\n"
"v_mfma_f32_16x16x32_bf16 v[168:171], a[240:243], v[188:191], v[88:91]\n"
"v_cvt_pk_bf16_f32 v133, v50, v51\n"
"v_add_f32 v36, v36, v53\n"
"v_mfma_f32_16x16x32_bf16 v[168:171], a[244:247], v[208:211], v[168:171]\n"
"v_add_f32 v36, v36, v54\n"
"v_add_f32 v36, v36, v55\n"
"v_mfma_f32_16x16x32_bf16 v[168:171], a[248:251], v[228:231], v[168:171]\n"
"v_add_f32 v37, v37, v48\n"
"v_add_f32 v37, v37, v49\n"
"v_mfma_f32_16x16x32_bf16 v[168:171], a[252:255], v[248:251], v[168:171]\n"
"v_add_f32 v37, v37, v50\n"
"v_add_f32 v37, v37, v51\n"
"v_mfma_f32_16x16x32_bf16 v[168:171], v[60:63], a[172:175], v[168:171]\n"
"v_mfma_f32_16x16x32_bf16 v[168:171], v[64:67], a[192:195], v[168:171]\n"
"v_mfma_f32_16x16x32_bf16 v[168:171], v[68:71], a[212:215], v[168:171]\n"
"v_mfma_f32_16x16x32_bf16 v[168:171], v[72:75], a[232:235], v[168:171]\n"
"v_mfma_f32_16x16x32_bf16 v[172:175], v[60:63], a[176:179], v[92:95]\n"
"ds_read_b64_tr_b16 v[60:61], v22 offset:49152\n"
"v_mfma_f32_16x16x32_bf16 v[172:175], v[64:67], a[196:199], v[172:175]\n"
"ds_read_b64_tr_b16 v[62:63], v22 offset:53248\n"
"v_mfma_f32_16x16x32_bf16 v[172:175], v[68:71], a[216:219], v[172:175]\n"
"ds_read_b64_tr_b16 v[64:65], v23 offset:49152\n"
"v_mfma_f32_16x16x32_bf16 v[172:175], v[72:75], a[236:239], v[172:175]\n"
"ds_read_b64_tr_b16 v[66:67], v23 offset:53248\n"
"v_mfma_f32_16x16x32_bf16 v[172:175], a[240:243], v[192:195], v[172:175]\n"
"ds_read_b64_tr_b16 v[68:69], v24 offset:49152\n"
"v_mfma_f32_16x16x32_bf16 v[172:175], a[244:247], v[212:215], v[172:175]\n"
"ds_read_b64_tr_b16 v[70:71], v24 offset:53248\n"
"v_mfma_f32_16x16x32_bf16 v[172:175], a[248:251], v[232:235], v[172:175]\n"
"s_cmp_lg_u32 s56, 0\n"
"ds_read_b64_tr_b16 v[72:73], v25 offset:49152\n"
"v_mfma_f32_16x16x32_bf16 v[172:175], a[252:255], v[252:255], v[172:175]\n"
"s_waitcnt lgkmcnt(6)\n"
"s_cbranch_scc1 S3_14_%=\n"
"S3_14_r_%=:\n"
"ds_read_b64_tr_b16 v[74:75], v25 offset:53248\n"
"v_mfma_f32_16x16x32_bf16 a[0:3], v[56:59], v[96:99], a[0:3]\n"
"v_max3_f32 v10, v156, v157, v158\n"
"v_max3_f32 v10, v10, v159, v160\n"
"v_mfma_f32_16x16x32_bf16 a[4:7], v[56:59], v[100:103], a[4:7]\n"
"v_max3_f32 v10, v10, v161, v162\n"
"v_max3_f32 v10, v10, v163, v164\n"
"v_mfma_f32_16x16x32_bf16 a[8:11], v[56:59], v[104:107], a[8:11]\n"
"v_max3_f32 v10, v10, v165, v166\n"
"v_max3_f32 v10, v10, v167, v168\n"
"v_mfma_f32_16x16x32_bf16 a[12:15], v[56:59], v[108:111], a[12:15]\n"
"v_max3_f32 v10, v10, v169, v170\n"
"v_max3_f32 v10, v10, v171, v172\n"
"v_mfma_f32_16x16x32_bf16 a[16:19], v[56:59], v[112:115], a[16:19]\n"
"s_waitcnt lgkmcnt(0)\n"
"ds_read_b64_tr_b16 v[56:57], v26 offset:49152\n"
"v_max3_f32 v10, v10, v173, v174\n"
"v_mfma_f32_16x16x32_bf16 a[20:23], v[60:63], v[96:99], a[20:23]\n"
"ds_read_b64_tr_b16 v[58:59], v26 offset:53248\n"
"v_max_f32 v10, v10, v175\n"
"v_mfma_f32_16x16x32_bf16 a[24:27], v[60:63], v[100:103], a[24:27]\n"
"v_cmp_nge_f32 vcc, s62, v10\n"
"v_mfma_f32_16x16x32_bf16 a[28:31], v[60:63], v[104:107], a[28:31]\n"
"v_mfma_f32_16x16x32_bf16 a[32:35], v[60:63], v[108:111], a[32:35]\n"
"v_mfma_f32_16x16x32_bf16 a[36:39], v[60:63], v[112:115], a[36:39]\n"
"s_cbranch_vccnz R1_14_%=\n"
"R1_14_r_%=:\n"
"ds_read_b64_tr_b16 v[60:61], v27 offset:49152\n"
"v_mfma_f32_16x16x32_bf16 a[40:43], v[64:67], v[96:99], a[40:43]\n"
"ds_read_b64_tr_b16 v[62:63], v27 offset:53248\n"
"v_mfma_f32_16x16x32_bf16 a[44:47], v[64:67], v[100:103], a[44:47]\n"
"v_exp_f32 v52, v156\n"
"v_mfma_f32_16x16x32_bf16 a[48:51], v[64:67], v[104:107], a[48:51]\n"
"v_exp_f32 v53, v157\n"
"v_mfma_f32_16x16x32_bf16 a[52:55], v[64:67], v[108:111], a[52:55]\n"
"v_exp_f32 v54, v158\n"
"v_mfma_f32_16x16x32_bf16 a[56:59], v[64:67], v[112:115], a[56:59]\n"
"ds_read_b64_tr_b16 v[64:65], v28 offset:49152\n"
"v_cvt_pk_bf16_f32 v118, v52, v53\n"
"v_mfma_f32_16x16x32_bf16 a[60:63], v[68:71], v[96:99], a[60:63]\n"
"ds_read_b64_tr_b16 v[66:67], v28 offset:53248\n"
"v_add_f32 v33, v33, v52\n"
"v_mfma_f32_16x16x32_bf16 a[64:67], v[68:71], v[100:103], a[64:67]\n"
"v_exp_f32 v55, v159\n"
"v_mfma_f32_16x16x32_bf16 a[68:71], v[68:71], v[104:107], a[68:71]\n"
"v_exp_f32 v48, v160\n"
"v_mfma_f32_16x16x32_bf16 a[72:75], v[68:71], v[108:111], a[72:75]\n"
"v_exp_f32 v49, v161\n"
"v_mfma_f32_16x16x32_bf16 a[76:79], v[68:71], v[112:115], a[76:79]\n"
"v_exp_f32 v50, v162\n"
"v_mfma_f32_16x16x32_bf16 a[80:83], v[72:75], v[96:99], a[80:83]\n"
"v_exp_f32 v51, v163\n"
"v_mfma_f32_16x16x32_bf16 a[84:87], v[72:75], v[100:103], a[84:87]\n"
"v_exp_f32 v52, v164\n"
"v_mfma_f32_16x16x32_bf16 a[88:91], v[72:75], v[104:107], a[88:91]\n"
"v_cvt_pk_bf16_f32 v119, v54, v55\n"
"v_cvt_pk_bf16_f32 v122, v48, v49\n"
"v_mfma_f32_16x16x32_bf16 a[92:95], v[72:75], v[108:111], a[92:95]\n"
"v_cvt_pk_bf16_f32 v123, v50, v51\n"
"v_add_f32 v33, v33, v53\n"
"v_mfma_f32_16x16x32_bf16 a[96:99], v[72:75], v[112:115], a[96:99]\n"
"s_waitcnt vmcnt(13) lgkmcnt(0)\n"
"s_barrier\n"
"v_mfma_f32_16x16x32_bf16 a[100:103], v[56:59], v[96:99], a[100:103]\n"
"ds_read_b128 a[240:243], v29 offset:32768\n"
"v_add_f32 v33, v33, v54\n"
"v_mfma_f32_16x16x32_bf16 a[104:107], v[56:59], v[100:103], a[104:107]\n"
"ds_read_b128 a[244:247], v30 offset:32768\n"
"v_add_f32 v33, v33, v55\n"
"v_mfma_f32_16x16x32_bf16 a[108:111], v[56:59], v[104:107], a[108:111]\n"
"ds_read_b128 a[248:251], v31 offset:32768\n"
"v_add_f32 v34, v34, v48\n"
"v_mfma_f32_16x16x32_bf16 a[112:115], v[56:59], v[108:111], a[112:115]\n"
"ds_read_b128 a[252:255], v32 offset:32768\n"
"v_add_f32 v34, v34, v49\n"
"v_mfma_f32_16x16x32_bf16 a[116:119], v[56:59], v[112:115], a[116:119]\n"
"ds_read_b128 v[68:71], v31 offset:33024\n"
"v_add_f32 v34, v34, v50\n"
"v_mfma_f32_16x16x32_bf16 a[120:123], v[60:63], v[96:99], a[120:123]\n"
"ds_read_b128 v[72:75], v32 offset:33024\n"
"v_add_f32 v34, v34, v51\n"
"v_mfma_f32_16x16x32_bf16 a[124:127], v[60:63], v[100:103], a[124:127]\n"
"v_exp_f32 v53, v165\n"
"v_mfma_f32_16x16x32_bf16 a[128:131], v[60:63], v[104:107], a[128:131]\n"
"v_exp_f32 v54, v166\n"
"v_mfma_f32_16x16x32_bf16 a[132:135], v[60:63], v[108:111], a[132:135]\n"
"v_exp_f32 v55, v167\n"
"v_mfma_f32_16x16x32_bf16 a[136:139], v[60:63], v[112:115], a[136:139]\n"
"v_exp_f32 v48, v168\n"
"v_mfma_f32_16x16x32_bf16 a[140:143], v[64:67], v[96:99], a[140:143]\n"
"ds_read_b128 v[60:63], v29 offset:33024\n"
"v_cvt_pk_bf16_f32 v126, v52, v53\n"
"v_mfma_f32_16x16x32_bf16 a[144:147], v[64:67], v[100:103], a[144:147]\n"
"v_exp_f32 v49, v169\n"
"v_mfma_f32_16x16x32_bf16 a[148:151], v[64:67], v[104:107], a[148:151]\n"
"v_exp_f32 v50, v170\n"
"v_mfma_f32_16x16x32_bf16 a[152:155], v[64:67], v[108:111], a[152:155]\n"
"s_cmp_lg_u32 s56, 0\n"
"v_exp_f32 v51, v171\n"
"v_mfma_f32_16x16x32_bf16 a[156:159], v[64:67], v[112:115], a[156:159]\n"
"s_cbranch_scc1 S1_14_%=\n"
"S1_14_r_%=:\n"
"ds_read_b128 v[64:67], v30 offset:33024\n"
"s_waitcnt lgkmcnt(1)\n"
"v_mfma_f32_16x16x32_bf16 v[136:139], a[240:243], v[176:179], v[76:79]\n"
"s_mov_b32 m0, s55\n"
"v_cvt_pk_bf16_f32 v127, v54, v55\n"
"v_cvt_pk_bf16_f32 v130, v48, v49\n"
"v_mfma_f32_16x16x32_bf16 v[136:139], a[244:247], v[196:199], v[136:139]\n"
"buffer_load_dwordx4 v13, s[40:43], s47 offen nt lds\n"
"v_cvt_pk_bf16_f32 v131, v50, v51\n"
"v_mfma_f32_16x16x32_bf16 v[136:139], a[248:251], v[216:219], v[136:139]\n"
"v_add_f32 v35, v35, v52\n"
"v_add_f32 v35, v35, v53\n"
"v_mfma_f32_16x16x32_bf16 v[136:139], a[252:255], v[236:239], v[136:139]\n"
"v_exp_f32 v52, v172\n"
"v_mfma_f32_16x16x32_bf16 v[136:139], v[68:71], a[200:203], v[136:139]\n"
"ds_read_b64_tr_b16 v[56:57], v21 offset:0\n"
"v_add_f32 v35, v35, v54\n"
"v_mfma_f32_16x16x32_bf16 v[136:139], v[72:75], a[220:223], v[136:139]\n"
"ds_read_b64_tr_b16 v[58:59], v21 offset:4096\n"
"v_add_f32 v35, v35, v55\n"
"v_mfma_f32_16x16x32_bf16 v[136:139], v[60:63], a[160:163], v[136:139]\n"
"s_waitcnt lgkmcnt(2)\n"
"buffer_load_dwordx4 v14, s[40:43], s47 offen offset:1024 nt lds\n"
"v_add_f32 v36, v36, v48\n"
"v_mfma_f32_16x16x32_bf16 v[136:139], v[64:67], a[180:183], v[136:139]\n"
"v_exp_f32 v53, v173\n"
"v_mfma_f32_16x16x32_bf16 v[140:143], a[240:243], v[180:183], v[80:83]\n"
"v_exp_f32 v54, v174\n"
"v_mfma_f32_16x16x32_bf16 v[140:143], a[244:247], v[200:203], v[140:143]\n"
"v_exp_f32 v55, v175\n"
"v_mfma_f32_16x16x32_bf16 v[140:143], a[248:251], v[220:223], v[140:143]\n"
"v_cvt_pk_bf16_f32 v134, v52, v53\n"
"v_cvt_pk_bf16_f32 v135, v54, v55\n"
"v_mfma_f32_16x16x32_bf16 v[140:143], a[252:255], v[240:243], v[140:143]\n"
"buffer_load_dwordx4 v15, s[40:43], s47 offen offset:2048 nt lds\n"
"v_add_f32 v36, v36, v49\n"
"v_mfma_f32_16x16x32_bf16 v[140:143], v[60:63], a[164:167], v[140:143]\n"
"v_add_f32 v36, v36, v50\n"
"v_add_f32 v36, v36, v51\n"
"v_mfma_f32_16x16x32_bf16 v[140:143], v[64:67], a[184:187], v[140:143]\n"
"v_add_f32 v37, v37, v52\n"
"v_add_f32 v37, v37, v53\n"
"v_mfma_f32_16x16x32_bf16 v[140:143], v[68:71], a[204:207], v[140:143]\n"
"v_add_f32 v37, v37, v54\n"
"v_add_f32 v37, v37, v55\n"
"v_mfma_f32_16x16x32_bf16 v[140:143], v[72:75], a[224:227], v[140:143]\n"
"v_mfma_f32_16x16x32_bf16 v[144:147], a[240:243], v[184:187], v[84:87]\n"
"buffer_load_dwordx4 v16, s[40:43], s47 offen offset:3072 nt lds\n"
"v_mfma_f32_16x16x32_bf16 v[144:147], a[244:247], v[204:207], v[144:147]\n"
"s_add_u32 s47, s47, 32768\n"
"v_mfma_f32_16x16x32_bf16 v[144:147], a[248:251], v[224:227], v[144:147]\n"
"v_mfma_f32_16x16x32_bf16 v[144:147], a[252:255], v[244:247], v[144:147]\n"
"v_mfma_f32_16x16x32_bf16 v[144:147], v[60:63], a[168:171], v[144:147]\n"
"ds_write_b128 v12, v[116:119] offset:0\n"
"v_mfma_f32_16x16x32_bf16 v[144:147], v[64:67], a[188:191], v[144:147]\n"
"v_mfma_f32_16x16x32_bf16 v[144:147], v[68:71], a[208:211], v[144:147]\n"
"ds_write_b128 v12, v[120:123] offset:1024\n"
"v_mfma_f32_16x16x32_bf16 v[144:147], v[72:75], a[228:231], v[144:147]\n"
"v_mfma_f32_16x16x32_bf16 v[148:151], a[240:243], v[188:191], v[88:91]\n"
"ds_write_b128 v12, v[124:127] offset:2048\n"
"v_mfma_f32_16x16x32_bf16 v[148:151], a[244:247], v[208:211], v[148:151]\n"
"v_mfma_f32_16x16x32_bf16 v[148:151], a[248:251], v[228:231], v[148:151]\n"
"v_mfma_f32_16x16x32_bf16 v[148:151], a[252:255], v[248:251], v[148:151]\n"
"ds_write_b128 v12, v[128:131] offset:3072\n"
"v_mfma_f32_16x16x32_bf16 v[148:151], v[60:63], a[172:175], v[148:151]\n"
"v_mfma_f32_16x16x32_bf16 v[148:151], v[64:67], a[192:195], v[148:151]\n"
"ds_write_b128 v12, v[132:135] offset:4096\n"
"v_mfma_f32_16x16x32_bf16 v[148:151], v[68:71], a[212:215], v[148:151]\n"
"v_mfma_f32_16x16x32_bf16 v[148:151], v[72:75], a[232:235], v[148:151]\n"
"v_mfma_f32_16x16x32_bf16 v[152:155], v[60:63], a[176:179], v[92:95]\n"
"ds_read_b64_tr_b16 v[60:61], v22 offset:0\n"
"v_mfma_f32_16x16x32_bf16 v[152:155], v[64:67], a[196:199], v[152:155]\n"
"ds_read_b64_tr_b16 v[62:63], v22 offset:4096\n"
"v_mfma_f32_16x16x32_bf16 v[152:155], v[68:71], a[216:219], v[152:155]\n"
"ds_read_b64_tr_b16 v[64:65], v23 offset:0\n"
"v_mfma_f32_16x16x32_bf16 v[152:155], v[72:75], a[236:239], v[152:155]\n"
"ds_read_b64_tr_b16 v[66:67], v23 offset:4096\n"
"v_mfma_f32_16x16x32_bf16 v[152:155], a[240:243], v[192:195], v[152:155]\n"
"ds_read_b64_tr_b16 v[68:69], v24 offset:0\n"
"v_mfma_f32_16x16x32_bf16 v[152:155], a[244:247], v[212:215], v[152:155]\n"
"ds_read_b64_tr_b16 v[70:71], v24 offset:4096\n"
"v_mfma_f32_16x16x32_bf16 v[152:155], a[248:251], v[232:235], v[152:155]\n"
"s_cmp_lg_u32 s56, 0\n"
"ds_read_b64_tr_b16 v[72:73], v25 offset:0\n"
"v_mfma_f32_16x16x32_bf16 v[152:155], a[252:255], v[252:255], v[152:155]\n"
"s_waitcnt lgkmcnt(6)\n"
"s_cbranch_scc1 S2_15_%=\n"
"S2_15_r_%=:\n"
"ds_read_b64_tr_b16 v[74:75], v25 offset:4096\n"
"s_getpc_b64 s[64:65]\n"
"PC1_%=:\n"
"v_mfma_f32_16x16x32_bf16 a[0:3], v[56:59], v[116:119], a[0:3]\n"
"v_max3_f32 v10, v136, v137, v138\n"
"v_max3_f32 v10, v10, v139, v140\n"
"v_mfma_f32_16x16x32_bf16 a[4:7], v[56:59], v[120:123], a[4:7]\n"
"v_max3_f32 v10, v10, v141, v142\n"
"v_max3_f32 v10, v10, v143, v144\n"
"v_mfma_f32_16x16x32_bf16 a[8:11], v[56:59], v[124:127], a[8:11]\n"
"v_max3_f32 v10, v10, v145, v146\n"
"v_max3_f32 v10, v10, v147, v148\n"
"v_mfma_f32_16x16x32_bf16 a[12:15], v[56:59], v[128:131], a[12:15]\n"
"v_max3_f32 v10, v10, v149, v150\n"
"v_max3_f32 v10, v10, v151, v152\n"
"v_mfma_f32_16x16x32_bf16 a[16:19], v[56:59], v[132:135], a[16:19]\n"
"s_waitcnt lgkmcnt(0)\n"
"ds_read_b64_tr_b16 v[56:57], v26 offset:0\n"
"v_max3_f32 v10, v10, v153, v154\n"
"v_mfma_f32_16x16x32_bf16 a[20:23], v[60:63], v[116:119], a[20:23]\n"
"ds_read_b64_tr_b16 v[58:59], v26 offset:4096\n"
"v_max_f32 v10, v10, v155\n"
"v_mfma_f32_16x16x32_bf16 a[24:27], v[60:63], v[120:123], a[24:27]\n"
"s_waitcnt vmcnt(10)\n"
"s_lshl_b32 s60, s70, 13\n"
"v_lshlrev_b32 v6, 7, v0\n"
"v_cmp_nge_f32 vcc, s62, v10\n"
"v_mfma_f32_16x16x32_bf16 a[28:31], v[60:63], v[124:127], a[28:31]\n"
"v_add_u32 v6, s60, v6\n"
"v_mfma_f32_16x16x32_bf16 a[32:35], v[60:63], v[128:131], a[32:35]\n"
"s_mov_b32 s60, HOT_END_%=-PC1_%=-4\n"
"v_min_u32 v6, s60, v6\n"
"v_mfma_f32_16x16x32_bf16 a[36:39], v[60:63], v[132:135], a[36:39]\n"
"global_load_dword v6, v6, s[64:65]\n"
"ds_read_b64_tr_b16 v[60:61], v27 offset:0\n"
"v_mfma_f32_16x16x32_bf16 a[40:43], v[64:67], v[116:119], a[40:43]\n"
"ds_read_b64_tr_b16 v[62:63], v27 offset:4096\n"
"v_mfma_f32_16x16x32_bf16 a[44:47], v[64:67], v[120:123], a[44:47]\n"
"v_mfma_f32_16x16x32_bf16 a[48:51], v[64:67], v[124:127], a[48:51]\n"
"v_mfma_f32_16x16x32_bf16 a[52:55], v[64:67], v[128:131], a[52:55]\n"
"v_mfma_f32_16x16x32_bf16 a[56:59], v[64:67], v[132:135], a[56:59]\n"
"s_cbranch_vccnz R0_15_%=\n"
"R0_15_r_%=:\n"
"ds_read_b64_tr_b16 v[64:65], v28 offset:0\n"
"v_mfma_f32_16x16x32_bf16 a[60:63], v[68:71], v[116:119], a[60:63]\n"
"ds_read_b64_tr_b16 v[66:67], v28 offset:4096\n"
"v_mfma_f32_16x16x32_bf16 a[64:67], v[68:71], v[120:123], a[64:67]\n"
"v_exp_f32 v48, v136\n"
"v_mfma_f32_16x16x32_bf16 a[68:71], v[68:71], v[124:127], a[68:71]\n"
"v_exp_f32 v49, v137\n"
"v_mfma_f32_16x16x32_bf16 a[72:75], v[68:71], v[128:131], a[72:75]\n"
"v_exp_f32 v50, v138\n"
"v_mfma_f32_16x16x32_bf16 a[76:79], v[68:71], v[132:135], a[76:79]\n"
"v_exp_f32 v51, v139\n"
"v_mfma_f32_16x16x32_bf16 a[80:83], v[72:75], v[116:119], a[80:83]\n"
"v_exp_f32 v52, v140\n"
"v_mfma_f32_16x16x32_bf16 a[84:87], v[72:75], v[120:123], a[84:87]\n"
"v_exp_f32 v53, v141\n"
"v_mfma_f32_16x16x32_bf16 a[88:91], v[72:75], v[124:127], a[88:91]\n"
"v_exp_f32 v54, v142\n"
"v_mfma_f32_16x16x32_bf16 a[92:95], v[72:75], v[128:131], a[92:95]\n"
"v_exp_f32 v55, v143\n"
"v_mfma_f32_16x16x32_bf16 a[96:99], v[72:75], v[132:135], a[96:99]\n"
"s_waitcnt vmcnt(9) lgkmcnt(0)\n"
"s_barrier\n"
"v_mfma_f32_16x16x32_bf16 a[100:103], v[56:59], v[116:119], a[100:103]\n"
"ds_read_b128 a[240:243], v29 offset:49152\n"
"v_add_f32 v33, v33, v48\n"
"v_mfma_f32_16x16x32_bf16 a[104:107], v[56:59], v[120:123], a[104:107]\n"
"ds_read_b128 a[244:247], v30 offset:49152\n"
"v_add_f32 v33, v33, v49\n"
"v_mfma_f32_16x16x32_bf16 a[108:111], v[56:59], v[124:127], a[108:111]\n"
"v_add_f32 v33, v33, v50\n"
"v_add_f32 v33, v33, v51\n"
"v_mfma_f32_16x16x32_bf16 a[112:115], v[56:59], v[128:131], a[112:115]\n"
"ds_read_b128 a[248:251], v31 offset:49152\n"
"v_add_f32 v34, v34, v52\n"
"v_mfma_f32_16x16x32_bf16 a[116:119], v[56:59], v[132:135], a[116:119]\n"
"ds_read_b128 a[252:255], v32 offset:49152\n"
"v_add_f32 v34, v34, v53\n"
"v_mfma_f32_16x16x32_bf16 a[120:123], v[60:63], v[116:119], a[120:123]\n"
"ds_read_b128 v[68:71], v31 offset:49408\n"
"v_add_f32 v34, v34, v54\n"
"v_mfma_f32_16x16x32_bf16 a[124:127], v[60:63], v[120:123], a[124:127]\n"
"ds_read_b128 v[72:75], v32 offset:49408\n"
"v_add_f32 v34, v34, v55\n"
"v_mfma_f32_16x16x32_bf16 a[128:131], v[60:63], v[124:127], a[128:131]\n"
"v_mfma_f32_16x16x32_bf16 a[132:135], v[60:63], v[128:131], a[132:135]\n"
"v_mfma_f32_16x16x32_bf16 a[136:139], v[60:63], v[132:135], a[136:139]\n"
"v_mfma_f32_16x16x32_bf16 a[140:143], v[64:67], v[116:119], a[140:143]\n"
"ds_read_b128 v[60:63], v29 offset:49408\n"
"v_cvt_pk_bf16_f32 v116, v48, v49\n"
"v_mfma_f32_16x16x32_bf16 a[144:147], v[64:67], v[120:123], a[144:147]\n"
"v_exp_f32 v48, v144\n"
"v_mfma_f32_16x16x32_bf16 a[148:151], v[64:67], v[124:127], a[148:151]\n"
"v_exp_f32 v49, v145\n"
"v_mfma_f32_16x16x32_bf16 a[152:155], v[64:67], v[128:131], a[152:155]\n"
"ds_read_b32 v9, v7 offset:1280\n"
"v_cvt_pk_bf16_f32 v117, v50, v51\n"
"v_mfma_f32_16x16x32_bf16 a[156:159], v[64:67], v[132:135], a[156:159]\n"
"s_waitcnt lgkmcnt(2)\n"
"ds_read_b128 v[64:67], v30 offset:49408\n"
"v_cvt_pk_bf16_f32 v120, v52, v53\n"
"v_mfma_f32_16x16x32_bf16 v[156:159], a[240:243], v[176:179], v[76:79]\n"
"s_mov_b64 s[80:81], s[20:21]\n"
"s_and_b32 s81, s81, 0xffff\n"
"s_mov_b32 s82, 229376\n"
"s_mov_b32 s83, 0x20000\n"
"s_mul_i32 s85, s70, 14336\n"
"s_mov_b32 s84, s85\n"
"s_movk_i32 s87, 1171\n"
"v_mul_u32_u24 v19, s87, v0\n"
"ds_read_b128 v[96:99], v11 offset:0\n"
"v_cvt_pk_bf16_f32 v121, v54, v55\n"
"v_mfma_f32_16x16x32_bf16 v[156:159], a[244:247], v[196:199], v[156:159]\n"
"v_lshlrev_b32 v20, 4, v0\n"
"ds_read_b128 v[100:103], v11 offset:1024\n"
"v_cvt_pk_bf16_f32 v124, v48, v49\n"
"v_mfma_f32_16x16x32_bf16 v[156:159], a[248:251], v[216:219], v[156:159]\n"
"v_add_u32 v13, 0, v19\n"
"ds_read_b128 v[104:107], v11 offset:2048\n"
"v_add_f32 v35, v35, v48\n"
"v_mfma_f32_16x16x32_bf16 v[156:159], a[252:255], v[236:239], v[156:159]\n"
"v_bfe_u32 v13, v13, 17, 2\n"
"v_exp_f32 v50, v146\n"
"v_mfma_f32_16x16x32_bf16 v[156:159], v[68:71], a[200:203], v[156:159]\n"
"v_lshlrev_b32 v13, 5, v13\n"
"ds_read_b64_tr_b16 v[56:57], v21 offset:32768\n"
"v_add_f32 v35, v35, v49\n"
"v_mfma_f32_16x16x32_bf16 v[156:159], v[72:75], a[220:223], v[156:159]\n"
"s_waitcnt lgkmcnt(2)\n"
"v_xor_b32 v13, v13, v20\n"
"ds_read_b64_tr_b16 v[58:59], v21 offset:36864\n"
"v_add_f32 v35, v35, v50\n"
"v_mfma_f32_16x16x32_bf16 v[156:159], v[60:63], a[160:163], v[156:159]\n"
"v_add_u32 v14, 74944, v19\n"
"ds_read_b128 v[108:111], v11 offset:3072\n"
"v_mfma_f32_16x16x32_bf16 v[156:159], v[64:67], a[180:183], v[156:159]\n"
"v_bfe_u32 v14, v14, 17, 2\n"
"ds_read_b128 v[112:115], v11 offset:4096\n"
"v_mfma_f32_16x16x32_bf16 v[160:163], a[240:243], v[180:183], v[80:83]\n"
"v_lshlrev_b32 v14, 5, v14\n"
"v_exp_f32 v51, v147\n"
"v_mfma_f32_16x16x32_bf16 v[160:163], a[244:247], v[200:203], v[160:163]\n"
"v_xor_b32 v14, v14, v20\n"
"v_exp_f32 v52, v148\n"
"v_mfma_f32_16x16x32_bf16 v[160:163], a[248:251], v[220:223], v[160:163]\n"
"v_add_u32 v15, 149888, v19\n"
"v_readfirstlane_b32 s59, v9\n"
"v_cvt_pk_bf16_f32 v125, v50, v51\n"
"v_mfma_f32_16x16x32_bf16 v[160:163], a[252:255], v[240:243], v[160:163]\n"
"v_bfe_u32 v15, v15, 17, 2\n"
"v_exp_f32 v53, v149\n"
"v_mfma_f32_16x16x32_bf16 v[160:163], v[60:63], a[164:167], v[160:163]\n"
"v_lshlrev_b32 v15, 5, v15\n"
"s_cmp_lg_u32 s59, s58\n"
"v_exp_f32 v54, v150\n"
"v_mfma_f32_16x16x32_bf16 v[160:163], v[64:67], a[184:187], v[160:163]\n"
"v_xor_b32 v15, v15, v20\n"
"s_cselect_b32 s60, 3, 0\n"
"v_exp_f32 v55, v151\n"
"v_mfma_f32_16x16x32_bf16 v[160:163], v[68:71], a[204:207], v[160:163]\n"
"v_add_u32 v16, 224832, v19\n"
"v_exp_f32 v48, v152\n"
"v_mfma_f32_16x16x32_bf16 v[160:163], v[72:75], a[224:227], v[160:163]\n"
"v_bfe_u32 v16, v16, 17, 2\n"
"s_max_u32 s56, s56, s60\n"
"v_exp_f32 v49, v153\n"
"v_mfma_f32_16x16x32_bf16 v[164:167], a[240:243], v[184:187], v[84:87]\n"
"v_lshlrev_b32 v16, 5, v16\n"
"s_mov_b32 s58, s59\n"
"v_exp_f32 v50, v154\n"
"v_mfma_f32_16x16x32_bf16 v[164:167], a[244:247], v[204:207], v[164:167]\n"
"v_xor_b32 v16, v16, v20\n"
"v_cvt_pk_bf16_f32 v128, v52, v53\n"
"v_cvt_pk_bf16_f32 v129, v54, v55\n"
"v_mfma_f32_16x16x32_bf16 v[164:167], a[248:251], v[224:227], v[164:167]\n"
"v_add_u32 v17, 299776, v19\n"
"v_cvt_pk_bf16_f32 v132, v48, v49\n"
"v_add_f32 v35, v35, v51\n"
"v_mfma_f32_16x16x32_bf16 v[164:167], a[252:255], v[244:247], v[164:167]\n"
"v_bfe_u32 v17, v17, 17, 2\n"
"v_exp_f32 v51, v155\n"
"v_mfma_f32_16x16x32_bf16 v[164:167], v[60:63], a[168:171], v[164:167]\n"
"v_lshlrev_b32 v17, 5, v17\n"
"v_cvt_pk_bf16_f32 v133, v50, v51\n"
"v_add_f32 v36, v36, v52\n"
"v_mfma_f32_16x16x32_bf16 v[164:167], v[64:67], a[188:191], v[164:167]\n"
"v_xor_b32 v17, v17, v20\n"
"v_add_f32 v36, v36, v53\n"
"v_add_f32 v36, v36, v54\n"
"v_mfma_f32_16x16x32_bf16 v[164:167], v[68:71], a[208:211], v[164:167]\n"
"v_add_u32 v18, 374720, v19\n"
"v_add_f32 v36, v36, v55\n"
"v_add_f32 v37, v37, v48\n"
"v_mfma_f32_16x16x32_bf16 v[164:167], v[72:75], a[228:231], v[164:167]\n"
"v_bfe_u32 v18, v18, 17, 2\n"
"v_add_f32 v37, v37, v49\n"
"v_add_f32 v37, v37, v50\n"
"v_mfma_f32_16x16x32_bf16 v[168:171], a[240:243], v[188:191], v[88:91]\n"
"v_lshlrev_b32 v18, 5, v18\n"
"v_add_f32 v37, v37, v51\n"
"v_mfma_f32_16x16x32_bf16 v[168:171], a[244:247], v[208:211], v[168:171]\n"
"v_xor_b32 v18, v18, v20\n"
"v_mfma_f32_16x16x32_bf16 v[168:171], a[248:251], v[228:231], v[168:171]\n"
"v_add_u32 v19, 449664, v19\n"
"v_mfma_f32_16x16x32_bf16 v[168:171], a[252:255], v[248:251], v[168:171]\n"
"v_bfe_u32 v19, v19, 17, 2\n"
"v_mfma_f32_16x16x32_bf16 v[168:171], v[60:63], a[172:175], v[168:171]\n"
"v_lshlrev_b32 v19, 5, v19\n"
"v_mfma_f32_16x16x32_bf16 v[168:171], v[64:67], a[192:195], v[168:171]\n"
"v_xor_b32 v19, v19, v20\n"
"v_mfma_f32_16x16x32_bf16 v[168:171], v[68:71], a[212:215], v[168:171]\n"
"v_mfma_f32_16x16x32_bf16 v[168:171], v[72:75], a[232:235], v[168:171]\n"
"v_mfma_f32_16x16x32_bf16 v[172:175], v[60:63], a[176:179], v[92:95]\n"
"ds_read_b64_tr_b16 v[60:61], v22 offset:32768\n"
"v_mfma_f32_16x16x32_bf16 v[172:175], v[64:67], a[196:199], v[172:175]\n"
"ds_read_b64_tr_b16 v[62:63], v22 offset:36864\n"
"v_mfma_f32_16x16x32_bf16 v[172:175], v[68:71], a[216:219], v[172:175]\n"
"ds_read_b64_tr_b16 v[64:65], v23 offset:32768\n"
"v_mfma_f32_16x16x32_bf16 v[172:175], v[72:75], a[236:239], v[172:175]\n"
"ds_read_b64_tr_b16 v[66:67], v23 offset:36864\n"
"v_mfma_f32_16x16x32_bf16 v[172:175], a[240:243], v[192:195], v[172:175]\n"
"ds_read_b64_tr_b16 v[68:69], v24 offset:32768\n"
"v_mfma_f32_16x16x32_bf16 v[172:175], a[244:247], v[212:215], v[172:175]\n"
"ds_read_b64_tr_b16 v[70:71], v24 offset:36864\n"
"v_mfma_f32_16x16x32_bf16 v[172:175], a[248:251], v[232:235], v[172:175]\n"
"s_cmp_lg_u32 s56, 0\n"
"ds_read_b64_tr_b16 v[72:73], v25 offset:32768\n"
"v_mfma_f32_16x16x32_bf16 v[172:175], a[252:255], v[252:255], v[172:175]\n"
"s_waitcnt lgkmcnt(6)\n"
"s_cbranch_scc1 S3_15_%=\n"
"S3_15_r_%=:\n"
"ds_read_b64_tr_b16 v[74:75], v25 offset:36864\n"
"v_mfma_f32_16x16x32_bf16 a[0:3], v[56:59], v[96:99], a[0:3]\n"
"v_max3_f32 v10, v156, v157, v158\n"
"v_max3_f32 v10, v10, v159, v160\n"
"v_mfma_f32_16x16x32_bf16 a[4:7], v[56:59], v[100:103], a[4:7]\n"
"v_max3_f32 v10, v10, v161, v162\n"
"v_max3_f32 v10, v10, v163, v164\n"
"v_mfma_f32_16x16x32_bf16 a[8:11], v[56:59], v[104:107], a[8:11]\n"
"v_max3_f32 v10, v10, v165, v166\n"
"v_max3_f32 v10, v10, v167, v168\n"
"v_mfma_f32_16x16x32_bf16 a[12:15], v[56:59], v[108:111], a[12:15]\n"
"v_max3_f32 v10, v10, v169, v170\n"
"v_max3_f32 v10, v10, v171, v172\n"
"v_mfma_f32_16x16x32_bf16 a[16:19], v[56:59], v[112:115], a[16:19]\n"
"s_waitcnt lgkmcnt(0)\n"
"ds_read_b64_tr_b16 v[56:57], v26 offset:32768\n"
"v_max3_f32 v10, v10, v173, v174\n"
"v_mfma_f32_16x16x32_bf16 a[20:23], v[60:63], v[96:99], a[20:23]\n"
"ds_read_b64_tr_b16 v[58:59], v26 offset:36864\n"
"v_max_f32 v10, v10, v175\n"
"v_mfma_f32_16x16x32_bf16 a[24:27], v[60:63], v[100:103], a[24:27]\n"
"v_cmp_nge_f32 vcc, s62, v10\n"
"v_mfma_f32_16x16x32_bf16 a[28:31], v[60:63], v[104:107], a[28:31]\n"
"v_mfma_f32_16x16x32_bf16 a[32:35], v[60:63], v[108:111], a[32:35]\n"
"v_mfma_f32_16x16x32_bf16 a[36:39], v[60:63], v[112:115], a[36:39]\n"
"s_cbranch_vccnz R1_15_%=\n"
"R1_15_r_%=:\n"
"ds_read_b64_tr_b16 v[60:61], v27 offset:32768\n"
"v_mfma_f32_16x16x32_bf16 a[40:43], v[64:67], v[96:99], a[40:43]\n"
"ds_read_b64_tr_b16 v[62:63], v27 offset:36864\n"
"v_mfma_f32_16x16x32_bf16 a[44:47], v[64:67], v[100:103], a[44:47]\n"
"v_exp_f32 v52, v156\n"
"v_mfma_f32_16x16x32_bf16 a[48:51], v[64:67], v[104:107], a[48:51]\n"
"v_exp_f32 v53, v157\n"
"v_mfma_f32_16x16x32_bf16 a[52:55], v[64:67], v[108:111], a[52:55]\n"
"v_exp_f32 v54, v158\n"
"v_mfma_f32_16x16x32_bf16 a[56:59], v[64:67], v[112:115], a[56:59]\n"
"ds_read_b64_tr_b16 v[64:65], v28 offset:32768\n"
"v_cvt_pk_bf16_f32 v118, v52, v53\n"
"v_mfma_f32_16x16x32_bf16 a[60:63], v[68:71], v[96:99], a[60:63]\n"
"ds_read_b64_tr_b16 v[66:67], v28 offset:36864\n"
"v_add_f32 v33, v33, v52\n"
"v_mfma_f32_16x16x32_bf16 a[64:67], v[68:71], v[100:103], a[64:67]\n"
"v_exp_f32 v55, v159\n"
"v_mfma_f32_16x16x32_bf16 a[68:71], v[68:71], v[104:107], a[68:71]\n"
"v_exp_f32 v48, v160\n"
"v_mfma_f32_16x16x32_bf16 a[72:75], v[68:71], v[108:111], a[72:75]\n"
"v_exp_f32 v49, v161\n"
"v_mfma_f32_16x16x32_bf16 a[76:79], v[68:71], v[112:115], a[76:79]\n"
"v_exp_f32 v50, v162\n"
"v_mfma_f32_16x16x32_bf16 a[80:83], v[72:75], v[96:99], a[80:83]\n"
"v_exp_f32 v51, v163\n"
"v_mfma_f32_16x16x32_bf16 a[84:87], v[72:75], v[100:103], a[84:87]\n"
"v_exp_f32 v52, v164\n"
"v_mfma_f32_16x16x32_bf16 a[88:91], v[72:75], v[104:107], a[88:91]\n"
"s_waitcnt vmcnt(5) lgkmcnt(0)\n"
"s_barrier\n"
"v_mfma_f32_16x16x32_bf16 a[92:95], v[72:75], v[108:111], a[92:95]\n"
"v_cvt_pk_bf16_f32 v119, v54, v55\n"
"v_cvt_pk_bf16_f32 v122, v48, v49\n"
"v_mfma_f32_16x16x32_bf16 a[96:99], v[72:75], v[112:115], a[96:99]\n"
"v_cvt_pk_bf16_f32 v123, v50, v51\n"
"v_add_f32 v33, v33, v53\n"
"v_mfma_f32_16x16x32_bf16 a[100:103], v[56:59], v[96:99], a[100:103]\n"
"ds_read_b64_tr_b16 v[68:69], v24 offset:16384\n"
"v_add_f32 v33, v33, v54\n"
"v_mfma_f32_16x16x32_bf16 a[104:107], v[56:59], v[100:103], a[104:107]\n"
"ds_read_b64_tr_b16 v[70:71], v24 offset:20480\n"
"v_add_f32 v33, v33, v55\n"
"v_mfma_f32_16x16x32_bf16 a[108:111], v[56:59], v[104:107], a[108:111]\n"
"ds_read_b64_tr_b16 v[72:73], v25 offset:16384\n"
"v_add_f32 v34, v34, v48\n"
"v_mfma_f32_16x16x32_bf16 a[112:115], v[56:59], v[108:111], a[112:115]\n"
"ds_read_b64_tr_b16 v[74:75], v25 offset:20480\n"
"v_add_f32 v34, v34, v49\n"
"v_mfma_f32_16x16x32_bf16 a[116:119], v[56:59], v[112:115], a[116:119]\n"
"v_exp_f32 v53, v165\n"
"v_mfma_f32_16x16x32_bf16 a[120:123], v[60:63], v[96:99], a[120:123]\n"
"ds_read_b64_tr_b16 v[56:57], v21 offset:16384\n"
"v_cvt_pk_bf16_f32 v126, v52, v53\n"
"v_mfma_f32_16x16x32_bf16 a[124:127], v[60:63], v[100:103], a[124:127]\n"
"ds_read_b64_tr_b16 v[58:59], v21 offset:20480\n"
"v_add_f32 v34, v34, v50\n"
"v_mfma_f32_16x16x32_bf16 a[128:131], v[60:63], v[104:107], a[128:131]\n"
"v_exp_f32 v54, v166\n"
"v_mfma_f32_16x16x32_bf16 a[132:135], v[60:63], v[108:111], a[132:135]\n"
"v_exp_f32 v55, v167\n"
"v_mfma_f32_16x16x32_bf16 a[136:139], v[60:63], v[112:115], a[136:139]\n"
"v_exp_f32 v48, v168\n"
"v_mfma_f32_16x16x32_bf16 a[140:143], v[64:67], v[96:99], a[140:143]\n"
"ds_read_b64_tr_b16 v[60:61], v22 offset:16384\n"
"v_cvt_pk_bf16_f32 v127, v54, v55\n"
"v_mfma_f32_16x16x32_bf16 a[144:147], v[64:67], v[100:103], a[144:147]\n"
"ds_read_b64_tr_b16 v[62:63], v22 offset:20480\n"
"v_add_f32 v34, v34, v51\n"
"v_mfma_f32_16x16x32_bf16 a[148:151], v[64:67], v[104:107], a[148:151]\n"
"v_exp_f32 v49, v169\n"
"v_mfma_f32_16x16x32_bf16 a[152:155], v[64:67], v[108:111], a[152:155]\n"
"s_cmp_lg_u32 s56, 0\n"
"v_exp_f32 v50, v170\n"
"v_mfma_f32_16x16x32_bf16 a[156:159], v[64:67], v[112:115], a[156:159]\n"
"s_cbranch_scc1 S1_15_%=\n"
"S1_15_r_%=:\n"
"v_cvt_pk_bf16_f32 v130, v48, v49\n"
"ds_read_b64_tr_b16 v[64:65], v23 offset:16384\n"
"ds_read_b64_tr_b16 v[66:67], v23 offset:20480\n"
"v_add_f32 v35, v35, v52\n"
"v_add_f32 v35, v35, v53\n"
"v_add_f32 v35, v35, v54\n"
"v_add_f32 v35, v35, v55\n"
"v_exp_f32 v51, v171\n"
"s_nop 0\n"
"v_cvt_pk_bf16_f32 v131, v50, v51\n"
"v_add_f32 v36, v36, v48\n"
"v_add_f32 v36, v36, v49\n"
"v_add_f32 v36, v36, v50\n"
"v_add_f32 v36, v36, v51\n"
"v_exp_f32 v52, v172\n"
"v_exp_f32 v53, v173\n"
"v_exp_f32 v54, v174\n"
"v_exp_f32 v55, v175\n"
"v_cvt_pk_bf16_f32 v134, v52, v53\n"
"v_cvt_pk_bf16_f32 v135, v54, v55\n"
"v_add_f32 v37, v37, v52\n"
"v_add_f32 v37, v37, v53\n"
"v_add_f32 v37, v37, v54\n"
"v_add_f32 v37, v37, v55\n"
"ds_write_b128 v12, v[116:119] offset:0\n"
"ds_write_b128 v12, v[120:123] offset:1024\n"
"ds_write_b128 v12, v[124:127] offset:2048\n"
"ds_write_b128 v12, v[128:131] offset:3072\n"
"ds_write_b128 v12, v[132:135] offset:4096\n"
"s_cmp_lg_u32 s56, 0\n"
"s_cbranch_scc1 S2_16_%=\n"
"S2_16_r_%=:\n"
"s_waitcnt lgkmcnt(5)\n"
"v_mfma_f32_16x16x32_bf16 a[0:3], v[56:59], v[116:119], a[0:3]\n"
"s_add_u32 m0, s85, 0\n"
"s_nop 0\n"
"buffer_load_dwordx4 v13, s[80:83], s84 offen lds\n"
"v_mov_b32 v2, v33\n"
"v_mfma_f32_16x16x32_bf16 a[4:7], v[56:59], v[120:123], a[4:7]\n"
"buffer_load_dwordx4 v14, s[80:83], s84 offen offset:1024 lds\n"
"v_mov_b32 v3, v33\n"
"s_nop 1\n"
"v_permlane16_swap_b32 v2, v3\n"
"v_mfma_f32_16x16x32_bf16 a[8:11], v[56:59], v[124:127], a[8:11]\n"
"buffer_load_dwordx4 v15, s[80:83], s84 offen offset:2048 lds\n"
"v_add_f32 v33, v2, v3\n"
"v_mov_b32 v2, v33\n"
"v_mfma_f32_16x16x32_bf16 a[12:15], v[56:59], v[128:131], a[12:15]\n"
"buffer_load_dwordx4 v16, s[80:83], s84 offen offset:3072 lds\n"
"v_mov_b32 v3, v33\n"
"s_nop 1\n"
"v_permlane32_swap_b32 v2, v3\n"
"v_mfma_f32_16x16x32_bf16 a[16:19], v[56:59], v[132:135], a[16:19]\n"
"ds_read_b64_tr_b16 v[56:57], v26 offset:16384\n"
"v_add_f32 v33, v2, v3\n"
"v_mov_b32 v2, v34\n"
"v_mfma_f32_16x16x32_bf16 a[20:23], v[60:63], v[116:119], a[20:23]\n"
"ds_read_b64_tr_b16 v[58:59], v26 offset:20480\n"
"v_mov_b32 v3, v34\n"
"s_nop 1\n"
"v_permlane16_swap_b32 v2, v3\n"
"v_mfma_f32_16x16x32_bf16 a[24:27], v[60:63], v[120:123], a[24:27]\n"
"s_add_u32 m0, s85, 4096\n"
"s_add_u32 s84, s84, 4096\n"
"buffer_load_dwordx4 v17, s[80:83], s84 offen lds\n"
"v_add_f32 v34, v2, v3\n"
"v_mov_b32 v2, v34\n"
"v_mfma_f32_16x16x32_bf16 a[28:31], v[60:63], v[124:127], a[28:31]\n"
"buffer_load_dwordx4 v18, s[80:83], s84 offen offset:1024 lds\n"
"v_mov_b32 v3, v34\n"
"s_nop 1\n"
"v_permlane32_swap_b32 v2, v3\n"
"v_mfma_f32_16x16x32_bf16 a[32:35], v[60:63], v[128:131], a[32:35]\n"
"buffer_load_dwordx4 v19, s[80:83], s84 offen offset:2048 lds\n"
"v_add_f32 v34, v2, v3\n"
"v_mov_b32 v2, v35\n"
"v_mfma_f32_16x16x32_bf16 a[36:39], v[60:63], v[132:135], a[36:39]\n"
"ds_read_b64_tr_b16 v[60:61], v27 offset:16384\n"
"v_mov_b32 v3, v35\n"
"s_nop 1\n"
"v_permlane16_swap_b32 v2, v3\n"
"v_mfma_f32_16x16x32_bf16 a[40:43], v[64:67], v[116:119], a[40:43]\n"
"ds_read_b64_tr_b16 v[62:63], v27 offset:20480\n"
"v_add_f32 v35, v2, v3\n"
"v_mov_b32 v2, v35\n"
"v_mfma_f32_16x16x32_bf16 a[44:47], v[64:67], v[120:123], a[44:47]\n"
"s_add_u32 s84, s84, 3072\n"
"s_add_u32 m0, s85, 7168\n"
"s_nop 0\n"
"buffer_load_dwordx4 v13, s[80:83], s84 offen lds\n"
"v_mov_b32 v3, v35\n"
"v_mfma_f32_16x16x32_bf16 a[48:51], v[64:67], v[124:127], a[48:51]\n"
"buffer_load_dwordx4 v14, s[80:83], s84 offen offset:1024 lds\n"
"v_permlane32_swap_b32 v2, v3\n"
"v_add_f32 v35, v2, v3\n"
"v_mfma_f32_16x16x32_bf16 a[52:55], v[64:67], v[128:131], a[52:55]\n"
"buffer_load_dwordx4 v15, s[80:83], s84 offen offset:2048 lds\n"
"v_mov_b32 v2, v36\n"
"v_mov_b32 v3, v36\n"
"v_mfma_f32_16x16x32_bf16 a[56:59], v[64:67], v[132:135], a[56:59]\n"
"ds_read_b64_tr_b16 v[64:65], v28 offset:16384\n"
"v_permlane16_swap_b32 v2, v3\n"
"v_add_f32 v36, v2, v3\n"
"v_mfma_f32_16x16x32_bf16 a[60:63], v[68:71], v[116:119], a[60:63]\n"
"ds_read_b64_tr_b16 v[66:67], v28 offset:20480\n"
"v_mov_b32 v2, v36\n"
"v_mov_b32 v3, v36\n"
"v_mfma_f32_16x16x32_bf16 a[64:67], v[68:71], v[120:123], a[64:67]\n"
"buffer_load_dwordx4 v16, s[80:83], s84 offen offset:3072 lds\n"
"v_permlane32_swap_b32 v2, v3\n"
"v_add_f32 v36, v2, v3\n"
"v_mfma_f32_16x16x32_bf16 a[68:71], v[68:71], v[124:127], a[68:71]\n"
"s_add_u32 m0, s85, 11264\n"
"s_add_u32 s84, s84, 4096\n"
"buffer_load_dwordx4 v17, s[80:83], s84 offen lds\n"
"v_mov_b32 v2, v37\n"
"v_mov_b32 v3, v37\n"
"v_mfma_f32_16x16x32_bf16 a[72:75], v[68:71], v[128:131], a[72:75]\n"
"buffer_load_dwordx4 v18, s[80:83], s84 offen offset:1024 lds\n"
"v_permlane16_swap_b32 v2, v3\n"
"v_add_f32 v37, v2, v3\n"
"v_mfma_f32_16x16x32_bf16 a[76:79], v[68:71], v[132:135], a[76:79]\n"
"buffer_load_dwordx4 v19, s[80:83], s84 offen offset:2048 lds\n"
"s_add_u32 s84, s84, 46080\n"
"v_mov_b32 v2, v37\n"
"v_mov_b32 v3, v37\n"
"v_mfma_f32_16x16x32_bf16 a[80:83], v[72:75], v[116:119], a[80:83]\n"
"s_waitcnt vmcnt(15) lgkmcnt(0)\n"
"s_barrier\n"
"v_mfma_f32_16x16x32_bf16 a[84:87], v[72:75], v[120:123], a[84:87]\n"
"ds_read_b32 v9, v7 offset:1280\n"
"ds_read_b128 v[96:99], v11 offset:0\n"
"s_add_u32 m0, s85, 57344\n"
"s_nop 0\n"
"buffer_load_dwordx4 v13, s[80:83], s84 offen lds\n"
"v_mfma_f32_16x16x32_bf16 a[88:91], v[72:75], v[124:127], a[88:91]\n"
"ds_read_b128 v[100:103], v11 offset:1024\n"
"buffer_load_dwordx4 v14, s[80:83], s84 offen offset:1024 lds\n"
"v_permlane32_swap_b32 v2, v3\n"
"v_mfma_f32_16x16x32_bf16 a[92:95], v[72:75], v[128:131], a[92:95]\n"
"ds_read_b128 v[104:107], v11 offset:2048\n"
"buffer_load_dwordx4 v15, s[80:83], s84 offen offset:2048 lds\n"
"v_add_f32 v37, v2, v3\n"
"v_mfma_f32_16x16x32_bf16 a[96:99], v[72:75], v[132:135], a[96:99]\n"
"ds_read_b128 v[108:111], v11 offset:3072\n"
"buffer_load_dwordx4 v16, s[80:83], s84 offen offset:3072 lds\n"
"v_min_u32 v224, 19, v0\n"
"v_mfma_f32_16x16x32_bf16 a[100:103], v[56:59], v[116:119], a[100:103]\n"
"ds_read_b128 v[112:115], v11 offset:4096\n"
"s_add_u32 m0, s85, 61440\n"
"s_add_u32 s84, s84, 4096\n"
"buffer_load_dwordx4 v17, s[80:83], s84 offen lds\n"
"v_lshlrev_b32 v224, 2, v224\n"
"v_mfma_f32_16x16x32_bf16 a[104:107], v[56:59], v[120:123], a[104:107]\n"
"ds_read_b64_tr_b16 v[68:69], v24 offset:49152\n"
"buffer_load_dwordx4 v18, s[80:83], s84 offen offset:1024 lds\n"
"global_load_dword v225, v224, s[24:25] offset:128 sc1\n"
"v_mfma_f32_16x16x32_bf16 a[108:111], v[56:59], v[124:127], a[108:111]\n"
"ds_read_b64_tr_b16 v[70:71], v24 offset:53248\n"
"buffer_load_dwordx4 v19, s[80:83], s84 offen offset:2048 lds\n"
"v_mfma_f32_16x16x32_bf16 a[112:115], v[56:59], v[128:131], a[112:115]\n"
"ds_read_b64_tr_b16 v[72:73], v25 offset:49152\n"
"s_add_u32 s84, s84, 3072\n"
"s_add_u32 m0, s85, 64512\n"
"s_nop 0\n"
"buffer_load_dwordx4 v13, s[80:83], s84 offen lds\n"
"v_mfma_f32_16x16x32_bf16 a[116:119], v[56:59], v[132:135], a[116:119]\n"
"ds_read_b64_tr_b16 v[74:75], v25 offset:53248\n"
"buffer_load_dwordx4 v14, s[80:83], s84 offen offset:1024 lds\n"
"v_mfma_f32_16x16x32_bf16 a[120:123], v[60:63], v[116:119], a[120:123]\n"
"ds_read_b64_tr_b16 v[56:57], v21 offset:49152\n"
"buffer_load_dwordx4 v15, s[80:83], s84 offen offset:2048 lds\n"
"v_mfma_f32_16x16x32_bf16 a[124:127], v[60:63], v[120:123], a[124:127]\n"
"ds_read_b64_tr_b16 v[58:59], v21 offset:53248\n"
"buffer_load_dwordx4 v16, s[80:83], s84 offen offset:3072 lds\n"
"v_mfma_f32_16x16x32_bf16 a[128:131], v[60:63], v[124:127], a[128:131]\n"
"s_add_u32 m0, s85, 68608\n"
"s_add_u32 s84, s84, 4096\n"
"buffer_load_dwordx4 v17, s[80:83], s84 offen lds\n"
"v_mfma_f32_16x16x32_bf16 a[132:135], v[60:63], v[128:131], a[132:135]\n"
"s_waitcnt lgkmcnt(3)\n"
"buffer_load_dwordx4 v18, s[80:83], s84 offen offset:1024 lds\n"
"v_readfirstlane_b32 s59, v9\n"
"v_mfma_f32_16x16x32_bf16 a[136:139], v[60:63], v[132:135], a[136:139]\n"
"buffer_load_dwordx4 v19, s[80:83], s84 offen offset:2048 lds\n"
"v_mfma_f32_16x16x32_bf16 a[140:143], v[64:67], v[116:119], a[140:143]\n"
"ds_read_b64_tr_b16 v[60:61], v22 offset:49152\n"
"s_add_u32 s84, s84, 3072\n"
"v_mfma_f32_16x16x32_bf16 a[144:147], v[64:67], v[120:123], a[144:147]\n"
"ds_read_b64_tr_b16 v[62:63], v22 offset:53248\n"
"s_cmp_lg_u32 s59, s58\n"
"s_cselect_b32 s60, 3, 0\n"
"s_max_u32 s56, s56, s60\n"
"v_mfma_f32_16x16x32_bf16 a[148:151], v[64:67], v[124:127], a[148:151]\n"
"v_mfma_f32_16x16x32_bf16 a[152:155], v[64:67], v[128:131], a[152:155]\n"
"v_mfma_f32_16x16x32_bf16 a[156:159], v[64:67], v[132:135], a[156:159]\n"
"ds_read_b64_tr_b16 v[64:65], v23 offset:49152\n"
"ds_read_b64_tr_b16 v[66:67], v23 offset:53248\n"
"s_cmp_lg_u32 s56, 0\n"
"s_cbranch_scc1 S3_16_%=\n"
"S3_16_r_%=:\n"
"s_lshr_b32 s60, s70, 1\n"
"s_cmp_eq_u32 s60, s96\n"
"s_cbranch_scc1 RECV_%=\n"
"s_waitcnt lgkmcnt(2)\n"
"v_mfma_f32_16x16x32_bf16 a[0:3], v[56:59], v[96:99], a[0:3]\n"
"s_waitcnt vmcnt(29)\n"
"v_add_f32 v4, v76, v43\n"
"v_exp_f32 v4, -v4\n"
"v_mov_b32 v6, v43\n"
"v_mul_f32 v7, v33, v4\n"
"v_mfma_f32_16x16x32_bf16 a[4:7], v[56:59], v[100:103], a[4:7]\n"
"v_add_f32 v4, v80, v44\n"
"v_exp_f32 v4, -v4\n"
"v_mov_b32 v8, v44\n"
"v_mul_f32 v9, v34, v4\n"
"v_mfma_f32_16x16x32_bf16 a[8:11], v[56:59], v[104:107], a[8:11]\n"
"v_add_f32 v4, v84, v45\n"
"v_exp_f32 v4, -v4\n"
"v_mov_b32 v10, v45\n"
"v_mul_f32 v11, v35, v4\n"
"v_mfma_f32_16x16x32_bf16 a[12:15], v[56:59], v[108:111], a[12:15]\n"
"v_add_f32 v4, v88, v46\n"
"v_exp_f32 v4, -v4\n"
"v_mov_b32 v22, v46\n"
"v_mul_f32 v23, v36, v4\n"
"v_mfma_f32_16x16x32_bf16 a[16:19], v[56:59], v[112:115], a[16:19]\n"
"v_add_f32 v4, v92, v47\n"
"v_exp_f32 v4, -v4\n"
"v_mov_b32 v24, v47\n"
"v_mul_f32 v25, v37, v4\n"
"ds_read_b64_tr_b16 v[56:57], v26 offset:49152\n"
"v_mfma_f32_16x16x32_bf16 a[20:23], v[60:63], v[96:99], a[20:23]\n"
"v_and_b32 v4, 15, v0\n"
"v_lshlrev_b32 v4, 3, v4\n"
"ds_read_b64_tr_b16 v[58:59], v26 offset:53248\n"
"v_mfma_f32_16x16x32_bf16 a[24:27], v[60:63], v[100:103], a[24:27]\n"
"s_and_b32 s60, s70, 1\n"
"s_lshl_b32 s60, s60, 10\n"
"s_add_u32 s60, s60, 40960\n"
"v_add_u32 v1, s60, v4\n"
"global_store_dwordx2 v1, v[6:7], s[16:17] offset:0 sc1\n"
"global_store_dwordx2 v1, v[8:9], s[16:17] offset:128 sc1\n"
"global_store_dwordx2 v1, v[10:11], s[16:17] offset:256 sc1\n"
"v_mfma_f32_16x16x32_bf16 a[28:31], v[60:63], v[104:107], a[28:31]\n"
"global_store_dwordx2 v1, v[22:23], s[16:17] offset:384 sc1\n"
"global_store_dwordx2 v1, v[24:25], s[16:17] offset:512 sc1\n"
"v_lshlrev_b32 v21, 4, v0\n"
"v_mfma_f32_16x16x32_bf16 a[32:35], v[60:63], v[108:111], a[32:35]\n"
"s_and_b32 s60, s70, 1\n"
"s_mul_i32 s60, s60, 20480\n"
"v_add_u32 v5, s60, v21\n"
"v_mfma_f32_16x16x32_bf16 a[36:39], v[60:63], v[112:115], a[36:39]\n"
"s_waitcnt lgkmcnt(0)\n"
"v_accvgpr_read_b32 v136, a0\n"
"v_accvgpr_read_b32 v137, a1\n"
"v_accvgpr_read_b32 v138, a2\n"
"v_accvgpr_read_b32 v139, a3\n"
"ds_read_b64_tr_b16 v[60:61], v27 offset:49152\n"
"v_mfma_f32_16x16x32_bf16 a[40:43], v[64:67], v[96:99], a[40:43]\n"
"v_accvgpr_read_b32 v140, a20\n"
"v_accvgpr_read_b32 v141, a21\n"
"v_accvgpr_read_b32 v142, a22\n"
"v_accvgpr_read_b32 v143, a23\n"
"ds_read_b64_tr_b16 v[62:63], v27 offset:53248\n"
"v_mfma_f32_16x16x32_bf16 a[44:47], v[64:67], v[100:103], a[44:47]\n"
"v_cvt_pk_bf16_f32 v176, v136, v137\n"
"v_cvt_pk_bf16_f32 v177, v138, v139\n"
"v_cvt_pk_bf16_f32 v178, v140, v141\n"
"v_cvt_pk_bf16_f32 v179, v142, v143\n"
"v_mfma_f32_16x16x32_bf16 a[48:51], v[64:67], v[104:107], a[48:51]\n"
"global_store_dwordx4 v5, v[176:179], s[16:17] offset:0 sc1\n"
"v_accvgpr_read_b32 v144, a4\n"
"v_accvgpr_read_b32 v145, a5\n"
"v_accvgpr_read_b32 v146, a6\n"
"v_mfma_f32_16x16x32_bf16 a[52:55], v[64:67], v[108:111], a[52:55]\n"
"v_accvgpr_read_b32 v147, a7\n"
"v_accvgpr_read_b32 v148, a24\n"
"v_accvgpr_read_b32 v149, a25\n"
"v_accvgpr_read_b32 v150, a26\n"
"v_mfma_f32_16x16x32_bf16 a[56:59], v[64:67], v[112:115], a[56:59]\n"
"v_accvgpr_read_b32 v151, a27\n"
"v_cvt_pk_bf16_f32 v180, v144, v145\n"
"v_cvt_pk_bf16_f32 v181, v146, v147\n"
"v_cvt_pk_bf16_f32 v182, v148, v149\n"
"ds_read_b64_tr_b16 v[64:65], v28 offset:49152\n"
"v_mfma_f32_16x16x32_bf16 a[60:63], v[68:71], v[96:99], a[60:63]\n"
"v_cvt_pk_bf16_f32 v183, v150, v151\n"
"global_store_dwordx4 v5, v[180:183], s[16:17] offset:1024 sc1\n"
"v_accvgpr_read_b32 v136, a8\n"
"v_accvgpr_read_b32 v137, a9\n"
"ds_read_b64_tr_b16 v[66:67], v28 offset:53248\n"
"v_mfma_f32_16x16x32_bf16 a[64:67], v[68:71], v[100:103], a[64:67]\n"
"v_accvgpr_read_b32 v138, a10\n"
"v_accvgpr_read_b32 v139, a11\n"
"v_accvgpr_read_b32 v140, a28\n"
"v_accvgpr_read_b32 v141, a29\n"
"v_mfma_f32_16x16x32_bf16 a[68:71], v[68:71], v[104:107], a[68:71]\n"
"v_accvgpr_read_b32 v142, a30\n"
"v_accvgpr_read_b32 v143, a31\n"
"v_cvt_pk_bf16_f32 v184, v136, v137\n"
"v_cvt_pk_bf16_f32 v185, v138, v139\n"
"v_mfma_f32_16x16x32_bf16 a[72:75], v[68:71], v[108:111], a[72:75]\n"
"v_cvt_pk_bf16_f32 v186, v140, v141\n"
"v_cvt_pk_bf16_f32 v187, v142, v143\n"
"global_store_dwordx4 v5, v[184:187], s[16:17] offset:2048 sc1\n"
"v_accvgpr_read_b32 v144, a12\n"
"v_mfma_f32_16x16x32_bf16 a[76:79], v[68:71], v[112:115], a[76:79]\n"
"v_accvgpr_read_b32 v145, a13\n"
"v_accvgpr_read_b32 v146, a14\n"
"v_accvgpr_read_b32 v147, a15\n"
"v_accvgpr_read_b32 v148, a32\n"
"v_mfma_f32_16x16x32_bf16 a[80:83], v[72:75], v[96:99], a[80:83]\n"
"v_accvgpr_read_b32 v149, a33\n"
"v_accvgpr_read_b32 v150, a34\n"
"v_accvgpr_read_b32 v151, a35\n"
"v_cvt_pk_bf16_f32 v188, v144, v145\n"
"v_mfma_f32_16x16x32_bf16 a[84:87], v[72:75], v[100:103], a[84:87]\n"
"v_cvt_pk_bf16_f32 v189, v146, v147\n"
"v_cvt_pk_bf16_f32 v190, v148, v149\n"
"v_cvt_pk_bf16_f32 v191, v150, v151\n"
"global_store_dwordx4 v5, v[188:191], s[16:17] offset:3072 sc1\n"
"v_mfma_f32_16x16x32_bf16 a[88:91], v[72:75], v[104:107], a[88:91]\n"
"v_accvgpr_read_b32 v136, a16\n"
"v_accvgpr_read_b32 v137, a17\n"
"v_accvgpr_read_b32 v138, a18\n"
"v_accvgpr_read_b32 v139, a19\n"
"v_mfma_f32_16x16x32_bf16 a[92:95], v[72:75], v[108:111], a[92:95]\n"
"v_accvgpr_read_b32 v140, a36\n"
"v_accvgpr_read_b32 v141, a37\n"
"v_accvgpr_read_b32 v142, a38\n"
"v_accvgpr_read_b32 v143, a39\n"
"v_mfma_f32_16x16x32_bf16 a[96:99], v[72:75], v[112:115], a[96:99]\n"
"v_cvt_pk_bf16_f32 v192, v136, v137\n"
"v_cvt_pk_bf16_f32 v193, v138, v139\n"
"v_cvt_pk_bf16_f32 v194, v140, v141\n"
"v_cvt_pk_bf16_f32 v195, v142, v143\n"
"v_mfma_f32_16x16x32_bf16 a[100:103], v[56:59], v[96:99], a[100:103]\n"
"v_add_u32 v5, 4096, v5\n"
"global_store_dwordx4 v5, v[192:195], s[16:17] offset:0 sc1\n"
"v_accvgpr_read_b32 v144, a40\n"
"v_accvgpr_read_b32 v145, a41\n"
"v_mfma_f32_16x16x32_bf16 a[104:107], v[56:59], v[100:103], a[104:107]\n"
"v_accvgpr_read_b32 v146, a42\n"
"v_accvgpr_read_b32 v147, a43\n"
"v_accvgpr_read_b32 v148, a60\n"
"v_accvgpr_read_b32 v149, a61\n"
"v_mfma_f32_16x16x32_bf16 a[108:111], v[56:59], v[104:107], a[108:111]\n"
"v_accvgpr_read_b32 v150, a62\n"
"v_accvgpr_read_b32 v151, a63\n"
"v_cvt_pk_bf16_f32 v196, v144, v145\n"
"v_cvt_pk_bf16_f32 v197, v146, v147\n"
"v_mfma_f32_16x16x32_bf16 a[112:115], v[56:59], v[108:111], a[112:115]\n"
"s_waitcnt vmcnt(10) lgkmcnt(0)\n"
"v_cvt_pk_bf16_f32 v198, v148, v149\n"
"v_cvt_pk_bf16_f32 v199, v150, v151\n"
"global_store_dwordx4 v5, v[196:199], s[16:17] offset:1024 sc1\n"
"v_accvgpr_read_b32 v136, a44\n"
"s_barrier\n"
"v_mfma_f32_16x16x32_bf16 a[116:119], v[56:59], v[112:115], a[116:119]\n"
"v_accvgpr_read_b32 v137, a45\n"
"v_accvgpr_read_b32 v138, a46\n"
"v_accvgpr_read_b32 v139, a47\n"
"v_accvgpr_read_b32 v140, a64\n"
"v_mfma_f32_16x16x32_bf16 a[120:123], v[60:63], v[96:99], a[120:123]\n"
"v_accvgpr_read_b32 v141, a65\n"
"v_accvgpr_read_b32 v142, a66\n"
"v_accvgpr_read_b32 v143, a67\n"
"v_cvt_pk_bf16_f32 v200, v136, v137\n"
"v_mfma_f32_16x16x32_bf16 a[124:127], v[60:63], v[100:103], a[124:127]\n"
"v_cvt_pk_bf16_f32 v201, v138, v139\n"
"v_cvt_pk_bf16_f32 v202, v140, v141\n"
"v_cvt_pk_bf16_f32 v203, v142, v143\n"
"global_store_dwordx4 v5, v[200:203], s[16:17] offset:2048 sc1\n"
"v_mfma_f32_16x16x32_bf16 a[128:131], v[60:63], v[104:107], a[128:131]\n"
"v_accvgpr_read_b32 v144, a48\n"
"v_accvgpr_read_b32 v145, a49\n"
"v_accvgpr_read_b32 v146, a50\n"
"v_accvgpr_read_b32 v147, a51\n"
"v_mfma_f32_16x16x32_bf16 a[132:135], v[60:63], v[108:111], a[132:135]\n"
"v_accvgpr_read_b32 v148, a68\n"
"v_accvgpr_read_b32 v149, a69\n"
"v_accvgpr_read_b32 v150, a70\n"
"v_accvgpr_read_b32 v151, a71\n"
"v_mfma_f32_16x16x32_bf16 a[136:139], v[60:63], v[112:115], a[136:139]\n"
"v_cvt_pk_bf16_f32 v204, v144, v145\n"
"v_cvt_pk_bf16_f32 v205, v146, v147\n"
"v_cvt_pk_bf16_f32 v206, v148, v149\n"
"v_cvt_pk_bf16_f32 v207, v150, v151\n"
"v_mfma_f32_16x16x32_bf16 a[140:143], v[64:67], v[96:99], a[140:143]\n"
"global_store_dwordx4 v5, v[204:207], s[16:17] offset:3072 sc1\n"
"v_accvgpr_read_b32 v136, a52\n"
"v_accvgpr_read_b32 v137, a53\n"
"v_accvgpr_read_b32 v138, a54\n"
"v_mfma_f32_16x16x32_bf16 a[144:147], v[64:67], v[100:103], a[144:147]\n"
"v_accvgpr_read_b32 v139, a55\n"
"v_accvgpr_read_b32 v140, a72\n"
"v_accvgpr_read_b32 v141, a73\n"
"v_accvgpr_read_b32 v142, a74\n"
"v_mfma_f32_16x16x32_bf16 a[148:151], v[64:67], v[104:107], a[148:151]\n"
"v_accvgpr_read_b32 v143, a75\n"
"v_cvt_pk_bf16_f32 v176, v136, v137\n"
"v_cvt_pk_bf16_f32 v177, v138, v139\n"
"v_cvt_pk_bf16_f32 v178, v140, v141\n"
"v_mfma_f32_16x16x32_bf16 a[152:155], v[64:67], v[108:111], a[152:155]\n"
"v_cvt_pk_bf16_f32 v179, v142, v143\n"
"v_add_u32 v5, 4096, v5\n"
"global_store_dwordx4 v5, v[176:179], s[16:17] offset:0 sc1\n"
"v_accvgpr_read_b32 v144, a56\n"
"v_mfma_f32_16x16x32_bf16 a[156:159], v[64:67], v[112:115], a[156:159]\n"
"v_accvgpr_read_b32 v145, a57\n"
"v_accvgpr_read_b32 v146, a58\n"
"v_accvgpr_read_b32 v147, a59\n"
"v_accvgpr_read_b32 v148, a76\n"
"s_nop 3\n"
"v_accvgpr_read_b32 v149, a77\n"
"v_accvgpr_read_b32 v150, a78\n"
"v_accvgpr_read_b32 v151, a79\n"
"v_cvt_pk_bf16_f32 v180, v144, v145\n"
"v_cvt_pk_bf16_f32 v181, v146, v147\n"
"v_cvt_pk_bf16_f32 v182, v148, v149\n"
"v_cvt_pk_bf16_f32 v183, v150, v151\n"
"global_store_dwordx4 v5, v[180:183], s[16:17] offset:1024 sc1\n"
"v_accvgpr_read_b32 v136, a80\n"
"v_accvgpr_read_b32 v137, a81\n"
"v_accvgpr_read_b32 v138, a82\n"
"v_accvgpr_read_b32 v139, a83\n"
"v_accvgpr_read_b32 v140, a100\n"
"v_accvgpr_read_b32 v141, a101\n"
"v_accvgpr_read_b32 v142, a102\n"
"v_accvgpr_read_b32 v143, a103\n"
"v_cvt_pk_bf16_f32 v184, v136, v137\n"
"v_cvt_pk_bf16_f32 v185, v138, v139\n"
"v_cvt_pk_bf16_f32 v186, v140, v141\n"
"v_cvt_pk_bf16_f32 v187, v142, v143\n"
"global_store_dwordx4 v5, v[184:187], s[16:17] offset:2048 sc1\n"
"v_accvgpr_read_b32 v144, a84\n"
"v_accvgpr_read_b32 v145, a85\n"
"v_accvgpr_read_b32 v146, a86\n"
"v_accvgpr_read_b32 v147, a87\n"
"v_accvgpr_read_b32 v148, a104\n"
"v_accvgpr_read_b32 v149, a105\n"
"v_accvgpr_read_b32 v150, a106\n"
"v_accvgpr_read_b32 v151, a107\n"
"v_cvt_pk_bf16_f32 v188, v144, v145\n"
"v_cvt_pk_bf16_f32 v189, v146, v147\n"
"v_cvt_pk_bf16_f32 v190, v148, v149\n"
"v_cvt_pk_bf16_f32 v191, v150, v151\n"
"global_store_dwordx4 v5, v[188:191], s[16:17] offset:3072 sc1\n"
"v_accvgpr_read_b32 v136, a88\n"
"v_accvgpr_read_b32 v137, a89\n"
"v_accvgpr_read_b32 v138, a90\n"
"v_accvgpr_read_b32 v139, a91\n"
"v_accvgpr_read_b32 v140, a108\n"
"v_accvgpr_read_b32 v141, a109\n"
"v_accvgpr_read_b32 v142, a110\n"
"v_accvgpr_read_b32 v143, a111\n"
"v_cvt_pk_bf16_f32 v192, v136, v137\n"
"v_cvt_pk_bf16_f32 v193, v138, v139\n"
"v_cvt_pk_bf16_f32 v194, v140, v141\n"
"v_cvt_pk_bf16_f32 v195, v142, v143\n"
"v_add_u32 v5, 4096, v5\n"
"global_store_dwordx4 v5, v[192:195], s[16:17] offset:0 sc1\n"
"v_accvgpr_read_b32 v144, a92\n"
"v_accvgpr_read_b32 v145, a93\n"
"v_accvgpr_read_b32 v146, a94\n"
"v_accvgpr_read_b32 v147, a95\n"
"v_accvgpr_read_b32 v148, a112\n"
"v_accvgpr_read_b32 v149, a113\n"
"v_accvgpr_read_b32 v150, a114\n"
"v_accvgpr_read_b32 v151, a115\n"
"v_cvt_pk_bf16_f32 v196, v144, v145\n"
"v_cvt_pk_bf16_f32 v197, v146, v147\n"
"v_cvt_pk_bf16_f32 v198, v148, v149\n"
"v_cvt_pk_bf16_f32 v199, v150, v151\n"
"global_store_dwordx4 v5, v[196:199], s[16:17] offset:1024 sc1\n"
"v_accvgpr_read_b32 v136, a96\n"
"v_accvgpr_read_b32 v137, a97\n"
"v_accvgpr_read_b32 v138, a98\n"
"v_accvgpr_read_b32 v139, a99\n"
"v_accvgpr_read_b32 v140, a116\n"
"v_accvgpr_read_b32 v141, a117\n"
"v_accvgpr_read_b32 v142, a118\n"
"v_accvgpr_read_b32 v143, a119\n"
"v_cvt_pk_bf16_f32 v200, v136, v137\n"
"v_cvt_pk_bf16_f32 v201, v138, v139\n"
"v_cvt_pk_bf16_f32 v202, v140, v141\n"
"v_cvt_pk_bf16_f32 v203, v142, v143\n"
"global_store_dwordx4 v5, v[200:203], s[16:17] offset:2048 sc1\n"
"v_accvgpr_read_b32 v144, a120\n"
"v_accvgpr_read_b32 v145, a121\n"
"v_accvgpr_read_b32 v146, a122\n"
"v_accvgpr_read_b32 v147, a123\n"
"v_accvgpr_read_b32 v148, a140\n"
"v_accvgpr_read_b32 v149, a141\n"
"v_accvgpr_read_b32 v150, a142\n"
"v_accvgpr_read_b32 v151, a143\n"
"v_cvt_pk_bf16_f32 v204, v144, v145\n"
"v_cvt_pk_bf16_f32 v205, v146, v147\n"
"v_cvt_pk_bf16_f32 v206, v148, v149\n"
"v_cvt_pk_bf16_f32 v207, v150, v151\n"
"global_store_dwordx4 v5, v[204:207], s[16:17] offset:3072 sc1\n"
"v_accvgpr_read_b32 v136, a124\n"
"v_accvgpr_read_b32 v137, a125\n"
"v_accvgpr_read_b32 v138, a126\n"
"v_accvgpr_read_b32 v139, a127\n"
"v_accvgpr_read_b32 v140, a144\n"
"v_accvgpr_read_b32 v141, a145\n"
"v_accvgpr_read_b32 v142, a146\n"
"v_accvgpr_read_b32 v143, a147\n"
"v_cvt_pk_bf16_f32 v176, v136, v137\n"
"v_cvt_pk_bf16_f32 v177, v138, v139\n"
"v_cvt_pk_bf16_f32 v178, v140, v141\n"
"v_cvt_pk_bf16_f32 v179, v142, v143\n"
"v_add_u32 v5, 4096, v5\n"
"global_store_dwordx4 v5, v[176:179], s[16:17] offset:0 sc1\n"
"v_accvgpr_read_b32 v144, a128\n"
"v_accvgpr_read_b32 v145, a129\n"
"v_accvgpr_read_b32 v146, a130\n"
"v_accvgpr_read_b32 v147, a131\n"
"v_accvgpr_read_b32 v148, a148\n"
"v_accvgpr_read_b32 v149, a149\n"
"v_accvgpr_read_b32 v150, a150\n"
"v_accvgpr_read_b32 v151, a151\n"
"v_cvt_pk_bf16_f32 v180, v144, v145\n"
"v_cvt_pk_bf16_f32 v181, v146, v147\n"
"v_cvt_pk_bf16_f32 v182, v148, v149\n"
"v_cvt_pk_bf16_f32 v183, v150, v151\n"
"global_store_dwordx4 v5, v[180:183], s[16:17] offset:1024 sc1\n"
"v_accvgpr_read_b32 v136, a132\n"
"v_accvgpr_read_b32 v137, a133\n"
"v_accvgpr_read_b32 v138, a134\n"
"v_accvgpr_read_b32 v139, a135\n"
"v_accvgpr_read_b32 v140, a152\n"
"v_accvgpr_read_b32 v141, a153\n"
"v_accvgpr_read_b32 v142, a154\n"
"v_accvgpr_read_b32 v143, a155\n"
"v_cvt_pk_bf16_f32 v184, v136, v137\n"
"v_cvt_pk_bf16_f32 v185, v138, v139\n"
"v_cvt_pk_bf16_f32 v186, v140, v141\n"
"v_cvt_pk_bf16_f32 v187, v142, v143\n"
"global_store_dwordx4 v5, v[184:187], s[16:17] offset:2048 sc1\n"
"v_accvgpr_read_b32 v144, a136\n"
"v_accvgpr_read_b32 v145, a137\n"
"v_accvgpr_read_b32 v146, a138\n"
"v_accvgpr_read_b32 v147, a139\n"
"v_accvgpr_read_b32 v148, a156\n"
"v_accvgpr_read_b32 v149, a157\n"
"v_accvgpr_read_b32 v150, a158\n"
"v_accvgpr_read_b32 v151, a159\n"
"v_cvt_pk_bf16_f32 v188, v144, v145\n"
"v_cvt_pk_bf16_f32 v189, v146, v147\n"
"v_cvt_pk_bf16_f32 v190, v148, v149\n"
"v_cvt_pk_bf16_f32 v191, v150, v151\n"
"global_store_dwordx4 v5, v[188:191], s[16:17] offset:3072 sc1\n"
"v_lshrrev_b32 v2, 2, v0\n"
"v_mul_u32_u24 v2, 896, v2\n"
"v_and_b32 v3, 3, v0\n"
"v_lshl_add_u32 v2, v3, 3, v2\n"
"v_bfe_u32 v3, v0, 3, 2\n"
"s_mul_i32 s60, s70, 7\n"
"v_xor_b32 v156, s60, v3\n"
"v_lshl_add_u32 v156, v156, 5, v2\n"
"s_add_u32 s60, s60, 1\n"
"v_xor_b32 v157, s60, v3\n"
"v_lshl_add_u32 v157, v157, 5, v2\n"
"s_add_u32 s60, s60, 1\n"
"v_xor_b32 v158, s60, v3\n"
"v_lshl_add_u32 v158, v158, 5, v2\n"
"s_add_u32 s60, s60, 1\n"
"v_xor_b32 v159, s60, v3\n"
"v_lshl_add_u32 v159, v159, 5, v2\n"
"s_add_u32 s60, s60, 1\n"
"v_xor_b32 v160, s60, v3\n"
"v_lshl_add_u32 v160, v160, 5, v2\n"
"s_add_u32 s60, s60, 1\n"
"v_xor_b32 v161, s60, v3\n"
"v_lshl_add_u32 v161, v161, 5, v2\n"
"s_add_u32 s60, s60, 1\n"
"v_xor_b32 v162, s60, v3\n"
"v_lshl_add_u32 v162, v162, 5, v2\n"
"s_add_u32 s60, s60, 1\n"
"ds_read_b64_tr_b16 a[160:161], v156 offset:0\n"
"ds_read_b64_tr_b16 a[162:163], v156 offset:14336\n"
"ds_read_b64_tr_b16 a[164:165], v156 offset:28672\n"
"ds_read_b64_tr_b16 a[166:167], v156 offset:43008\n"
"ds_read_b64_tr_b16 a[176:177], v157 offset:0\n"
"ds_read_b64_tr_b16 a[178:179], v157 offset:14336\n"
"ds_read_b64_tr_b16 a[180:181], v157 offset:28672\n"
"ds_read_b64_tr_b16 a[182:183], v157 offset:43008\n"
"ds_read_b64_tr_b16 a[192:193], v158 offset:0\n"
"ds_read_b64_tr_b16 a[194:195], v158 offset:14336\n"
"ds_read_b64_tr_b16 a[196:197], v158 offset:28672\n"
"ds_read_b64_tr_b16 a[198:199], v158 offset:43008\n"
"ds_read_b64_tr_b16 a[208:209], v159 offset:0\n"
"ds_read_b64_tr_b16 a[210:211], v159 offset:14336\n"
"ds_read_b64_tr_b16 a[212:213], v159 offset:28672\n"
"ds_read_b64_tr_b16 a[214:215], v159 offset:43008\n"
"ds_read_b64_tr_b16 a[224:225], v160 offset:0\n"
"ds_read_b64_tr_b16 a[226:227], v160 offset:14336\n"
"ds_read_b64_tr_b16 a[228:229], v160 offset:28672\n"
"ds_read_b64_tr_b16 a[230:231], v160 offset:43008\n"
"ds_read_b64_tr_b16 a[240:241], v161 offset:0\n"
"ds_read_b64_tr_b16 a[242:243], v161 offset:14336\n"
"ds_read_b64_tr_b16 a[244:245], v161 offset:28672\n"
"ds_read_b64_tr_b16 a[246:247], v161 offset:43008\n"
"ds_read_b64_tr_b16 v[32:33], v162 offset:0\n"
"ds_read_b64_tr_b16 v[34:35], v162 offset:14336\n"
"ds_read_b64_tr_b16 v[36:37], v162 offset:28672\n"
"ds_read_b64_tr_b16 v[38:39], v162 offset:43008\n"
"v_add_u32 v156, 57344, v156\n"
"v_add_u32 v157, 57344, v157\n"
"v_add_u32 v158, 57344, v158\n"
"v_add_u32 v159, 57344, v159\n"
"v_add_u32 v160, 57344, v160\n"
"v_add_u32 v161, 57344, v161\n"
"v_add_u32 v162, 57344, v162\n"
"ds_read_b64_tr_b16 a[168:169], v156 offset:0\n"
"ds_read_b64_tr_b16 a[170:171], v156 offset:14336\n"
"ds_read_b64_tr_b16 a[172:173], v156 offset:28672\n"
"ds_read_b64_tr_b16 a[174:175], v156 offset:43008\n"
"ds_read_b64_tr_b16 a[184:185], v157 offset:0\n"
"ds_read_b64_tr_b16 a[186:187], v157 offset:14336\n"
"ds_read_b64_tr_b16 a[188:189], v157 offset:28672\n"
"ds_read_b64_tr_b16 a[190:191], v157 offset:43008\n"
"ds_read_b64_tr_b16 a[200:201], v158 offset:0\n"
"ds_read_b64_tr_b16 a[202:203], v158 offset:14336\n"
"ds_read_b64_tr_b16 a[204:205], v158 offset:28672\n"
"ds_read_b64_tr_b16 a[206:207], v158 offset:43008\n"
"ds_read_b64_tr_b16 a[216:217], v159 offset:0\n"
"ds_read_b64_tr_b16 a[218:219], v159 offset:14336\n"
"ds_read_b64_tr_b16 a[220:221], v159 offset:28672\n"
"ds_read_b64_tr_b16 a[222:223], v159 offset:43008\n"
"ds_read_b64_tr_b16 a[232:233], v160 offset:0\n"
"ds_read_b64_tr_b16 a[234:235], v160 offset:14336\n"
"ds_read_b64_tr_b16 a[236:237], v160 offset:28672\n"
"ds_read_b64_tr_b16 a[238:239], v160 offset:43008\n"
"ds_read_b64_tr_b16 a[248:249], v161 offset:0\n"
"ds_read_b64_tr_b16 a[250:251], v161 offset:14336\n"
"ds_read_b64_tr_b16 a[252:253], v161 offset:28672\n"
"ds_read_b64_tr_b16 a[254:255], v161 offset:43008\n"
"ds_read_b64_tr_b16 v[40:41], v162 offset:0\n"
"ds_read_b64_tr_b16 v[42:43], v162 offset:14336\n"
"ds_read_b64_tr_b16 v[44:45], v162 offset:28672\n"
"ds_read_b64_tr_b16 v[46:47], v162 offset:43008\n"
"v_subrev_u32 v156, 57344, v156\n"
"v_subrev_u32 v157, 57344, v157\n"
"v_subrev_u32 v158, 57344, v158\n"
"v_subrev_u32 v159, 57344, v159\n"
"v_subrev_u32 v160, 57344, v160\n"
"v_subrev_u32 v161, 57344, v161\n"
"v_subrev_u32 v162, 57344, v162\n"
"s_waitcnt lgkmcnt(0)\n"
"s_barrier\n"
"s_and_b32 s60, s70, 1\n"
"s_mul_i32 s85, s60, 57344\n"
"s_add_u32 s84, s85, 114688\n"
"v_mov_b32 v229, s78\n"
"s_mov_b64 exec, 15\n"
"v_mov_b32 v229, s76\n"
"s_mov_b64 exec, -1\n"
"v_cmp_ne_u32 vcc, v229, v225\n"
"s_mov_b32 s86, 0x40000\n"
"s_cbranch_vccz ACKOK_%=\n"
"ACK_%=:\n"
"global_load_dword v225, v224, s[24:25] offset:128 sc1\n"
"s_waitcnt vmcnt(0)\n"
"v_cmp_ne_u32 vcc, v229, v225\n"
"s_sub_u32 s86, s86, 1\n"
"s_cbranch_vccz ACKOK_%=\n"
"s_sleep 1\n"
"s_cmp_lg_u32 s86, 0\n"
"s_cbranch_scc1 ACK_%=\n"
"ACKOK_%=:\n"
"s_waitcnt vmcnt(0)\n"
"s_barrier\n"
"v_mov_b32 v2, s76\n"
"v_mov_b32 v3, 0\n"
"s_and_b32 s60, s70, 1\n"
"s_cbranch_scc1 NOFLAG_%=\n"
"s_mov_b64 exec, 1\n"
"global_store_dword v3, v2, s[24:25] sc1\n"
"s_mov_b64 exec, -1\n"
"NOFLAG_%=:\n"
"s_add_u32 m0, s85, 0\n"
"s_nop 0\n"
"buffer_load_dwordx4 v13, s[80:83], s84 offen lds\n"
"buffer_load_dwordx4 v14, s[80:83], s84 offen offset:1024 lds\n"
"buffer_load_dwordx4 v15, s[80:83], s84 offen offset:2048 lds\n"
"buffer_load_dwordx4 v16, s[80:83], s84 offen offset:3072 lds\n"
"s_add_u32 m0, s85, 4096\n"
"s_add_u32 s84, s84, 4096\n"
"buffer_load_dwordx4 v17, s[80:83], s84 offen lds\n"
"buffer_load_dwordx4 v18, s[80:83], s84 offen offset:1024 lds\n"
"buffer_load_dwordx4 v19, s[80:83], s84 offen offset:2048 lds\n"
"s_add_u32 s84, s84, 3072\n"
"s_add_u32 m0, s85, 7168\n"
"s_nop 0\n"
"buffer_load_dwordx4 v13, s[80:83], s84 offen lds\n"
"buffer_load_dwordx4 v14, s[80:83], s84 offen offset:1024 lds\n"
"buffer_load_dwordx4 v15, s[80:83], s84 offen offset:2048 lds\n"
"buffer_load_dwordx4 v16, s[80:83], s84 offen offset:3072 lds\n"
"s_add_u32 m0, s85, 11264\n"
"s_add_u32 s84, s84, 4096\n"
"buffer_load_dwordx4 v17, s[80:83], s84 offen lds\n"
"buffer_load_dwordx4 v18, s[80:83], s84 offen offset:1024 lds\n"
"buffer_load_dwordx4 v19, s[80:83], s84 offen offset:2048 lds\n"
"s_add_u32 s84, s84, 3072\n"
"s_add_u32 m0, s85, 14336\n"
"s_nop 0\n"
"buffer_load_dwordx4 v13, s[80:83], s84 offen lds\n"
"buffer_load_dwordx4 v14, s[80:83], s84 offen offset:1024 lds\n"
"buffer_load_dwordx4 v15, s[80:83], s84 offen offset:2048 lds\n"
"buffer_load_dwordx4 v16, s[80:83], s84 offen offset:3072 lds\n"
"s_add_u32 m0, s85, 18432\n"
"s_add_u32 s84, s84, 4096\n"
"buffer_load_dwordx4 v17, s[80:83], s84 offen lds\n"
"buffer_load_dwordx4 v18, s[80:83], s84 offen offset:1024 lds\n"
"buffer_load_dwordx4 v19, s[80:83], s84 offen offset:2048 lds\n"
"s_add_u32 s84, s84, 3072\n"
"s_add_u32 m0, s85, 21504\n"
"s_nop 0\n"
"buffer_load_dwordx4 v13, s[80:83], s84 offen lds\n"
"buffer_load_dwordx4 v14, s[80:83], s84 offen offset:1024 lds\n"
"buffer_load_dwordx4 v15, s[80:83], s84 offen offset:2048 lds\n"
"buffer_load_dwordx4 v16, s[80:83], s84 offen offset:3072 lds\n"
"s_add_u32 m0, s85, 25600\n"
"s_add_u32 s84, s84, 4096\n"
"buffer_load_dwordx4 v17, s[80:83], s84 offen lds\n"
"buffer_load_dwordx4 v18, s[80:83], s84 offen offset:1024 lds\n"
"buffer_load_dwordx4 v19, s[80:83], s84 offen offset:2048 lds\n"
"s_add_u32 s84, s84, 3072\n"
"s_add_u32 m0, s85, 28672\n"
"s_nop 0\n"
"buffer_load_dwordx4 v13, s[80:83], s84 offen lds\n"
"buffer_load_dwordx4 v14, s[80:83], s84 offen offset:1024 lds\n"
"buffer_load_dwordx4 v15, s[80:83], s84 offen offset:2048 lds\n"
"buffer_load_dwordx4 v16, s[80:83], s84 offen offset:3072 lds\n"
"s_add_u32 m0, s85, 32768\n"
"s_add_u32 s84, s84, 4096\n"
"buffer_load_dwordx4 v17, s[80:83], s84 offen lds\n"
"buffer_load_dwordx4 v18, s[80:83], s84 offen offset:1024 lds\n"
"buffer_load_dwordx4 v19, s[80:83], s84 offen offset:2048 lds\n"
"s_add_u32 s84, s84, 3072\n"
"s_add_u32 m0, s85, 35840\n"
"s_nop 0\n"
"buffer_load_dwordx4 v13, s[80:83], s84 offen lds\n"
"buffer_load_dwordx4 v14, s[80:83], s84 offen offset:1024 lds\n"
"buffer_load_dwordx4 v15, s[80:83], s84 offen offset:2048 lds\n"
"buffer_load_dwordx4 v16, s[80:83], s84 offen offset:3072 lds\n"
"s_add_u32 m0, s85, 39936\n"
"s_add_u32 s84, s84, 4096\n"
"buffer_load_dwordx4 v17, s[80:83], s84 offen lds\n"
"buffer_load_dwordx4 v18, s[80:83], s84 offen offset:1024 lds\n"
"buffer_load_dwordx4 v19, s[80:83], s84 offen offset:2048 lds\n"
"s_add_u32 s84, s84, 3072\n"
"s_add_u32 m0, s85, 43008\n"
"s_nop 0\n"
"buffer_load_dwordx4 v13, s[80:83], s84 offen lds\n"
"buffer_load_dwordx4 v14, s[80:83], s84 offen offset:1024 lds\n"
"buffer_load_dwordx4 v15, s[80:83], s84 offen offset:2048 lds\n"
"buffer_load_dwordx4 v16, s[80:83], s84 offen offset:3072 lds\n"
"s_add_u32 m0, s85, 47104\n"
"s_add_u32 s84, s84, 4096\n"
"buffer_load_dwordx4 v17, s[80:83], s84 offen lds\n"
"buffer_load_dwordx4 v18, s[80:83], s84 offen offset:1024 lds\n"
"buffer_load_dwordx4 v19, s[80:83], s84 offen offset:2048 lds\n"
"s_add_u32 s84, s84, 3072\n"
"s_add_u32 m0, s85, 50176\n"
"s_nop 0\n"
"buffer_load_dwordx4 v13, s[80:83], s84 offen lds\n"
"buffer_load_dwordx4 v14, s[80:83], s84 offen offset:1024 lds\n"
"buffer_load_dwordx4 v15, s[80:83], s84 offen offset:2048 lds\n"
"buffer_load_dwordx4 v16, s[80:83], s84 offen offset:3072 lds\n"
"s_add_u32 m0, s85, 54272\n"
"s_add_u32 s84, s84, 4096\n"
"buffer_load_dwordx4 v17, s[80:83], s84 offen lds\n"
"buffer_load_dwordx4 v18, s[80:83], s84 offen offset:1024 lds\n"
"buffer_load_dwordx4 v19, s[80:83], s84 offen offset:2048 lds\n"
"s_add_u32 s84, s84, 3072\n"
"s_waitcnt vmcnt(0)\n"
"s_barrier\n"
"v_add_u32 v20, 114688, v21\n"
"ds_read_b128 a[0:3], v20 offset:0\n"
"ds_read_b128 a[20:23], v20 offset:5120\n"
"ds_read_b128 a[40:43], v20 offset:10240\n"
"ds_read_b128 a[60:63], v20 offset:15360\n"
"v_add_u32 v137, 57344, v156\n"
"v_add_u32 v138, 57344, v157\n"
"v_add_u32 v139, 57344, v158\n"
"v_add_u32 v140, 57344, v159\n"
"v_add_u32 v141, 57344, v160\n"
"v_add_u32 v142, 57344, v161\n"
"v_add_u32 v143, 57344, v162\n"
"ds_read_b64_tr_b16 v[88:89], v156 offset:0\n"
"ds_read_b64_tr_b16 v[90:91], v156 offset:14336\n"
"ds_read_b128 a[80:83], v20 offset:20480\n"
"ds_read_b64_tr_b16 v[92:93], v156 offset:28672\n"
"ds_read_b64_tr_b16 v[94:95], v156 offset:43008\n"
"ds_read_b128 a[100:103], v20 offset:25600\n"
"ds_read_b64_tr_b16 v[96:97], v137 offset:0\n"
"ds_read_b64_tr_b16 v[98:99], v137 offset:14336\n"
"ds_read_b64_tr_b16 v[100:101], v137 offset:28672\n"
"ds_read_b64_tr_b16 v[102:103], v137 offset:43008\n"
"ds_read_b128 a[120:123], v20 offset:30720\n"
"ds_read_b128 a[140:143], v20 offset:35840\n"
"ds_read_b128 a[4:7], v20 offset:1024\n"
"ds_read_b128 a[24:27], v20 offset:6144\n"
"ds_read_b128 a[44:47], v20 offset:11264\n"
"ds_read_b128 a[64:67], v20 offset:16384\n"
"v_lshlrev_b32 v136, 3, v0\n"
"s_waitcnt lgkmcnt(12)\n"
"v_mfma_f32_16x16x32_bf16 v[48:51], a[160:163], a[0:3], 0\n"
"ds_read_b128 a[84:87], v20 offset:21504\n"
"v_mfma_f32_16x16x32_bf16 v[48:51], a[164:167], a[20:23], v[48:51]\n"
"ds_read_b128 a[104:107], v20 offset:26624\n"
"v_mfma_f32_16x16x32_bf16 v[48:51], a[168:171], a[40:43], v[48:51]\n"
"ds_read_b128 a[124:127], v20 offset:31744\n"
"v_mfma_f32_16x16x32_bf16 v[48:51], a[172:175], a[60:63], v[48:51]\n"
"ds_read_b128 a[144:147], v20 offset:36864\n"
"v_mfma_f32_16x16x32_bf16 v[48:51], v[88:91], a[80:83], v[48:51]\n"
"s_waitcnt lgkmcnt(6)\n"
"ds_read_b128 a[8:11], v20 offset:2048\n"
"v_mfma_f32_16x16x32_bf16 v[48:51], v[92:95], a[100:103], v[48:51]\n"
"ds_read_b128 a[28:31], v20 offset:7168\n"
"v_mfma_f32_16x16x32_bf16 v[48:51], v[96:99], a[120:123], v[48:51]\n"
"ds_read_b128 a[48:51], v20 offset:12288\n"
"v_mfma_f32_16x16x32_bf16 v[48:51], v[100:103], a[140:143], v[48:51]\n"
"ds_read_b128 a[68:71], v20 offset:17408\n"
"v_mfma_f32_16x16x32_bf16 v[52:55], a[160:163], a[4:7], 0\n"
"ds_read_b128 a[88:91], v20 offset:22528\n"
"v_mfma_f32_16x16x32_bf16 v[52:55], a[164:167], a[24:27], v[52:55]\n"
"s_waitcnt lgkmcnt(6)\n"
"ds_read_b128 a[108:111], v20 offset:27648\n"
"v_mfma_f32_16x16x32_bf16 v[52:55], a[168:171], a[44:47], v[52:55]\n"
"ds_read_b128 a[128:131], v20 offset:32768\n"
"v_mfma_f32_16x16x32_bf16 v[52:55], a[172:175], a[64:67], v[52:55]\n"
"ds_read_b128 a[148:151], v20 offset:37888\n"
"v_mfma_f32_16x16x32_bf16 v[52:55], v[88:91], a[84:87], v[52:55]\n"
"ds_read_b128 a[12:15], v20 offset:3072\n"
"v_mfma_f32_16x16x32_bf16 v[52:55], v[92:95], a[104:107], v[52:55]\n"
"ds_read_b128 a[32:35], v20 offset:8192\n"
"v_mfma_f32_16x16x32_bf16 v[52:55], v[96:99], a[124:127], v[52:55]\n"
"s_waitcnt lgkmcnt(6)\n"
"ds_read_b128 a[52:55], v20 offset:13312\n"
"v_cvt_pk_bf16_f32 v120, v48, v49\n"
"v_mfma_f32_16x16x32_bf16 v[52:55], v[100:103], a[144:147], v[52:55]\n"
"ds_read_b128 a[72:75], v20 offset:18432\n"
"v_cvt_pk_bf16_f32 v121, v50, v51\n"
"v_mfma_f32_16x16x32_bf16 v[56:59], a[160:163], a[8:11], 0\n"
"global_store_dwordx2 v136, v[120:121], s[22:23] offset:0 sc1\n"
"v_mfma_f32_16x16x32_bf16 v[56:59], a[164:167], a[28:31], v[56:59]\n"
"ds_read_b128 a[92:95], v20 offset:23552\n"
"v_mfma_f32_16x16x32_bf16 v[56:59], a[168:171], a[48:51], v[56:59]\n"
"ds_read_b128 a[112:115], v20 offset:28672\n"
"v_mfma_f32_16x16x32_bf16 v[56:59], a[172:175], a[68:71], v[56:59]\n"
"s_waitcnt lgkmcnt(4)\n"
"ds_read_b128 a[132:135], v20 offset:33792\n"
"v_mfma_f32_16x16x32_bf16 v[56:59], v[88:91], a[88:91], v[56:59]\n"
"ds_read_b128 a[152:155], v20 offset:38912\n"
"v_mfma_f32_16x16x32_bf16 v[56:59], v[92:95], a[108:111], v[56:59]\n"
"ds_read_b128 a[16:19], v20 offset:4096\n"
"v_mfma_f32_16x16x32_bf16 v[56:59], v[96:99], a[128:131], v[56:59]\n"
"ds_read_b128 a[36:39], v20 offset:9216\n"
"v_cvt_pk_bf16_f32 v122, v52, v53\n"
"v_mfma_f32_16x16x32_bf16 v[56:59], v[100:103], a[148:151], v[56:59]\n"
"ds_read_b128 a[56:59], v20 offset:14336\n"
"v_cvt_pk_bf16_f32 v123, v54, v55\n"
"v_mfma_f32_16x16x32_bf16 v[60:63], a[160:163], a[12:15], 0\n"
"global_store_dwordx2 v136, v[122:123], s[22:23] offset:512 sc1\n"
"v_mfma_f32_16x16x32_bf16 v[60:63], a[164:167], a[32:35], v[60:63]\n"
"s_waitcnt lgkmcnt(4)\n"
"ds_read_b128 a[76:79], v20 offset:19456\n"
"v_mfma_f32_16x16x32_bf16 v[60:63], a[168:171], a[52:55], v[60:63]\n"
"ds_read_b128 a[96:99], v20 offset:24576\n"
"v_mfma_f32_16x16x32_bf16 v[60:63], a[172:175], a[72:75], v[60:63]\n"
"ds_read_b128 a[116:119], v20 offset:29696\n"
"v_mfma_f32_16x16x32_bf16 v[60:63], v[88:91], a[92:95], v[60:63]\n"
"ds_read_b128 a[136:139], v20 offset:34816\n"
"v_mfma_f32_16x16x32_bf16 v[60:63], v[92:95], a[112:115], v[60:63]\n"
"ds_read_b128 a[156:159], v20 offset:39936\n"
"v_mfma_f32_16x16x32_bf16 v[60:63], v[96:99], a[132:135], v[60:63]\n"
"s_waitcnt lgkmcnt(5)\n"
"ds_read_b64_tr_b16 v[104:105], v157 offset:0\n"
"v_cvt_pk_bf16_f32 v124, v56, v57\n"
"v_mfma_f32_16x16x32_bf16 v[60:63], v[100:103], a[152:155], v[60:63]\n"
"ds_read_b64_tr_b16 v[106:107], v157 offset:14336\n"
"v_cvt_pk_bf16_f32 v125, v58, v59\n"
"v_mfma_f32_16x16x32_bf16 v[64:67], a[160:163], a[16:19], 0\n"
"global_store_dwordx2 v136, v[124:125], s[22:23] offset:1024 sc1\n"
"v_mfma_f32_16x16x32_bf16 v[64:67], a[164:167], a[36:39], v[64:67]\n"
"ds_read_b64_tr_b16 v[108:109], v157 offset:28672\n"
"v_mfma_f32_16x16x32_bf16 v[64:67], a[168:171], a[56:59], v[64:67]\n"
"s_waitcnt lgkmcnt(4)\n"
"ds_read_b64_tr_b16 v[110:111], v157 offset:43008\n"
"v_mfma_f32_16x16x32_bf16 v[64:67], a[172:175], a[76:79], v[64:67]\n"
"ds_read_b64_tr_b16 v[112:113], v138 offset:0\n"
"v_mfma_f32_16x16x32_bf16 v[64:67], v[88:91], a[96:99], v[64:67]\n"
"ds_read_b64_tr_b16 v[114:115], v138 offset:14336\n"
"v_mfma_f32_16x16x32_bf16 v[64:67], v[92:95], a[116:119], v[64:67]\n"
"ds_read_b64_tr_b16 v[116:117], v138 offset:28672\n"
"v_mfma_f32_16x16x32_bf16 v[64:67], v[96:99], a[136:139], v[64:67]\n"
"s_waitcnt lgkmcnt(5)\n"
"ds_read_b64_tr_b16 v[118:119], v138 offset:43008\n"
"v_cvt_pk_bf16_f32 v126, v60, v61\n"
"v_mfma_f32_16x16x32_bf16 v[64:67], v[100:103], a[156:159], v[64:67]\n"
"v_cvt_pk_bf16_f32 v127, v62, v63\n"
"v_mfma_f32_16x16x32_bf16 v[68:71], a[176:179], a[0:3], 0\n"
"global_store_dwordx2 v136, v[126:127], s[22:23] offset:1536 sc1\n"
"v_mfma_f32_16x16x32_bf16 v[68:71], a[180:183], a[20:23], v[68:71]\n"
"ds_read_b64_tr_b16 v[88:89], v158 offset:0\n"
"v_mfma_f32_16x16x32_bf16 v[68:71], a[184:187], a[40:43], v[68:71]\n"
"ds_read_b64_tr_b16 v[90:91], v158 offset:14336\n"
"v_mfma_f32_16x16x32_bf16 v[68:71], a[188:191], a[60:63], v[68:71]\n"
"ds_read_b64_tr_b16 v[92:93], v158 offset:28672\n"
"v_mfma_f32_16x16x32_bf16 v[68:71], v[104:107], a[80:83], v[68:71]\n"
"s_waitcnt lgkmcnt(3)\n"
"ds_read_b64_tr_b16 v[94:95], v158 offset:43008\n"
"v_mfma_f32_16x16x32_bf16 v[68:71], v[108:111], a[100:103], v[68:71]\n"
"ds_read_b64_tr_b16 v[96:97], v139 offset:0\n"
"v_mfma_f32_16x16x32_bf16 v[68:71], v[112:115], a[120:123], v[68:71]\n"
"ds_read_b64_tr_b16 v[98:99], v139 offset:14336\n"
"v_cvt_pk_bf16_f32 v128, v64, v65\n"
"v_mfma_f32_16x16x32_bf16 v[68:71], v[116:119], a[140:143], v[68:71]\n"
"ds_read_b64_tr_b16 v[100:101], v139 offset:28672\n"
"v_cvt_pk_bf16_f32 v129, v66, v67\n"
"v_mfma_f32_16x16x32_bf16 v[72:75], a[176:179], a[4:7], 0\n"
"global_store_dwordx2 v136, v[128:129], s[22:23] offset:2048 sc1\n"
"s_nop 1\n"
"v_add_u32 v136, 2560, v136\n"
"v_mfma_f32_16x16x32_bf16 v[72:75], a[180:183], a[24:27], v[72:75]\n"
"ds_read_b64_tr_b16 v[102:103], v139 offset:43008\n"
"v_mfma_f32_16x16x32_bf16 v[72:75], a[184:187], a[44:47], v[72:75]\n"
"v_mfma_f32_16x16x32_bf16 v[72:75], a[188:191], a[64:67], v[72:75]\n"
"v_mfma_f32_16x16x32_bf16 v[72:75], v[104:107], a[84:87], v[72:75]\n"
"v_mfma_f32_16x16x32_bf16 v[72:75], v[108:111], a[104:107], v[72:75]\n"
"v_mfma_f32_16x16x32_bf16 v[72:75], v[112:115], a[124:127], v[72:75]\n"
"v_cvt_pk_bf16_f32 v130, v68, v69\n"
"v_cvt_pk_bf16_f32 v131, v70, v71\n"
"v_mfma_f32_16x16x32_bf16 v[72:75], v[116:119], a[144:147], v[72:75]\n"
"global_store_dwordx2 v136, v[130:131], s[22:23] offset:0 sc1\n"
"v_mfma_f32_16x16x32_bf16 v[76:79], a[176:179], a[8:11], 0\n"
"v_mfma_f32_16x16x32_bf16 v[76:79], a[180:183], a[28:31], v[76:79]\n"
"v_mfma_f32_16x16x32_bf16 v[76:79], a[184:187], a[48:51], v[76:79]\n"
"v_mfma_f32_16x16x32_bf16 v[76:79], a[188:191], a[68:71], v[76:79]\n"
"v_mfma_f32_16x16x32_bf16 v[76:79], v[104:107], a[88:91], v[76:79]\n"
"v_mfma_f32_16x16x32_bf16 v[76:79], v[108:111], a[108:111], v[76:79]\n"
"v_mfma_f32_16x16x32_bf16 v[76:79], v[112:115], a[128:131], v[76:79]\n"
"v_cvt_pk_bf16_f32 v132, v72, v73\n"
"v_cvt_pk_bf16_f32 v133, v74, v75\n"
"v_mfma_f32_16x16x32_bf16 v[76:79], v[116:119], a[148:151], v[76:79]\n"
"global_store_dwordx2 v136, v[132:133], s[22:23] offset:512 sc1\n"
"v_mfma_f32_16x16x32_bf16 v[80:83], a[176:179], a[12:15], 0\n"
"v_mfma_f32_16x16x32_bf16 v[80:83], a[180:183], a[32:35], v[80:83]\n"
"v_mfma_f32_16x16x32_bf16 v[80:83], a[184:187], a[52:55], v[80:83]\n"
"v_mfma_f32_16x16x32_bf16 v[80:83], a[188:191], a[72:75], v[80:83]\n"
"v_mfma_f32_16x16x32_bf16 v[80:83], v[104:107], a[92:95], v[80:83]\n"
"v_mfma_f32_16x16x32_bf16 v[80:83], v[108:111], a[112:115], v[80:83]\n"
"v_mfma_f32_16x16x32_bf16 v[80:83], v[112:115], a[132:135], v[80:83]\n"
"v_cvt_pk_bf16_f32 v134, v76, v77\n"
"v_cvt_pk_bf16_f32 v135, v78, v79\n"
"v_mfma_f32_16x16x32_bf16 v[80:83], v[116:119], a[152:155], v[80:83]\n"
"global_store_dwordx2 v136, v[134:135], s[22:23] offset:1024 sc1\n"
"v_mfma_f32_16x16x32_bf16 v[84:87], a[176:179], a[16:19], 0\n"
"v_mfma_f32_16x16x32_bf16 v[84:87], a[180:183], a[36:39], v[84:87]\n"
"v_mfma_f32_16x16x32_bf16 v[84:87], a[184:187], a[56:59], v[84:87]\n"
"v_mfma_f32_16x16x32_bf16 v[84:87], a[188:191], a[76:79], v[84:87]\n"
"v_mfma_f32_16x16x32_bf16 v[84:87], v[104:107], a[96:99], v[84:87]\n"
"v_mfma_f32_16x16x32_bf16 v[84:87], v[108:111], a[116:119], v[84:87]\n"
"v_mfma_f32_16x16x32_bf16 v[84:87], v[112:115], a[136:139], v[84:87]\n"
"v_cvt_pk_bf16_f32 v120, v80, v81\n"
"v_cvt_pk_bf16_f32 v121, v82, v83\n"
"v_mfma_f32_16x16x32_bf16 v[84:87], v[116:119], a[156:159], v[84:87]\n"
"global_store_dwordx2 v136, v[120:121], s[22:23] offset:1536 sc1\n"
"v_mfma_f32_16x16x32_bf16 v[48:51], a[192:195], a[0:3], 0\n"
"ds_read_b64_tr_b16 v[104:105], v159 offset:0\n"
"v_mfma_f32_16x16x32_bf16 v[48:51], a[196:199], a[20:23], v[48:51]\n"
"ds_read_b64_tr_b16 v[106:107], v159 offset:14336\n"
"v_mfma_f32_16x16x32_bf16 v[48:51], a[200:203], a[40:43], v[48:51]\n"
"ds_read_b64_tr_b16 v[108:109], v159 offset:28672\n"
"v_mfma_f32_16x16x32_bf16 v[48:51], a[204:207], a[60:63], v[48:51]\n"
"s_waitcnt lgkmcnt(3)\n"
"ds_read_b64_tr_b16 v[110:111], v159 offset:43008\n"
"v_mfma_f32_16x16x32_bf16 v[48:51], v[88:91], a[80:83], v[48:51]\n"
"ds_read_b64_tr_b16 v[112:113], v140 offset:0\n"
"v_mfma_f32_16x16x32_bf16 v[48:51], v[92:95], a[100:103], v[48:51]\n"
"ds_read_b64_tr_b16 v[114:115], v140 offset:14336\n"
"v_mfma_f32_16x16x32_bf16 v[48:51], v[96:99], a[120:123], v[48:51]\n"
"ds_read_b64_tr_b16 v[116:117], v140 offset:28672\n"
"v_cvt_pk_bf16_f32 v122, v84, v85\n"
"v_mfma_f32_16x16x32_bf16 v[48:51], v[100:103], a[140:143], v[48:51]\n"
"ds_read_b64_tr_b16 v[118:119], v140 offset:43008\n"
"v_cvt_pk_bf16_f32 v123, v86, v87\n"
"v_mfma_f32_16x16x32_bf16 v[52:55], a[192:195], a[4:7], 0\n"
"global_store_dwordx2 v136, v[122:123], s[22:23] offset:2048 sc1\n"
"s_nop 1\n"
"v_add_u32 v136, 2560, v136\n"
"v_mfma_f32_16x16x32_bf16 v[52:55], a[196:199], a[24:27], v[52:55]\n"
"v_mfma_f32_16x16x32_bf16 v[52:55], a[200:203], a[44:47], v[52:55]\n"
"v_mfma_f32_16x16x32_bf16 v[52:55], a[204:207], a[64:67], v[52:55]\n"
"v_mfma_f32_16x16x32_bf16 v[52:55], v[88:91], a[84:87], v[52:55]\n"
"v_mfma_f32_16x16x32_bf16 v[52:55], v[92:95], a[104:107], v[52:55]\n"
"v_mfma_f32_16x16x32_bf16 v[52:55], v[96:99], a[124:127], v[52:55]\n"
"v_cvt_pk_bf16_f32 v124, v48, v49\n"
"v_cvt_pk_bf16_f32 v125, v50, v51\n"
"v_mfma_f32_16x16x32_bf16 v[52:55], v[100:103], a[144:147], v[52:55]\n"
"global_store_dwordx2 v136, v[124:125], s[22:23] offset:0 sc1\n"
"v_mfma_f32_16x16x32_bf16 v[56:59], a[192:195], a[8:11], 0\n"
"v_mfma_f32_16x16x32_bf16 v[56:59], a[196:199], a[28:31], v[56:59]\n"
"v_mfma_f32_16x16x32_bf16 v[56:59], a[200:203], a[48:51], v[56:59]\n"
"v_mfma_f32_16x16x32_bf16 v[56:59], a[204:207], a[68:71], v[56:59]\n"
"v_mfma_f32_16x16x32_bf16 v[56:59], v[88:91], a[88:91], v[56:59]\n"
"v_mfma_f32_16x16x32_bf16 v[56:59], v[92:95], a[108:111], v[56:59]\n"
"v_mfma_f32_16x16x32_bf16 v[56:59], v[96:99], a[128:131], v[56:59]\n"
"v_cvt_pk_bf16_f32 v126, v52, v53\n"
"v_cvt_pk_bf16_f32 v127, v54, v55\n"
"v_mfma_f32_16x16x32_bf16 v[56:59], v[100:103], a[148:151], v[56:59]\n"
"global_store_dwordx2 v136, v[126:127], s[22:23] offset:512 sc1\n"
"v_mfma_f32_16x16x32_bf16 v[60:63], a[192:195], a[12:15], 0\n"
"v_mfma_f32_16x16x32_bf16 v[60:63], a[196:199], a[32:35], v[60:63]\n"
"v_mfma_f32_16x16x32_bf16 v[60:63], a[200:203], a[52:55], v[60:63]\n"
"v_mfma_f32_16x16x32_bf16 v[60:63], a[204:207], a[72:75], v[60:63]\n"
"v_mfma_f32_16x16x32_bf16 v[60:63], v[88:91], a[92:95], v[60:63]\n"
"v_mfma_f32_16x16x32_bf16 v[60:63], v[92:95], a[112:115], v[60:63]\n"
"v_mfma_f32_16x16x32_bf16 v[60:63], v[96:99], a[132:135], v[60:63]\n"
"v_cvt_pk_bf16_f32 v128, v56, v57\n"
"v_cvt_pk_bf16_f32 v129, v58, v59\n"
"v_mfma_f32_16x16x32_bf16 v[60:63], v[100:103], a[152:155], v[60:63]\n"
"global_store_dwordx2 v136, v[128:129], s[22:23] offset:1024 sc1\n"
"v_mfma_f32_16x16x32_bf16 v[64:67], a[192:195], a[16:19], 0\n"
"v_mfma_f32_16x16x32_bf16 v[64:67], a[196:199], a[36:39], v[64:67]\n"
"v_mfma_f32_16x16x32_bf16 v[64:67], a[200:203], a[56:59], v[64:67]\n"
"v_mfma_f32_16x16x32_bf16 v[64:67], a[204:207], a[76:79], v[64:67]\n"
"v_mfma_f32_16x16x32_bf16 v[64:67], v[88:91], a[96:99], v[64:67]\n"
"v_mfma_f32_16x16x32_bf16 v[64:67], v[92:95], a[116:119], v[64:67]\n"
"v_mfma_f32_16x16x32_bf16 v[64:67], v[96:99], a[136:139], v[64:67]\n"
"v_cvt_pk_bf16_f32 v130, v60, v61\n"
"v_cvt_pk_bf16_f32 v131, v62, v63\n"
"v_mfma_f32_16x16x32_bf16 v[64:67], v[100:103], a[156:159], v[64:67]\n"
"global_store_dwordx2 v136, v[130:131], s[22:23] offset:1536 sc1\n"
"v_mfma_f32_16x16x32_bf16 v[68:71], a[208:211], a[0:3], 0\n"
"ds_read_b64_tr_b16 v[88:89], v160 offset:0\n"
"v_mfma_f32_16x16x32_bf16 v[68:71], a[212:215], a[20:23], v[68:71]\n"
"ds_read_b64_tr_b16 v[90:91], v160 offset:14336\n"
"v_mfma_f32_16x16x32_bf16 v[68:71], a[216:219], a[40:43], v[68:71]\n"
"ds_read_b64_tr_b16 v[92:93], v160 offset:28672\n"
"v_mfma_f32_16x16x32_bf16 v[68:71], a[220:223], a[60:63], v[68:71]\n"
"s_waitcnt lgkmcnt(3)\n"
"ds_read_b64_tr_b16 v[94:95], v160 offset:43008\n"
"v_mfma_f32_16x16x32_bf16 v[68:71], v[104:107], a[80:83], v[68:71]\n"
"ds_read_b64_tr_b16 v[96:97], v141 offset:0\n"
"v_mfma_f32_16x16x32_bf16 v[68:71], v[108:111], a[100:103], v[68:71]\n"
"ds_read_b64_tr_b16 v[98:99], v141 offset:14336\n"
"v_mfma_f32_16x16x32_bf16 v[68:71], v[112:115], a[120:123], v[68:71]\n"
"ds_read_b64_tr_b16 v[100:101], v141 offset:28672\n"
"v_cvt_pk_bf16_f32 v132, v64, v65\n"
"v_mfma_f32_16x16x32_bf16 v[68:71], v[116:119], a[140:143], v[68:71]\n"
"ds_read_b64_tr_b16 v[102:103], v141 offset:43008\n"
"v_cvt_pk_bf16_f32 v133, v66, v67\n"
"v_mfma_f32_16x16x32_bf16 v[72:75], a[208:211], a[4:7], 0\n"
"global_store_dwordx2 v136, v[132:133], s[22:23] offset:2048 sc1\n"
"s_nop 1\n"
"v_add_u32 v136, 2560, v136\n"
"v_mfma_f32_16x16x32_bf16 v[72:75], a[212:215], a[24:27], v[72:75]\n"
"v_mfma_f32_16x16x32_bf16 v[72:75], a[216:219], a[44:47], v[72:75]\n"
"v_mfma_f32_16x16x32_bf16 v[72:75], a[220:223], a[64:67], v[72:75]\n"
"v_mfma_f32_16x16x32_bf16 v[72:75], v[104:107], a[84:87], v[72:75]\n"
"v_mfma_f32_16x16x32_bf16 v[72:75], v[108:111], a[104:107], v[72:75]\n"
"v_mfma_f32_16x16x32_bf16 v[72:75], v[112:115], a[124:127], v[72:75]\n"
"v_cvt_pk_bf16_f32 v134, v68, v69\n"
"v_cvt_pk_bf16_f32 v135, v70, v71\n"
"v_mfma_f32_16x16x32_bf16 v[72:75], v[116:119], a[144:147], v[72:75]\n"
"global_store_dwordx2 v136, v[134:135], s[22:23] offset:0 sc1\n"
"v_mfma_f32_16x16x32_bf16 v[76:79], a[208:211], a[8:11], 0\n"
"v_mfma_f32_16x16x32_bf16 v[76:79], a[212:215], a[28:31], v[76:79]\n"
"v_mfma_f32_16x16x32_bf16 v[76:79], a[216:219], a[48:51], v[76:79]\n"
"v_mfma_f32_16x16x32_bf16 v[76:79], a[220:223], a[68:71], v[76:79]\n"
"v_mfma_f32_16x16x32_bf16 v[76:79], v[104:107], a[88:91], v[76:79]\n"
"v_mfma_f32_16x16x32_bf16 v[76:79], v[108:111], a[108:111], v[76:79]\n"
"v_mfma_f32_16x16x32_bf16 v[76:79], v[112:115], a[128:131], v[76:79]\n"
"v_cvt_pk_bf16_f32 v120, v72, v73\n"
"v_cvt_pk_bf16_f32 v121, v74, v75\n"
"v_mfma_f32_16x16x32_bf16 v[76:79], v[116:119], a[148:151], v[76:79]\n"
"global_store_dwordx2 v136, v[120:121], s[22:23] offset:512 sc1\n"
"v_mfma_f32_16x16x32_bf16 v[80:83], a[208:211], a[12:15], 0\n"
"v_mfma_f32_16x16x32_bf16 v[80:83], a[212:215], a[32:35], v[80:83]\n"
"v_mfma_f32_16x16x32_bf16 v[80:83], a[216:219], a[52:55], v[80:83]\n"
"v_mfma_f32_16x16x32_bf16 v[80:83], a[220:223], a[72:75], v[80:83]\n"
"v_mfma_f32_16x16x32_bf16 v[80:83], v[104:107], a[92:95], v[80:83]\n"
"v_mfma_f32_16x16x32_bf16 v[80:83], v[108:111], a[112:115], v[80:83]\n"
"v_mfma_f32_16x16x32_bf16 v[80:83], v[112:115], a[132:135], v[80:83]\n"
"v_cvt_pk_bf16_f32 v122, v76, v77\n"
"v_cvt_pk_bf16_f32 v123, v78, v79\n"
"v_mfma_f32_16x16x32_bf16 v[80:83], v[116:119], a[152:155], v[80:83]\n"
"global_store_dwordx2 v136, v[122:123], s[22:23] offset:1024 sc1\n"
"v_mfma_f32_16x16x32_bf16 v[84:87], a[208:211], a[16:19], 0\n"
"v_mfma_f32_16x16x32_bf16 v[84:87], a[212:215], a[36:39], v[84:87]\n"
"v_mfma_f32_16x16x32_bf16 v[84:87], a[216:219], a[56:59], v[84:87]\n"
"v_mfma_f32_16x16x32_bf16 v[84:87], a[220:223], a[76:79], v[84:87]\n"
"v_mfma_f32_16x16x32_bf16 v[84:87], v[104:107], a[96:99], v[84:87]\n"
"v_mfma_f32_16x16x32_bf16 v[84:87], v[108:111], a[116:119], v[84:87]\n"
"v_mfma_f32_16x16x32_bf16 v[84:87], v[112:115], a[136:139], v[84:87]\n"
"v_cvt_pk_bf16_f32 v124, v80, v81\n"
"v_cvt_pk_bf16_f32 v125, v82, v83\n"
"v_mfma_f32_16x16x32_bf16 v[84:87], v[116:119], a[156:159], v[84:87]\n"
"global_store_dwordx2 v136, v[124:125], s[22:23] offset:1536 sc1\n"
"v_mfma_f32_16x16x32_bf16 v[48:51], a[224:227], a[0:3], 0\n"
"ds_read_b64_tr_b16 v[104:105], v161 offset:0\n"
"v_mfma_f32_16x16x32_bf16 v[48:51], a[228:231], a[20:23], v[48:51]\n"
"ds_read_b64_tr_b16 v[106:107], v161 offset:14336\n"
"v_mfma_f32_16x16x32_bf16 v[48:51], a[232:235], a[40:43], v[48:51]\n"
"ds_read_b64_tr_b16 v[108:109], v161 offset:28672\n"
"v_mfma_f32_16x16x32_bf16 v[48:51], a[236:239], a[60:63], v[48:51]\n"
"s_waitcnt lgkmcnt(3)\n"
"ds_read_b64_tr_b16 v[110:111], v161 offset:43008\n"
"v_mfma_f32_16x16x32_bf16 v[48:51], v[88:91], a[80:83], v[48:51]\n"
"ds_read_b64_tr_b16 v[112:113], v142 offset:0\n"
"v_mfma_f32_16x16x32_bf16 v[48:51], v[92:95], a[100:103], v[48:51]\n"
"ds_read_b64_tr_b16 v[114:115], v142 offset:14336\n"
"v_mfma_f32_16x16x32_bf16 v[48:51], v[96:99], a[120:123], v[48:51]\n"
"ds_read_b64_tr_b16 v[116:117], v142 offset:28672\n"
"v_cvt_pk_bf16_f32 v126, v84, v85\n"
"v_mfma_f32_16x16x32_bf16 v[48:51], v[100:103], a[140:143], v[48:51]\n"
"ds_read_b64_tr_b16 v[118:119], v142 offset:43008\n"
"v_cvt_pk_bf16_f32 v127, v86, v87\n"
"v_mfma_f32_16x16x32_bf16 v[52:55], a[224:227], a[4:7], 0\n"
"global_store_dwordx2 v136, v[126:127], s[22:23] offset:2048 sc1\n"
"s_nop 1\n"
"v_add_u32 v136, 2560, v136\n"
"v_mfma_f32_16x16x32_bf16 v[52:55], a[228:231], a[24:27], v[52:55]\n"
"v_mfma_f32_16x16x32_bf16 v[52:55], a[232:235], a[44:47], v[52:55]\n"
"v_mfma_f32_16x16x32_bf16 v[52:55], a[236:239], a[64:67], v[52:55]\n"
"v_mfma_f32_16x16x32_bf16 v[52:55], v[88:91], a[84:87], v[52:55]\n"
"v_mfma_f32_16x16x32_bf16 v[52:55], v[92:95], a[104:107], v[52:55]\n"
"v_mfma_f32_16x16x32_bf16 v[52:55], v[96:99], a[124:127], v[52:55]\n"
"v_cvt_pk_bf16_f32 v128, v48, v49\n"
"v_cvt_pk_bf16_f32 v129, v50, v51\n"
"v_mfma_f32_16x16x32_bf16 v[52:55], v[100:103], a[144:147], v[52:55]\n"
"global_store_dwordx2 v136, v[128:129], s[22:23] offset:0 sc1\n"
"v_mfma_f32_16x16x32_bf16 v[56:59], a[224:227], a[8:11], 0\n"
"v_mfma_f32_16x16x32_bf16 v[56:59], a[228:231], a[28:31], v[56:59]\n"
"v_mfma_f32_16x16x32_bf16 v[56:59], a[232:235], a[48:51], v[56:59]\n"
"v_mfma_f32_16x16x32_bf16 v[56:59], a[236:239], a[68:71], v[56:59]\n"
"v_mfma_f32_16x16x32_bf16 v[56:59], v[88:91], a[88:91], v[56:59]\n"
"v_mfma_f32_16x16x32_bf16 v[56:59], v[92:95], a[108:111], v[56:59]\n"
"v_mfma_f32_16x16x32_bf16 v[56:59], v[96:99], a[128:131], v[56:59]\n"
"v_cvt_pk_bf16_f32 v130, v52, v53\n"
"v_cvt_pk_bf16_f32 v131, v54, v55\n"
"v_mfma_f32_16x16x32_bf16 v[56:59], v[100:103], a[148:151], v[56:59]\n"
"global_store_dwordx2 v136, v[130:131], s[22:23] offset:512 sc1\n"
"v_mfma_f32_16x16x32_bf16 v[60:63], a[224:227], a[12:15], 0\n"
"v_mfma_f32_16x16x32_bf16 v[60:63], a[228:231], a[32:35], v[60:63]\n"
"v_mfma_f32_16x16x32_bf16 v[60:63], a[232:235], a[52:55], v[60:63]\n"
"v_mfma_f32_16x16x32_bf16 v[60:63], a[236:239], a[72:75], v[60:63]\n"
"v_mfma_f32_16x16x32_bf16 v[60:63], v[88:91], a[92:95], v[60:63]\n"
"v_mfma_f32_16x16x32_bf16 v[60:63], v[92:95], a[112:115], v[60:63]\n"
"v_mfma_f32_16x16x32_bf16 v[60:63], v[96:99], a[132:135], v[60:63]\n"
"v_cvt_pk_bf16_f32 v132, v56, v57\n"
"v_cvt_pk_bf16_f32 v133, v58, v59\n"
"v_mfma_f32_16x16x32_bf16 v[60:63], v[100:103], a[152:155], v[60:63]\n"
"global_store_dwordx2 v136, v[132:133], s[22:23] offset:1024 sc1\n"
"v_mfma_f32_16x16x32_bf16 v[64:67], a[224:227], a[16:19], 0\n"
"v_mfma_f32_16x16x32_bf16 v[64:67], a[228:231], a[36:39], v[64:67]\n"
"v_mfma_f32_16x16x32_bf16 v[64:67], a[232:235], a[56:59], v[64:67]\n"
"v_mfma_f32_16x16x32_bf16 v[64:67], a[236:239], a[76:79], v[64:67]\n"
"v_mfma_f32_16x16x32_bf16 v[64:67], v[88:91], a[96:99], v[64:67]\n"
"v_mfma_f32_16x16x32_bf16 v[64:67], v[92:95], a[116:119], v[64:67]\n"
"v_mfma_f32_16x16x32_bf16 v[64:67], v[96:99], a[136:139], v[64:67]\n"
"v_cvt_pk_bf16_f32 v134, v60, v61\n"
"v_cvt_pk_bf16_f32 v135, v62, v63\n"
"v_mfma_f32_16x16x32_bf16 v[64:67], v[100:103], a[156:159], v[64:67]\n"
"global_store_dwordx2 v136, v[134:135], s[22:23] offset:1536 sc1\n"
"v_mfma_f32_16x16x32_bf16 v[68:71], a[240:243], a[0:3], 0\n"
"ds_read_b64_tr_b16 v[88:89], v162 offset:0\n"
"v_mfma_f32_16x16x32_bf16 v[68:71], a[244:247], a[20:23], v[68:71]\n"
"ds_read_b64_tr_b16 v[90:91], v162 offset:14336\n"
"v_mfma_f32_16x16x32_bf16 v[68:71], a[248:251], a[40:43], v[68:71]\n"
"ds_read_b64_tr_b16 v[92:93], v162 offset:28672\n"
"v_mfma_f32_16x16x32_bf16 v[68:71], a[252:255], a[60:63], v[68:71]\n"
"s_waitcnt lgkmcnt(3)\n"
"ds_read_b64_tr_b16 v[94:95], v162 offset:43008\n"
"v_mfma_f32_16x16x32_bf16 v[68:71], v[104:107], a[80:83], v[68:71]\n"
"ds_read_b64_tr_b16 v[96:97], v143 offset:0\n"
"v_mfma_f32_16x16x32_bf16 v[68:71], v[108:111], a[100:103], v[68:71]\n"
"ds_read_b64_tr_b16 v[98:99], v143 offset:14336\n"
"v_mfma_f32_16x16x32_bf16 v[68:71], v[112:115], a[120:123], v[68:71]\n"
"ds_read_b64_tr_b16 v[100:101], v143 offset:28672\n"
"v_cvt_pk_bf16_f32 v120, v64, v65\n"
"v_mfma_f32_16x16x32_bf16 v[68:71], v[116:119], a[140:143], v[68:71]\n"
"ds_read_b64_tr_b16 v[102:103], v143 offset:43008\n"
"v_cvt_pk_bf16_f32 v121, v66, v67\n"
"v_mfma_f32_16x16x32_bf16 v[72:75], a[240:243], a[4:7], 0\n"
"global_store_dwordx2 v136, v[120:121], s[22:23] offset:2048 sc1\n"
"s_nop 1\n"
"v_add_u32 v136, 2560, v136\n"
"v_mfma_f32_16x16x32_bf16 v[72:75], a[244:247], a[24:27], v[72:75]\n"
"v_mfma_f32_16x16x32_bf16 v[72:75], a[248:251], a[44:47], v[72:75]\n"
"v_mfma_f32_16x16x32_bf16 v[72:75], a[252:255], a[64:67], v[72:75]\n"
"v_mfma_f32_16x16x32_bf16 v[72:75], v[104:107], a[84:87], v[72:75]\n"
"v_mfma_f32_16x16x32_bf16 v[72:75], v[108:111], a[104:107], v[72:75]\n"
"v_mfma_f32_16x16x32_bf16 v[72:75], v[112:115], a[124:127], v[72:75]\n"
"v_cvt_pk_bf16_f32 v122, v68, v69\n"
"v_cvt_pk_bf16_f32 v123, v70, v71\n"
"v_mfma_f32_16x16x32_bf16 v[72:75], v[116:119], a[144:147], v[72:75]\n"
"global_store_dwordx2 v136, v[122:123], s[22:23] offset:0 sc1\n"
"v_mfma_f32_16x16x32_bf16 v[76:79], a[240:243], a[8:11], 0\n"
"v_mfma_f32_16x16x32_bf16 v[76:79], a[244:247], a[28:31], v[76:79]\n"
"v_mfma_f32_16x16x32_bf16 v[76:79], a[248:251], a[48:51], v[76:79]\n"
"v_mfma_f32_16x16x32_bf16 v[76:79], a[252:255], a[68:71], v[76:79]\n"
"v_mfma_f32_16x16x32_bf16 v[76:79], v[104:107], a[88:91], v[76:79]\n"
"v_mfma_f32_16x16x32_bf16 v[76:79], v[108:111], a[108:111], v[76:79]\n"
"v_mfma_f32_16x16x32_bf16 v[76:79], v[112:115], a[128:131], v[76:79]\n"
"v_cvt_pk_bf16_f32 v124, v72, v73\n"
"v_cvt_pk_bf16_f32 v125, v74, v75\n"
"v_mfma_f32_16x16x32_bf16 v[76:79], v[116:119], a[148:151], v[76:79]\n"
"global_store_dwordx2 v136, v[124:125], s[22:23] offset:512 sc1\n"
"v_mfma_f32_16x16x32_bf16 v[80:83], a[240:243], a[12:15], 0\n"
"v_mfma_f32_16x16x32_bf16 v[80:83], a[244:247], a[32:35], v[80:83]\n"
"v_mfma_f32_16x16x32_bf16 v[80:83], a[248:251], a[52:55], v[80:83]\n"
"v_mfma_f32_16x16x32_bf16 v[80:83], a[252:255], a[72:75], v[80:83]\n"
"v_mfma_f32_16x16x32_bf16 v[80:83], v[104:107], a[92:95], v[80:83]\n"
"v_mfma_f32_16x16x32_bf16 v[80:83], v[108:111], a[112:115], v[80:83]\n"
"v_mfma_f32_16x16x32_bf16 v[80:83], v[112:115], a[132:135], v[80:83]\n"
"v_cvt_pk_bf16_f32 v126, v76, v77\n"
"v_cvt_pk_bf16_f32 v127, v78, v79\n"
"v_mfma_f32_16x16x32_bf16 v[80:83], v[116:119], a[152:155], v[80:83]\n"
"global_store_dwordx2 v136, v[126:127], s[22:23] offset:1024 sc1\n"
"v_mfma_f32_16x16x32_bf16 v[84:87], a[240:243], a[16:19], 0\n"
"v_mfma_f32_16x16x32_bf16 v[84:87], a[244:247], a[36:39], v[84:87]\n"
"v_mfma_f32_16x16x32_bf16 v[84:87], a[248:251], a[56:59], v[84:87]\n"
"v_mfma_f32_16x16x32_bf16 v[84:87], a[252:255], a[76:79], v[84:87]\n"
"v_mfma_f32_16x16x32_bf16 v[84:87], v[104:107], a[96:99], v[84:87]\n"
"v_mfma_f32_16x16x32_bf16 v[84:87], v[108:111], a[116:119], v[84:87]\n"
"v_mfma_f32_16x16x32_bf16 v[84:87], v[112:115], a[136:139], v[84:87]\n"
"v_cvt_pk_bf16_f32 v128, v80, v81\n"
"v_cvt_pk_bf16_f32 v129, v82, v83\n"
"v_mfma_f32_16x16x32_bf16 v[84:87], v[116:119], a[156:159], v[84:87]\n"
"global_store_dwordx2 v136, v[128:129], s[22:23] offset:1536 sc1\n"
"v_mfma_f32_16x16x32_bf16 v[48:51], v[32:35], a[0:3], 0\n"
"v_mfma_f32_16x16x32_bf16 v[48:51], v[36:39], a[20:23], v[48:51]\n"
"v_mfma_f32_16x16x32_bf16 v[48:51], v[40:43], a[40:43], v[48:51]\n"
"v_mfma_f32_16x16x32_bf16 v[48:51], v[44:47], a[60:63], v[48:51]\n"
"s_waitcnt lgkmcnt(0)\n"
"v_mfma_f32_16x16x32_bf16 v[48:51], v[88:91], a[80:83], v[48:51]\n"
"v_mfma_f32_16x16x32_bf16 v[48:51], v[92:95], a[100:103], v[48:51]\n"
"v_mfma_f32_16x16x32_bf16 v[48:51], v[96:99], a[120:123], v[48:51]\n"
"v_cvt_pk_bf16_f32 v130, v84, v85\n"
"v_cvt_pk_bf16_f32 v131, v86, v87\n"
"v_mfma_f32_16x16x32_bf16 v[48:51], v[100:103], a[140:143], v[48:51]\n"
"global_store_dwordx2 v136, v[130:131], s[22:23] offset:2048 sc1\n"
"s_nop 1\n"
"v_add_u32 v136, 2560, v136\n"
"v_mfma_f32_16x16x32_bf16 v[52:55], v[32:35], a[4:7], 0\n"
"v_mfma_f32_16x16x32_bf16 v[52:55], v[36:39], a[24:27], v[52:55]\n"
"v_mfma_f32_16x16x32_bf16 v[52:55], v[40:43], a[44:47], v[52:55]\n"
"v_mfma_f32_16x16x32_bf16 v[52:55], v[44:47], a[64:67], v[52:55]\n"
"v_mfma_f32_16x16x32_bf16 v[52:55], v[88:91], a[84:87], v[52:55]\n"
"v_mfma_f32_16x16x32_bf16 v[52:55], v[92:95], a[104:107], v[52:55]\n"
"v_mfma_f32_16x16x32_bf16 v[52:55], v[96:99], a[124:127], v[52:55]\n"
"v_cvt_pk_bf16_f32 v132, v48, v49\n"
"v_cvt_pk_bf16_f32 v133, v50, v51\n"
"v_mfma_f32_16x16x32_bf16 v[52:55], v[100:103], a[144:147], v[52:55]\n"
"global_store_dwordx2 v136, v[132:133], s[22:23] offset:0 sc1\n"
"v_mfma_f32_16x16x32_bf16 v[56:59], v[32:35], a[8:11], 0\n"
"v_mfma_f32_16x16x32_bf16 v[56:59], v[36:39], a[28:31], v[56:59]\n"
"v_mfma_f32_16x16x32_bf16 v[56:59], v[40:43], a[48:51], v[56:59]\n"
"v_mfma_f32_16x16x32_bf16 v[56:59], v[44:47], a[68:71], v[56:59]\n"
"v_mfma_f32_16x16x32_bf16 v[56:59], v[88:91], a[88:91], v[56:59]\n"
"v_mfma_f32_16x16x32_bf16 v[56:59], v[92:95], a[108:111], v[56:59]\n"
"v_mfma_f32_16x16x32_bf16 v[56:59], v[96:99], a[128:131], v[56:59]\n"
"v_cvt_pk_bf16_f32 v134, v52, v53\n"
"v_cvt_pk_bf16_f32 v135, v54, v55\n"
"v_mfma_f32_16x16x32_bf16 v[56:59], v[100:103], a[148:151], v[56:59]\n"
"global_store_dwordx2 v136, v[134:135], s[22:23] offset:512 sc1\n"
"v_mfma_f32_16x16x32_bf16 v[60:63], v[32:35], a[12:15], 0\n"
"v_mfma_f32_16x16x32_bf16 v[60:63], v[36:39], a[32:35], v[60:63]\n"
"v_mfma_f32_16x16x32_bf16 v[60:63], v[40:43], a[52:55], v[60:63]\n"
"v_mfma_f32_16x16x32_bf16 v[60:63], v[44:47], a[72:75], v[60:63]\n"
"v_mfma_f32_16x16x32_bf16 v[60:63], v[88:91], a[92:95], v[60:63]\n"
"v_mfma_f32_16x16x32_bf16 v[60:63], v[92:95], a[112:115], v[60:63]\n"
"v_mfma_f32_16x16x32_bf16 v[60:63], v[96:99], a[132:135], v[60:63]\n"
"v_cvt_pk_bf16_f32 v120, v56, v57\n"
"v_cvt_pk_bf16_f32 v121, v58, v59\n"
"v_mfma_f32_16x16x32_bf16 v[60:63], v[100:103], a[152:155], v[60:63]\n"
"global_store_dwordx2 v136, v[120:121], s[22:23] offset:1024 sc1\n"
"v_mfma_f32_16x16x32_bf16 v[64:67], v[32:35], a[16:19], 0\n"
"v_mfma_f32_16x16x32_bf16 v[64:67], v[36:39], a[36:39], v[64:67]\n"
"v_mfma_f32_16x16x32_bf16 v[64:67], v[40:43], a[56:59], v[64:67]\n"
"v_mfma_f32_16x16x32_bf16 v[64:67], v[44:47], a[76:79], v[64:67]\n"
"v_mfma_f32_16x16x32_bf16 v[64:67], v[88:91], a[96:99], v[64:67]\n"
"v_mfma_f32_16x16x32_bf16 v[64:67], v[92:95], a[116:119], v[64:67]\n"
"v_mfma_f32_16x16x32_bf16 v[64:67], v[96:99], a[136:139], v[64:67]\n"
"v_cvt_pk_bf16_f32 v122, v60, v61\n"
"v_cvt_pk_bf16_f32 v123, v62, v63\n"
"v_mfma_f32_16x16x32_bf16 v[64:67], v[100:103], a[156:159], v[64:67]\n"
"global_store_dwordx2 v136, v[122:123], s[22:23] offset:1536 sc1\n"
"s_nop 7\n"
"v_cvt_pk_bf16_f32 v124, v64, v65\n"
"v_cvt_pk_bf16_f32 v125, v66, v67\n"
"global_store_dwordx2 v136, v[124:125], s[22:23] offset:2048 sc1\n"
"s_nop 1\n"
"v_add_u32 v136, 2560, v136\n"
"v_and_b32 v8, 15, v0\n"
"v_lshlrev_b32 v9, 2, v8\n"
"v_add_u32 v9, 48128, v9\n"
"global_load_dword v7, v9, s[16:17] sc1\n"
"v_lshlrev_b32 v8, 2, v8\n"
"s_mul_i32 s60, s98, 70\n"
"s_mul_i32 s59, s70, 70\n"
"s_add_u32 s60, s60, s59\n"
"v_add_u32 v10, s60, v0\n"
"s_add_u32 s60, s60, 64\n"
"v_lshlrev_b32 v2, 4, v10\n"
"v_lshrrev_b32 v12, 5, v10\n"
"v_mul_u32_u24 v13, 205, v12\n"
"v_lshrrev_b32 v13, 10, v13\n"
"v_mul_u32_u24 v14, 5, v13\n"
"v_sub_u32 v12, v12, v14\n"
"v_and_b32 v14, 7, v10\n"
"v_lshl_add_u32 v12, v12, 3, v14\n"
"v_mul_u32_u24 v12, 1792, v12\n"
"v_bfe_u32 v14, v10, 3, 2\n"
"v_lshl_add_u32 v13, v13, 2, v14\n"
"v_lshl_add_u32 v4, v13, 3, v12\n"
"v_lshrrev_b32 v11, 2, v0\n"
"v_add_u32 v11, s60, v11\n"
"v_lshlrev_b32 v3, 4, v11\n"
"v_lshrrev_b32 v12, 5, v11\n"
"v_mul_u32_u24 v13, 205, v12\n"
"v_lshrrev_b32 v13, 10, v13\n"
"v_mul_u32_u24 v14, 5, v13\n"
"v_sub_u32 v12, v12, v14\n"
"v_and_b32 v14, 7, v11\n"
"v_lshl_add_u32 v12, v12, 3, v14\n"
"v_mul_u32_u24 v12, 1792, v12\n"
"v_bfe_u32 v14, v11, 3, 2\n"
"v_lshl_add_u32 v13, v13, 2, v14\n"
"v_lshl_add_u32 v5, v13, 3, v12\n"
"v_and_b32 v12, 3, v0\n"
"v_lshl_add_u32 v3, v12, 2, v3\n"
"v_bfe_u32 v13, v0, 1, 1\n"
"v_mul_u32_u24 v13, 896, v13\n"
"v_add_u32 v5, v5, v13\n"
"v_and_b32 v12, 1, v0\n"
"v_lshl_add_u32 v5, v12, 2, v5\n"
"v_mov_b32 v12, s78\n"
"v_mov_b32 v13, s98\n"
"s_waitcnt vmcnt(0)\n"
"s_barrier\n"
"s_mov_b32 s66, 0\n"
"s_mov_b32 s86, 0x40000\n"
"v_mov_b32 v9, 159744\n"
"s_cmp_lg_u32 s70, 0\n"
"s_cbranch_scc1 RDs_%=\n"
"s_mov_b64 exec, 1\n"
"global_store_dword v13, v12, s[94:95] sc1\n"
"s_mov_b64 exec, -1\n"
"RDs_%=:\n"
"s_cmp_lg_u32 s70, 0\n"
"s_cbranch_scc1 NPs_%=\n"
"PGs_%=:\n"
"global_load_dword v6, v8, s[94:95] sc1\n"
"s_waitcnt vmcnt(0)\n"
"v_cmp_eq_u32 vcc, v7, v6\n"
"s_sub_u32 s86, s86, 1\n"
"s_cselect_b32 s67, 0xffff, 0\n"
"s_or_b32 s67, s67, vcc_lo\n"
"s_and_b32 s67, s67, 0xffff\n"
"s_cmp_lg_u32 s67, s66\n"
"s_cbranch_scc1 PXs_%=\n"
"s_sleep 1\n"
"s_branch PGs_%=\n"
"PXs_%=:\n"
"v_mov_b32 v6, s67\n"
"ds_write_b32 v9, v6\n"
"s_waitcnt lgkmcnt(0)\n"
"NPs_%=:\n"
"s_barrier\n"
"ds_read_b32 v6, v9\n"
"v_xor_b32 v9, 4, v9\n"
"s_waitcnt lgkmcnt(0)\n"
"v_readfirstlane_b32 s67, v6\n"
"s_andn2_b32 s60, s67, s66\n"
"s_or_b32 s66, s66, s67\n"
"s_cmp_eq_u32 s66, 0xffff\n"
"s_cselect_b32 s59, 0xffff, 0\n"
"s_cmp_eq_u32 s70, 0\n"
"s_cselect_b32 s60, s59, s60\n"
"s_cmp_lg_u32 s60, 0xffff\n"
"s_cbranch_scc1 SELs_%=\n"
"v_add_u32 v16, 0, v2\n"
"global_load_dwordx4 v[128:131], v16, s[34:35] sc1\n"
"v_add_u32 v17, 2293760, v2\n"
"global_load_dwordx4 v[132:135], v17, s[34:35] sc1\n"
"v_add_u32 v16, 4587520, v2\n"
"global_load_dwordx4 v[136:139], v16, s[34:35] sc1\n"
"v_add_u32 v17, 6881280, v2\n"
"global_load_dwordx4 v[140:143], v17, s[34:35] sc1\n"
"v_add_u32 v16, 9175040, v2\n"
"global_load_dwordx4 v[144:147], v16, s[34:35] sc1\n"
"v_add_u32 v17, 11468800, v2\n"
"global_load_dwordx4 v[148:151], v17, s[34:35] sc1\n"
"v_add_u32 v16, 13762560, v2\n"
"global_load_dwordx4 v[152:155], v16, s[34:35] sc1\n"
"v_add_u32 v17, 16056320, v2\n"
"global_load_dwordx4 v[156:159], v17, s[34:35] sc1\n"
"v_add_u32 v16, 1146880, v2\n"
"global_load_dwordx4 v[160:163], v16, s[34:35] sc1\n"
"v_add_u32 v17, 3440640, v2\n"
"global_load_dwordx4 v[164:167], v17, s[34:35] sc1\n"
"v_add_u32 v16, 5734400, v2\n"
"global_load_dwordx4 v[168:171], v16, s[34:35] sc1\n"
"v_add_u32 v17, 8028160, v2\n"
"global_load_dwordx4 v[172:175], v17, s[34:35] sc1\n"
"v_add_u32 v16, 10321920, v2\n"
"global_load_dwordx4 v[176:179], v16, s[34:35] sc1\n"
"v_add_u32 v17, 12615680, v2\n"
"global_load_dwordx4 v[180:183], v17, s[34:35] sc1\n"
"v_add_u32 v16, 14909440, v2\n"
"global_load_dwordx4 v[184:187], v16, s[34:35] sc1\n"
"v_add_u32 v17, 17203200, v2\n"
"global_load_dwordx4 v[188:191], v17, s[34:35] sc1\n"
"s_mov_b64 exec, 0xffffff\n"
"v_add_u32 v16, 0, v3\n"
"global_load_dword v192, v16, s[34:35] sc1\n"
"v_add_u32 v17, 2293760, v3\n"
"global_load_dword v193, v17, s[34:35] sc1\n"
"v_add_u32 v16, 4587520, v3\n"
"global_load_dword v194, v16, s[34:35] sc1\n"
"v_add_u32 v17, 6881280, v3\n"
"global_load_dword v195, v17, s[34:35] sc1\n"
"v_add_u32 v16, 9175040, v3\n"
"global_load_dword v196, v16, s[34:35] sc1\n"
"v_add_u32 v17, 11468800, v3\n"
"global_load_dword v197, v17, s[34:35] sc1\n"
"v_add_u32 v16, 13762560, v3\n"
"global_load_dword v198, v16, s[34:35] sc1\n"
"v_add_u32 v17, 16056320, v3\n"
"global_load_dword v199, v17, s[34:35] sc1\n"
"v_add_u32 v16, 1146880, v3\n"
"global_load_dword v200, v16, s[34:35] sc1\n"
"v_add_u32 v17, 3440640, v3\n"
"global_load_dword v201, v17, s[34:35] sc1\n"
"v_add_u32 v16, 5734400, v3\n"
"global_load_dword v202, v16, s[34:35] sc1\n"
"v_add_u32 v17, 8028160, v3\n"
"global_load_dword v203, v17, s[34:35] sc1\n"
"v_add_u32 v16, 10321920, v3\n"
"global_load_dword v204, v16, s[34:35] sc1\n"
"v_add_u32 v17, 12615680, v3\n"
"global_load_dword v205, v17, s[34:35] sc1\n"
"v_add_u32 v16, 14909440, v3\n"
"global_load_dword v206, v16, s[34:35] sc1\n"
"v_add_u32 v17, 17203200, v3\n"
"global_load_dword v207, v17, s[34:35] sc1\n"
"s_mov_b64 exec, -1\n"
"s_branch ALLs_%=\n"
"SELs_%=:\n"
"s_bitcmp1_b32 s60, 0\n"
"s_cbranch_scc0 SKs0_%=\n"
"s_mov_b64 s[68:69], s[34:35]\n"
"s_add_u32 s68, s68, 0\n"
"s_addc_u32 s69, s69, 0\n"
"global_load_dwordx4 v[128:131], v2, s[68:69] sc1\n"
"s_mov_b64 exec, 0xffffff\n"
"global_load_dword v192, v3, s[68:69] sc1\n"
"s_mov_b64 exec, -1\n"
"SKs0_%=:\n"
"s_bitcmp1_b32 s60, 1\n"
"s_cbranch_scc0 SKs1_%=\n"
"s_mov_b64 s[68:69], s[34:35]\n"
"s_add_u32 s68, s68, 2293760\n"
"s_addc_u32 s69, s69, 0\n"
"global_load_dwordx4 v[132:135], v2, s[68:69] sc1\n"
"s_mov_b64 exec, 0xffffff\n"
"global_load_dword v193, v3, s[68:69] sc1\n"
"s_mov_b64 exec, -1\n"
"SKs1_%=:\n"
"s_bitcmp1_b32 s60, 2\n"
"s_cbranch_scc0 SKs2_%=\n"
"s_mov_b64 s[68:69], s[34:35]\n"
"s_add_u32 s68, s68, 4587520\n"
"s_addc_u32 s69, s69, 0\n"
"global_load_dwordx4 v[136:139], v2, s[68:69] sc1\n"
"s_mov_b64 exec, 0xffffff\n"
"global_load_dword v194, v3, s[68:69] sc1\n"
"s_mov_b64 exec, -1\n"
"SKs2_%=:\n"
"s_bitcmp1_b32 s60, 3\n"
"s_cbranch_scc0 SKs3_%=\n"
"s_mov_b64 s[68:69], s[34:35]\n"
"s_add_u32 s68, s68, 6881280\n"
"s_addc_u32 s69, s69, 0\n"
"global_load_dwordx4 v[140:143], v2, s[68:69] sc1\n"
"s_mov_b64 exec, 0xffffff\n"
"global_load_dword v195, v3, s[68:69] sc1\n"
"s_mov_b64 exec, -1\n"
"SKs3_%=:\n"
"s_bitcmp1_b32 s60, 4\n"
"s_cbranch_scc0 SKs4_%=\n"
"s_mov_b64 s[68:69], s[34:35]\n"
"s_add_u32 s68, s68, 9175040\n"
"s_addc_u32 s69, s69, 0\n"
"global_load_dwordx4 v[144:147], v2, s[68:69] sc1\n"
"s_mov_b64 exec, 0xffffff\n"
"global_load_dword v196, v3, s[68:69] sc1\n"
"s_mov_b64 exec, -1\n"
"SKs4_%=:\n"
"s_bitcmp1_b32 s60, 5\n"
"s_cbranch_scc0 SKs5_%=\n"
"s_mov_b64 s[68:69], s[34:35]\n"
"s_add_u32 s68, s68, 11468800\n"
"s_addc_u32 s69, s69, 0\n"
"global_load_dwordx4 v[148:151], v2, s[68:69] sc1\n"
"s_mov_b64 exec, 0xffffff\n"
"global_load_dword v197, v3, s[68:69] sc1\n"
"s_mov_b64 exec, -1\n"
"SKs5_%=:\n"
"s_bitcmp1_b32 s60, 6\n"
"s_cbranch_scc0 SKs6_%=\n"
"s_mov_b64 s[68:69], s[34:35]\n"
"s_add_u32 s68, s68, 13762560\n"
"s_addc_u32 s69, s69, 0\n"
"global_load_dwordx4 v[152:155], v2, s[68:69] sc1\n"
"s_mov_b64 exec, 0xffffff\n"
"global_load_dword v198, v3, s[68:69] sc1\n"
"s_mov_b64 exec, -1\n"
"SKs6_%=:\n"
"s_bitcmp1_b32 s60, 7\n"
"s_cbranch_scc0 SKs7_%=\n"
"s_mov_b64 s[68:69], s[34:35]\n"
"s_add_u32 s68, s68, 16056320\n"
"s_addc_u32 s69, s69, 0\n"
"global_load_dwordx4 v[156:159], v2, s[68:69] sc1\n"
"s_mov_b64 exec, 0xffffff\n"
"global_load_dword v199, v3, s[68:69] sc1\n"
"s_mov_b64 exec, -1\n"
"SKs7_%=:\n"
"s_bitcmp1_b32 s60, 8\n"
"s_cbranch_scc0 SKs8_%=\n"
"s_mov_b64 s[68:69], s[34:35]\n"
"s_add_u32 s68, s68, 1146880\n"
"s_addc_u32 s69, s69, 0\n"
"global_load_dwordx4 v[160:163], v2, s[68:69] sc1\n"
"s_mov_b64 exec, 0xffffff\n"
"global_load_dword v200, v3, s[68:69] sc1\n"
"s_mov_b64 exec, -1\n"
"SKs8_%=:\n"
"s_bitcmp1_b32 s60, 9\n"
"s_cbranch_scc0 SKs9_%=\n"
"s_mov_b64 s[68:69], s[34:35]\n"
"s_add_u32 s68, s68, 3440640\n"
"s_addc_u32 s69, s69, 0\n"
"global_load_dwordx4 v[164:167], v2, s[68:69] sc1\n"
"s_mov_b64 exec, 0xffffff\n"
"global_load_dword v201, v3, s[68:69] sc1\n"
"s_mov_b64 exec, -1\n"
"SKs9_%=:\n"
"s_bitcmp1_b32 s60, 10\n"
"s_cbranch_scc0 SKs10_%=\n"
"s_mov_b64 s[68:69], s[34:35]\n"
"s_add_u32 s68, s68, 5734400\n"
"s_addc_u32 s69, s69, 0\n"
"global_load_dwordx4 v[168:171], v2, s[68:69] sc1\n"
"s_mov_b64 exec, 0xffffff\n"
"global_load_dword v202, v3, s[68:69] sc1\n"
"s_mov_b64 exec, -1\n"
"SKs10_%=:\n"
"s_bitcmp1_b32 s60, 11\n"
"s_cbranch_scc0 SKs11_%=\n"
"s_mov_b64 s[68:69], s[34:35]\n"
"s_add_u32 s68, s68, 8028160\n"
"s_addc_u32 s69, s69, 0\n"
"global_load_dwordx4 v[172:175], v2, s[68:69] sc1\n"
"s_mov_b64 exec, 0xffffff\n"
"global_load_dword v203, v3, s[68:69] sc1\n"
"s_mov_b64 exec, -1\n"
"SKs11_%=:\n"
"s_bitcmp1_b32 s60, 12\n"
"s_cbranch_scc0 SKs12_%=\n"
"s_mov_b64 s[68:69], s[34:35]\n"
"s_add_u32 s68, s68, 10321920\n"
"s_addc_u32 s69, s69, 0\n"
"global_load_dwordx4 v[176:179], v2, s[68:69] sc1\n"
"s_mov_b64 exec, 0xffffff\n"
"global_load_dword v204, v3, s[68:69] sc1\n"
"s_mov_b64 exec, -1\n"
"SKs12_%=:\n"
"s_bitcmp1_b32 s60, 13\n"
"s_cbranch_scc0 SKs13_%=\n"
"s_mov_b64 s[68:69], s[34:35]\n"
"s_add_u32 s68, s68, 12615680\n"
"s_addc_u32 s69, s69, 0\n"
"global_load_dwordx4 v[180:183], v2, s[68:69] sc1\n"
"s_mov_b64 exec, 0xffffff\n"
"global_load_dword v205, v3, s[68:69] sc1\n"
"s_mov_b64 exec, -1\n"
"SKs13_%=:\n"
"s_bitcmp1_b32 s60, 14\n"
"s_cbranch_scc0 SKs14_%=\n"
"s_mov_b64 s[68:69], s[34:35]\n"
"s_add_u32 s68, s68, 14909440\n"
"s_addc_u32 s69, s69, 0\n"
"global_load_dwordx4 v[184:187], v2, s[68:69] sc1\n"
"s_mov_b64 exec, 0xffffff\n"
"global_load_dword v206, v3, s[68:69] sc1\n"
"s_mov_b64 exec, -1\n"
"SKs14_%=:\n"
"s_bitcmp1_b32 s60, 15\n"
"s_cbranch_scc0 SKs15_%=\n"
"s_mov_b64 s[68:69], s[34:35]\n"
"s_add_u32 s68, s68, 17203200\n"
"s_addc_u32 s69, s69, 0\n"
"global_load_dwordx4 v[188:191], v2, s[68:69] sc1\n"
"s_mov_b64 exec, 0xffffff\n"
"global_load_dword v207, v3, s[68:69] sc1\n"
"s_mov_b64 exec, -1\n"
"SKs15_%=:\n"
"s_cmp_lg_u32 s66, 0xffff\n"
"s_cbranch_scc1 RDs_%=\n"
"ALLs_%=:\n"
"s_waitcnt vmcnt(0)\n"
"v_lshlrev_b32 v48, 16, v128\n"
"v_and_b32 v49, 0xffff0000, v128\n"
"v_lshlrev_b32 v50, 16, v129\n"
"v_and_b32 v51, 0xffff0000, v129\n"
"v_lshlrev_b32 v52, 16, v130\n"
"v_and_b32 v53, 0xffff0000, v130\n"
"v_lshlrev_b32 v54, 16, v131\n"
"v_and_b32 v55, 0xffff0000, v131\n"
"v_lshlrev_b32 v72, 16, v132\n"
"v_and_b32 v73, 0xffff0000, v132\n"
"v_pk_add_f32 v[48:49], v[48:49], v[72:73]\n"
"v_lshlrev_b32 v74, 16, v133\n"
"v_and_b32 v75, 0xffff0000, v133\n"
"v_pk_add_f32 v[50:51], v[50:51], v[74:75]\n"
"v_lshlrev_b32 v76, 16, v134\n"
"v_and_b32 v77, 0xffff0000, v134\n"
"v_pk_add_f32 v[52:53], v[52:53], v[76:77]\n"
"v_lshlrev_b32 v78, 16, v135\n"
"v_and_b32 v79, 0xffff0000, v135\n"
"v_pk_add_f32 v[54:55], v[54:55], v[78:79]\n"
"v_lshlrev_b32 v72, 16, v136\n"
"v_and_b32 v73, 0xffff0000, v136\n"
"v_pk_add_f32 v[48:49], v[48:49], v[72:73]\n"
"v_lshlrev_b32 v74, 16, v137\n"
"v_and_b32 v75, 0xffff0000, v137\n"
"v_pk_add_f32 v[50:51], v[50:51], v[74:75]\n"
"v_lshlrev_b32 v76, 16, v138\n"
"v_and_b32 v77, 0xffff0000, v138\n"
"v_pk_add_f32 v[52:53], v[52:53], v[76:77]\n"
"v_lshlrev_b32 v78, 16, v139\n"
"v_and_b32 v79, 0xffff0000, v139\n"
"v_pk_add_f32 v[54:55], v[54:55], v[78:79]\n"
"v_lshlrev_b32 v72, 16, v140\n"
"v_and_b32 v73, 0xffff0000, v140\n"
"v_pk_add_f32 v[48:49], v[48:49], v[72:73]\n"
"v_lshlrev_b32 v74, 16, v141\n"
"v_and_b32 v75, 0xffff0000, v141\n"
"v_pk_add_f32 v[50:51], v[50:51], v[74:75]\n"
"v_lshlrev_b32 v76, 16, v142\n"
"v_and_b32 v77, 0xffff0000, v142\n"
"v_pk_add_f32 v[52:53], v[52:53], v[76:77]\n"
"v_lshlrev_b32 v78, 16, v143\n"
"v_and_b32 v79, 0xffff0000, v143\n"
"v_pk_add_f32 v[54:55], v[54:55], v[78:79]\n"
"v_lshlrev_b32 v72, 16, v144\n"
"v_and_b32 v73, 0xffff0000, v144\n"
"v_pk_add_f32 v[48:49], v[48:49], v[72:73]\n"
"v_lshlrev_b32 v74, 16, v145\n"
"v_and_b32 v75, 0xffff0000, v145\n"
"v_pk_add_f32 v[50:51], v[50:51], v[74:75]\n"
"v_lshlrev_b32 v76, 16, v146\n"
"v_and_b32 v77, 0xffff0000, v146\n"
"v_pk_add_f32 v[52:53], v[52:53], v[76:77]\n"
"v_lshlrev_b32 v78, 16, v147\n"
"v_and_b32 v79, 0xffff0000, v147\n"
"v_pk_add_f32 v[54:55], v[54:55], v[78:79]\n"
"v_lshlrev_b32 v72, 16, v148\n"
"v_and_b32 v73, 0xffff0000, v148\n"
"v_pk_add_f32 v[48:49], v[48:49], v[72:73]\n"
"v_lshlrev_b32 v74, 16, v149\n"
"v_and_b32 v75, 0xffff0000, v149\n"
"v_pk_add_f32 v[50:51], v[50:51], v[74:75]\n"
"v_lshlrev_b32 v76, 16, v150\n"
"v_and_b32 v77, 0xffff0000, v150\n"
"v_pk_add_f32 v[52:53], v[52:53], v[76:77]\n"
"v_lshlrev_b32 v78, 16, v151\n"
"v_and_b32 v79, 0xffff0000, v151\n"
"v_pk_add_f32 v[54:55], v[54:55], v[78:79]\n"
"v_lshlrev_b32 v72, 16, v152\n"
"v_and_b32 v73, 0xffff0000, v152\n"
"v_pk_add_f32 v[48:49], v[48:49], v[72:73]\n"
"v_lshlrev_b32 v74, 16, v153\n"
"v_and_b32 v75, 0xffff0000, v153\n"
"v_pk_add_f32 v[50:51], v[50:51], v[74:75]\n"
"v_lshlrev_b32 v76, 16, v154\n"
"v_and_b32 v77, 0xffff0000, v154\n"
"v_pk_add_f32 v[52:53], v[52:53], v[76:77]\n"
"v_lshlrev_b32 v78, 16, v155\n"
"v_and_b32 v79, 0xffff0000, v155\n"
"v_pk_add_f32 v[54:55], v[54:55], v[78:79]\n"
"v_lshlrev_b32 v72, 16, v156\n"
"v_and_b32 v73, 0xffff0000, v156\n"
"v_pk_add_f32 v[48:49], v[48:49], v[72:73]\n"
"v_lshlrev_b32 v74, 16, v157\n"
"v_and_b32 v75, 0xffff0000, v157\n"
"v_pk_add_f32 v[50:51], v[50:51], v[74:75]\n"
"v_lshlrev_b32 v76, 16, v158\n"
"v_and_b32 v77, 0xffff0000, v158\n"
"v_pk_add_f32 v[52:53], v[52:53], v[76:77]\n"
"v_lshlrev_b32 v78, 16, v159\n"
"v_and_b32 v79, 0xffff0000, v159\n"
"v_pk_add_f32 v[54:55], v[54:55], v[78:79]\n"
"v_lshlrev_b32 v72, 16, v160\n"
"v_and_b32 v73, 0xffff0000, v160\n"
"v_pk_add_f32 v[48:49], v[48:49], v[72:73]\n"
"v_lshlrev_b32 v74, 16, v161\n"
"v_and_b32 v75, 0xffff0000, v161\n"
"v_pk_add_f32 v[50:51], v[50:51], v[74:75]\n"
"v_lshlrev_b32 v76, 16, v162\n"
"v_and_b32 v77, 0xffff0000, v162\n"
"v_pk_add_f32 v[52:53], v[52:53], v[76:77]\n"
"v_lshlrev_b32 v78, 16, v163\n"
"v_and_b32 v79, 0xffff0000, v163\n"
"v_pk_add_f32 v[54:55], v[54:55], v[78:79]\n"
"v_lshlrev_b32 v72, 16, v164\n"
"v_and_b32 v73, 0xffff0000, v164\n"
"v_pk_add_f32 v[48:49], v[48:49], v[72:73]\n"
"v_lshlrev_b32 v74, 16, v165\n"
"v_and_b32 v75, 0xffff0000, v165\n"
"v_pk_add_f32 v[50:51], v[50:51], v[74:75]\n"
"v_lshlrev_b32 v76, 16, v166\n"
"v_and_b32 v77, 0xffff0000, v166\n"
"v_pk_add_f32 v[52:53], v[52:53], v[76:77]\n"
"v_lshlrev_b32 v78, 16, v167\n"
"v_and_b32 v79, 0xffff0000, v167\n"
"v_pk_add_f32 v[54:55], v[54:55], v[78:79]\n"
"v_lshlrev_b32 v72, 16, v168\n"
"v_and_b32 v73, 0xffff0000, v168\n"
"v_pk_add_f32 v[48:49], v[48:49], v[72:73]\n"
"v_lshlrev_b32 v74, 16, v169\n"
"v_and_b32 v75, 0xffff0000, v169\n"
"v_pk_add_f32 v[50:51], v[50:51], v[74:75]\n"
"v_lshlrev_b32 v76, 16, v170\n"
"v_and_b32 v77, 0xffff0000, v170\n"
"v_pk_add_f32 v[52:53], v[52:53], v[76:77]\n"
"v_lshlrev_b32 v78, 16, v171\n"
"v_and_b32 v79, 0xffff0000, v171\n"
"v_pk_add_f32 v[54:55], v[54:55], v[78:79]\n"
"v_lshlrev_b32 v72, 16, v172\n"
"v_and_b32 v73, 0xffff0000, v172\n"
"v_pk_add_f32 v[48:49], v[48:49], v[72:73]\n"
"v_lshlrev_b32 v74, 16, v173\n"
"v_and_b32 v75, 0xffff0000, v173\n"
"v_pk_add_f32 v[50:51], v[50:51], v[74:75]\n"
"v_lshlrev_b32 v76, 16, v174\n"
"v_and_b32 v77, 0xffff0000, v174\n"
"v_pk_add_f32 v[52:53], v[52:53], v[76:77]\n"
"v_lshlrev_b32 v78, 16, v175\n"
"v_and_b32 v79, 0xffff0000, v175\n"
"v_pk_add_f32 v[54:55], v[54:55], v[78:79]\n"
"v_lshlrev_b32 v72, 16, v176\n"
"v_and_b32 v73, 0xffff0000, v176\n"
"v_pk_add_f32 v[48:49], v[48:49], v[72:73]\n"
"v_lshlrev_b32 v74, 16, v177\n"
"v_and_b32 v75, 0xffff0000, v177\n"
"v_pk_add_f32 v[50:51], v[50:51], v[74:75]\n"
"v_lshlrev_b32 v76, 16, v178\n"
"v_and_b32 v77, 0xffff0000, v178\n"
"v_pk_add_f32 v[52:53], v[52:53], v[76:77]\n"
"v_lshlrev_b32 v78, 16, v179\n"
"v_and_b32 v79, 0xffff0000, v179\n"
"v_pk_add_f32 v[54:55], v[54:55], v[78:79]\n"
"v_lshlrev_b32 v72, 16, v180\n"
"v_and_b32 v73, 0xffff0000, v180\n"
"v_pk_add_f32 v[48:49], v[48:49], v[72:73]\n"
"v_lshlrev_b32 v74, 16, v181\n"
"v_and_b32 v75, 0xffff0000, v181\n"
"v_pk_add_f32 v[50:51], v[50:51], v[74:75]\n"
"v_lshlrev_b32 v76, 16, v182\n"
"v_and_b32 v77, 0xffff0000, v182\n"
"v_pk_add_f32 v[52:53], v[52:53], v[76:77]\n"
"v_lshlrev_b32 v78, 16, v183\n"
"v_and_b32 v79, 0xffff0000, v183\n"
"v_pk_add_f32 v[54:55], v[54:55], v[78:79]\n"
"v_lshlrev_b32 v72, 16, v184\n"
"v_and_b32 v73, 0xffff0000, v184\n"
"v_pk_add_f32 v[48:49], v[48:49], v[72:73]\n"
"v_lshlrev_b32 v74, 16, v185\n"
"v_and_b32 v75, 0xffff0000, v185\n"
"v_pk_add_f32 v[50:51], v[50:51], v[74:75]\n"
"v_lshlrev_b32 v76, 16, v186\n"
"v_and_b32 v77, 0xffff0000, v186\n"
"v_pk_add_f32 v[52:53], v[52:53], v[76:77]\n"
"v_lshlrev_b32 v78, 16, v187\n"
"v_and_b32 v79, 0xffff0000, v187\n"
"v_pk_add_f32 v[54:55], v[54:55], v[78:79]\n"
"v_lshlrev_b32 v72, 16, v188\n"
"v_and_b32 v73, 0xffff0000, v188\n"
"v_pk_add_f32 v[48:49], v[48:49], v[72:73]\n"
"v_lshlrev_b32 v74, 16, v189\n"
"v_and_b32 v75, 0xffff0000, v189\n"
"v_pk_add_f32 v[50:51], v[50:51], v[74:75]\n"
"v_lshlrev_b32 v76, 16, v190\n"
"v_and_b32 v77, 0xffff0000, v190\n"
"v_pk_add_f32 v[52:53], v[52:53], v[76:77]\n"
"v_lshlrev_b32 v78, 16, v191\n"
"v_and_b32 v79, 0xffff0000, v191\n"
"v_pk_add_f32 v[54:55], v[54:55], v[78:79]\n"
"v_cvt_pk_bf16_f32 v64, v48, v49\n"
"v_cvt_pk_bf16_f32 v65, v50, v51\n"
"v_cvt_pk_bf16_f32 v66, v52, v53\n"
"v_cvt_pk_bf16_f32 v67, v54, v55\n"
"global_store_dwordx2 v4, v[64:65], s[28:29]\n"
"global_store_dwordx2 v4, v[66:67], s[28:29] offset:896\n"
"v_lshlrev_b32 v56, 16, v192\n"
"v_and_b32 v57, 0xffff0000, v192\n"
"v_lshlrev_b32 v74, 16, v193\n"
"v_and_b32 v75, 0xffff0000, v193\n"
"v_pk_add_f32 v[56:57], v[56:57], v[74:75]\n"
"v_lshlrev_b32 v76, 16, v194\n"
"v_and_b32 v77, 0xffff0000, v194\n"
"v_pk_add_f32 v[56:57], v[56:57], v[76:77]\n"
"v_lshlrev_b32 v78, 16, v195\n"
"v_and_b32 v79, 0xffff0000, v195\n"
"v_pk_add_f32 v[56:57], v[56:57], v[78:79]\n"
"v_lshlrev_b32 v72, 16, v196\n"
"v_and_b32 v73, 0xffff0000, v196\n"
"v_pk_add_f32 v[56:57], v[56:57], v[72:73]\n"
"v_lshlrev_b32 v74, 16, v197\n"
"v_and_b32 v75, 0xffff0000, v197\n"
"v_pk_add_f32 v[56:57], v[56:57], v[74:75]\n"
"v_lshlrev_b32 v76, 16, v198\n"
"v_and_b32 v77, 0xffff0000, v198\n"
"v_pk_add_f32 v[56:57], v[56:57], v[76:77]\n"
"v_lshlrev_b32 v78, 16, v199\n"
"v_and_b32 v79, 0xffff0000, v199\n"
"v_pk_add_f32 v[56:57], v[56:57], v[78:79]\n"
"v_lshlrev_b32 v72, 16, v200\n"
"v_and_b32 v73, 0xffff0000, v200\n"
"v_pk_add_f32 v[56:57], v[56:57], v[72:73]\n"
"v_lshlrev_b32 v74, 16, v201\n"
"v_and_b32 v75, 0xffff0000, v201\n"
"v_pk_add_f32 v[56:57], v[56:57], v[74:75]\n"
"v_lshlrev_b32 v76, 16, v202\n"
"v_and_b32 v77, 0xffff0000, v202\n"
"v_pk_add_f32 v[56:57], v[56:57], v[76:77]\n"
"v_lshlrev_b32 v78, 16, v203\n"
"v_and_b32 v79, 0xffff0000, v203\n"
"v_pk_add_f32 v[56:57], v[56:57], v[78:79]\n"
"v_lshlrev_b32 v72, 16, v204\n"
"v_and_b32 v73, 0xffff0000, v204\n"
"v_pk_add_f32 v[56:57], v[56:57], v[72:73]\n"
"v_lshlrev_b32 v74, 16, v205\n"
"v_and_b32 v75, 0xffff0000, v205\n"
"v_pk_add_f32 v[56:57], v[56:57], v[74:75]\n"
"v_lshlrev_b32 v76, 16, v206\n"
"v_and_b32 v77, 0xffff0000, v206\n"
"v_pk_add_f32 v[56:57], v[56:57], v[76:77]\n"
"v_lshlrev_b32 v78, 16, v207\n"
"v_and_b32 v79, 0xffff0000, v207\n"
"v_pk_add_f32 v[56:57], v[56:57], v[78:79]\n"
"v_cvt_pk_bf16_f32 v68, v56, v57\n"
"s_mov_b64 exec, 0xffffff\n"
"global_store_dword v5, v68, s[28:29]\n"
"s_mov_b64 exec, -1\n"
"s_branch END_%=\n"
"RECV_%=:\n"
"s_waitcnt lgkmcnt(2)\n"
"v_mfma_f32_16x16x32_bf16 a[0:3], v[56:59], v[96:99], a[0:3]\n"
"s_waitcnt vmcnt(29)\n"
"v_add_f32 v4, v76, v43\n"
"v_exp_f32 v4, -v4\n"
"v_mov_b32 v6, v43\n"
"v_mfma_f32_16x16x32_bf16 a[4:7], v[56:59], v[100:103], a[4:7]\n"
"v_mul_f32 v7, v33, v4\n"
"v_add_f32 v4, v80, v44\n"
"v_exp_f32 v4, -v4\n"
"v_mfma_f32_16x16x32_bf16 a[8:11], v[56:59], v[104:107], a[8:11]\n"
"v_mov_b32 v8, v44\n"
"v_mul_f32 v9, v34, v4\n"
"v_add_f32 v4, v84, v45\n"
"v_mfma_f32_16x16x32_bf16 a[12:15], v[56:59], v[108:111], a[12:15]\n"
"v_exp_f32 v4, -v4\n"
"v_mov_b32 v10, v45\n"
"v_mul_f32 v11, v35, v4\n"
"v_mfma_f32_16x16x32_bf16 a[16:19], v[56:59], v[112:115], a[16:19]\n"
"v_add_f32 v4, v88, v46\n"
"v_exp_f32 v4, -v4\n"
"v_mov_b32 v22, v46\n"
"ds_read_b64_tr_b16 v[56:57], v26 offset:49152\n"
"v_mfma_f32_16x16x32_bf16 a[20:23], v[60:63], v[96:99], a[20:23]\n"
"v_mul_f32 v23, v36, v4\n"
"v_add_f32 v4, v92, v47\n"
"v_exp_f32 v4, -v4\n"
"ds_read_b64_tr_b16 v[58:59], v26 offset:53248\n"
"v_mfma_f32_16x16x32_bf16 a[24:27], v[60:63], v[100:103], a[24:27]\n"
"v_mov_b32 v24, v47\n"
"v_mul_f32 v25, v37, v4\n"
"v_and_b32 v4, 15, v0\n"
"v_mfma_f32_16x16x32_bf16 a[28:31], v[60:63], v[104:107], a[28:31]\n"
"v_lshlrev_b32 v4, 3, v4\n"
"v_mfma_f32_16x16x32_bf16 a[32:35], v[60:63], v[108:111], a[32:35]\n"
"s_mul_i32 s60, s70, 640\n"
"s_add_u32 s60, s60, 159744\n"
"v_add_u32 v5, s60, v4\n"
"ds_write_b64 v5, v[6:7] offset:0\n"
"ds_write_b64 v5, v[8:9] offset:128\n"
"v_mfma_f32_16x16x32_bf16 a[36:39], v[60:63], v[112:115], a[36:39]\n"
"s_waitcnt lgkmcnt(2)\n"
"ds_write_b64 v5, v[10:11] offset:256\n"
"ds_write_b64 v5, v[22:23] offset:384\n"
"ds_write_b64 v5, v[24:25] offset:512\n"
"ds_read_b64_tr_b16 v[60:61], v27 offset:49152\n"
"v_mfma_f32_16x16x32_bf16 a[40:43], v[64:67], v[96:99], a[40:43]\n"
"v_lshrrev_b32 v2, 2, v0\n"
"v_mul_u32_u24 v2, 896, v2\n"
"v_and_b32 v3, 3, v0\n"
"ds_read_b64_tr_b16 v[62:63], v27 offset:53248\n"
"v_mfma_f32_16x16x32_bf16 a[44:47], v[64:67], v[100:103], a[44:47]\n"
"v_lshl_add_u32 v2, v3, 3, v2\n"
"v_bfe_u32 v3, v0, 3, 2\n"
"v_mfma_f32_16x16x32_bf16 a[48:51], v[64:67], v[104:107], a[48:51]\n"
"s_mul_i32 s60, s70, 7\n"
"v_xor_b32 v13, s60, v3\n"
"v_lshl_add_u32 v13, v13, 5, v2\n"
"v_mfma_f32_16x16x32_bf16 a[52:55], v[64:67], v[108:111], a[52:55]\n"
"s_add_u32 s60, s60, 1\n"
"v_xor_b32 v14, s60, v3\n"
"v_lshl_add_u32 v14, v14, 5, v2\n"
"v_mfma_f32_16x16x32_bf16 a[56:59], v[64:67], v[112:115], a[56:59]\n"
"s_add_u32 s60, s60, 1\n"
"v_xor_b32 v15, s60, v3\n"
"v_lshl_add_u32 v15, v15, 5, v2\n"
"ds_read_b64_tr_b16 v[64:65], v28 offset:49152\n"
"v_mfma_f32_16x16x32_bf16 a[60:63], v[68:71], v[96:99], a[60:63]\n"
"s_add_u32 s60, s60, 1\n"
"v_xor_b32 v16, s60, v3\n"
"v_lshl_add_u32 v16, v16, 5, v2\n"
"ds_read_b64_tr_b16 v[66:67], v28 offset:53248\n"
"v_mfma_f32_16x16x32_bf16 a[64:67], v[68:71], v[100:103], a[64:67]\n"
"s_add_u32 s60, s60, 1\n"
"v_xor_b32 v17, s60, v3\n"
"v_lshl_add_u32 v17, v17, 5, v2\n"
"v_mfma_f32_16x16x32_bf16 a[68:71], v[68:71], v[104:107], a[68:71]\n"
"s_add_u32 s60, s60, 1\n"
"v_xor_b32 v18, s60, v3\n"
"v_lshl_add_u32 v18, v18, 5, v2\n"
"v_mfma_f32_16x16x32_bf16 a[72:75], v[68:71], v[108:111], a[72:75]\n"
"s_add_u32 s60, s60, 1\n"
"v_xor_b32 v19, s60, v3\n"
"v_lshl_add_u32 v19, v19, 5, v2\n"
"v_mfma_f32_16x16x32_bf16 a[76:79], v[68:71], v[112:115], a[76:79]\n"
"s_add_u32 s60, s60, 1\n"
"v_lshlrev_b32 v21, 4, v0\n"
"v_mfma_f32_16x16x32_bf16 a[80:83], v[72:75], v[96:99], a[80:83]\n"
"v_mfma_f32_16x16x32_bf16 a[84:87], v[72:75], v[100:103], a[84:87]\n"
"v_mfma_f32_16x16x32_bf16 a[88:91], v[72:75], v[104:107], a[88:91]\n"
"v_mfma_f32_16x16x32_bf16 a[92:95], v[72:75], v[108:111], a[92:95]\n"
"v_mfma_f32_16x16x32_bf16 a[96:99], v[72:75], v[112:115], a[96:99]\n"
"v_mfma_f32_16x16x32_bf16 a[100:103], v[56:59], v[96:99], a[100:103]\n"
"v_mfma_f32_16x16x32_bf16 a[104:107], v[56:59], v[100:103], a[104:107]\n"
"v_mfma_f32_16x16x32_bf16 a[108:111], v[56:59], v[104:107], a[108:111]\n"
"v_mfma_f32_16x16x32_bf16 a[112:115], v[56:59], v[108:111], a[112:115]\n"
"s_waitcnt vmcnt(0) lgkmcnt(0)\n"
"s_barrier\n"
"v_mfma_f32_16x16x32_bf16 a[116:119], v[56:59], v[112:115], a[116:119]\n"
"ds_read_b64_tr_b16 a[160:161], v13 offset:0\n"
"v_mfma_f32_16x16x32_bf16 a[120:123], v[60:63], v[96:99], a[120:123]\n"
"ds_read_b64_tr_b16 a[162:163], v13 offset:14336\n"
"v_mfma_f32_16x16x32_bf16 a[124:127], v[60:63], v[100:103], a[124:127]\n"
"ds_read_b64_tr_b16 a[164:165], v13 offset:28672\n"
"v_mfma_f32_16x16x32_bf16 a[128:131], v[60:63], v[104:107], a[128:131]\n"
"ds_read_b64_tr_b16 a[166:167], v13 offset:43008\n"
"v_mfma_f32_16x16x32_bf16 a[132:135], v[60:63], v[108:111], a[132:135]\n"
"ds_read_b64_tr_b16 a[176:177], v14 offset:0\n"
"v_mfma_f32_16x16x32_bf16 a[136:139], v[60:63], v[112:115], a[136:139]\n"
"ds_read_b64_tr_b16 a[178:179], v14 offset:14336\n"
"v_mfma_f32_16x16x32_bf16 a[140:143], v[64:67], v[96:99], a[140:143]\n"
"ds_read_b64_tr_b16 a[180:181], v14 offset:28672\n"
"v_mfma_f32_16x16x32_bf16 a[144:147], v[64:67], v[100:103], a[144:147]\n"
"ds_read_b64_tr_b16 a[182:183], v14 offset:43008\n"
"v_mfma_f32_16x16x32_bf16 a[148:151], v[64:67], v[104:107], a[148:151]\n"
"ds_read_b64_tr_b16 a[192:193], v15 offset:0\n"
"v_mfma_f32_16x16x32_bf16 a[152:155], v[64:67], v[108:111], a[152:155]\n"
"ds_read_b64_tr_b16 a[194:195], v15 offset:14336\n"
"v_mfma_f32_16x16x32_bf16 a[156:159], v[64:67], v[112:115], a[156:159]\n"
"ds_read_b64_tr_b16 a[196:197], v15 offset:28672\n"
"ds_read_b64_tr_b16 a[198:199], v15 offset:43008\n"
"ds_read_b64_tr_b16 a[208:209], v16 offset:0\n"
"ds_read_b64_tr_b16 a[210:211], v16 offset:14336\n"
"ds_read_b64_tr_b16 a[212:213], v16 offset:28672\n"
"ds_read_b64_tr_b16 a[214:215], v16 offset:43008\n"
"ds_read_b64_tr_b16 a[224:225], v17 offset:0\n"
"ds_read_b64_tr_b16 a[226:227], v17 offset:14336\n"
"ds_read_b64_tr_b16 a[228:229], v17 offset:28672\n"
"ds_read_b64_tr_b16 a[230:231], v17 offset:43008\n"
"ds_read_b64_tr_b16 a[240:241], v18 offset:0\n"
"ds_read_b64_tr_b16 a[242:243], v18 offset:14336\n"
"ds_read_b64_tr_b16 a[244:245], v18 offset:28672\n"
"ds_read_b64_tr_b16 a[246:247], v18 offset:43008\n"
"ds_read_b64_tr_b16 v[32:33], v19 offset:0\n"
"ds_read_b64_tr_b16 v[34:35], v19 offset:14336\n"
"ds_read_b64_tr_b16 v[36:37], v19 offset:28672\n"
"ds_read_b64_tr_b16 v[38:39], v19 offset:43008\n"
"v_add_u32 v13, 57344, v13\n"
"v_add_u32 v14, 57344, v14\n"
"v_add_u32 v15, 57344, v15\n"
"v_add_u32 v16, 57344, v16\n"
"v_add_u32 v17, 57344, v17\n"
"v_add_u32 v18, 57344, v18\n"
"v_add_u32 v19, 57344, v19\n"
"ds_read_b64_tr_b16 a[168:169], v13 offset:0\n"
"ds_read_b64_tr_b16 a[170:171], v13 offset:14336\n"
"ds_read_b64_tr_b16 a[172:173], v13 offset:28672\n"
"ds_read_b64_tr_b16 a[174:175], v13 offset:43008\n"
"ds_read_b64_tr_b16 a[184:185], v14 offset:0\n"
"ds_read_b64_tr_b16 a[186:187], v14 offset:14336\n"
"ds_read_b64_tr_b16 a[188:189], v14 offset:28672\n"
"ds_read_b64_tr_b16 a[190:191], v14 offset:43008\n"
"ds_read_b64_tr_b16 a[200:201], v15 offset:0\n"
"ds_read_b64_tr_b16 a[202:203], v15 offset:14336\n"
"ds_read_b64_tr_b16 a[204:205], v15 offset:28672\n"
"ds_read_b64_tr_b16 a[206:207], v15 offset:43008\n"
"ds_read_b64_tr_b16 a[216:217], v16 offset:0\n"
"ds_read_b64_tr_b16 a[218:219], v16 offset:14336\n"
"ds_read_b64_tr_b16 a[220:221], v16 offset:28672\n"
"ds_read_b64_tr_b16 a[222:223], v16 offset:43008\n"
"ds_read_b64_tr_b16 a[232:233], v17 offset:0\n"
"ds_read_b64_tr_b16 a[234:235], v17 offset:14336\n"
"ds_read_b64_tr_b16 a[236:237], v17 offset:28672\n"
"ds_read_b64_tr_b16 a[238:239], v17 offset:43008\n"
"ds_read_b64_tr_b16 a[248:249], v18 offset:0\n"
"ds_read_b64_tr_b16 a[250:251], v18 offset:14336\n"
"ds_read_b64_tr_b16 a[252:253], v18 offset:28672\n"
"ds_read_b64_tr_b16 a[254:255], v18 offset:43008\n"
"ds_read_b64_tr_b16 v[40:41], v19 offset:0\n"
"ds_read_b64_tr_b16 v[42:43], v19 offset:14336\n"
"ds_read_b64_tr_b16 v[44:45], v19 offset:28672\n"
"ds_read_b64_tr_b16 v[46:47], v19 offset:43008\n"
"v_subrev_u32 v13, 57344, v13\n"
"v_subrev_u32 v14, 57344, v14\n"
"v_subrev_u32 v15, 57344, v15\n"
"v_subrev_u32 v16, 57344, v16\n"
"v_subrev_u32 v17, 57344, v17\n"
"v_subrev_u32 v18, 57344, v18\n"
"v_subrev_u32 v19, 57344, v19\n"
"s_xor_b32 s60, s70, 1\n"
"s_mul_i32 s60, s60, 640\n"
"s_add_u32 s60, s60, 159748\n"
"v_add_u32 v5, s60, v4\n"
"ds_read_b32 v176, v5 offset:0\n"
"ds_read_b32 v177, v5 offset:128\n"
"ds_read_b32 v178, v5 offset:256\n"
"ds_read_b32 v179, v5 offset:384\n"
"ds_read_b32 v180, v5 offset:512\n"
"s_waitcnt lgkmcnt(0)\n"
"s_barrier\n"
"v_add_f32 v7, v7, v176\n"
"v_add_f32 v9, v9, v177\n"
"v_add_f32 v11, v11, v178\n"
"v_add_f32 v23, v23, v179\n"
"v_add_f32 v25, v25, v180\n"
"v_accvgpr_read_b32 v48, a0\n"
"v_accvgpr_read_b32 v49, a1\n"
"v_accvgpr_read_b32 v50, a2\n"
"v_accvgpr_read_b32 v51, a3\n"
"v_accvgpr_read_b32 v52, a20\n"
"v_accvgpr_read_b32 v53, a21\n"
"v_accvgpr_read_b32 v54, a22\n"
"v_accvgpr_read_b32 v55, a23\n"
"v_accvgpr_read_b32 v56, a4\n"
"v_accvgpr_read_b32 v57, a5\n"
"v_accvgpr_read_b32 v58, a6\n"
"v_accvgpr_read_b32 v59, a7\n"
"v_accvgpr_read_b32 v60, a24\n"
"v_accvgpr_read_b32 v61, a25\n"
"v_accvgpr_read_b32 v62, a26\n"
"v_accvgpr_read_b32 v63, a27\n"
"v_accvgpr_read_b32 v64, a8\n"
"v_accvgpr_read_b32 v65, a9\n"
"v_accvgpr_read_b32 v66, a10\n"
"v_accvgpr_read_b32 v67, a11\n"
"v_accvgpr_read_b32 v68, a28\n"
"v_accvgpr_read_b32 v69, a29\n"
"v_accvgpr_read_b32 v70, a30\n"
"v_accvgpr_read_b32 v71, a31\n"
"v_accvgpr_read_b32 v72, a12\n"
"v_accvgpr_read_b32 v73, a13\n"
"v_accvgpr_read_b32 v74, a14\n"
"v_accvgpr_read_b32 v75, a15\n"
"v_accvgpr_read_b32 v76, a32\n"
"v_accvgpr_read_b32 v77, a33\n"
"v_accvgpr_read_b32 v78, a34\n"
"v_accvgpr_read_b32 v79, a35\n"
"v_accvgpr_read_b32 v80, a16\n"
"v_accvgpr_read_b32 v81, a17\n"
"v_accvgpr_read_b32 v82, a18\n"
"v_accvgpr_read_b32 v83, a19\n"
"v_accvgpr_read_b32 v84, a36\n"
"v_accvgpr_read_b32 v85, a37\n"
"v_accvgpr_read_b32 v86, a38\n"
"v_accvgpr_read_b32 v87, a39\n"
"v_accvgpr_read_b32 v88, a40\n"
"v_accvgpr_read_b32 v89, a41\n"
"v_accvgpr_read_b32 v90, a42\n"
"v_accvgpr_read_b32 v91, a43\n"
"v_accvgpr_read_b32 v92, a60\n"
"v_accvgpr_read_b32 v93, a61\n"
"v_accvgpr_read_b32 v94, a62\n"
"v_accvgpr_read_b32 v95, a63\n"
"v_accvgpr_read_b32 v96, a44\n"
"v_accvgpr_read_b32 v97, a45\n"
"v_accvgpr_read_b32 v98, a46\n"
"v_accvgpr_read_b32 v99, a47\n"
"v_accvgpr_read_b32 v100, a64\n"
"v_accvgpr_read_b32 v101, a65\n"
"v_accvgpr_read_b32 v102, a66\n"
"v_accvgpr_read_b32 v103, a67\n"
"v_accvgpr_read_b32 v104, a48\n"
"v_accvgpr_read_b32 v105, a49\n"
"v_accvgpr_read_b32 v106, a50\n"
"v_accvgpr_read_b32 v107, a51\n"
"v_accvgpr_read_b32 v108, a68\n"
"v_accvgpr_read_b32 v109, a69\n"
"v_accvgpr_read_b32 v110, a70\n"
"v_accvgpr_read_b32 v111, a71\n"
"v_accvgpr_read_b32 v112, a52\n"
"v_accvgpr_read_b32 v113, a53\n"
"v_accvgpr_read_b32 v114, a54\n"
"v_accvgpr_read_b32 v115, a55\n"
"v_accvgpr_read_b32 v116, a72\n"
"v_accvgpr_read_b32 v117, a73\n"
"v_accvgpr_read_b32 v118, a74\n"
"v_accvgpr_read_b32 v119, a75\n"
"v_accvgpr_read_b32 v120, a56\n"
"v_accvgpr_read_b32 v121, a57\n"
"v_accvgpr_read_b32 v122, a58\n"
"v_accvgpr_read_b32 v123, a59\n"
"v_accvgpr_read_b32 v124, a76\n"
"v_accvgpr_read_b32 v125, a77\n"
"v_accvgpr_read_b32 v126, a78\n"
"v_accvgpr_read_b32 v127, a79\n"
"v_accvgpr_read_b32 v128, a80\n"
"v_accvgpr_read_b32 v129, a81\n"
"v_accvgpr_read_b32 v130, a82\n"
"v_accvgpr_read_b32 v131, a83\n"
"v_accvgpr_read_b32 v132, a100\n"
"v_accvgpr_read_b32 v133, a101\n"
"v_accvgpr_read_b32 v134, a102\n"
"v_accvgpr_read_b32 v135, a103\n"
"v_accvgpr_read_b32 v136, a84\n"
"v_accvgpr_read_b32 v137, a85\n"
"v_accvgpr_read_b32 v138, a86\n"
"v_accvgpr_read_b32 v139, a87\n"
"v_accvgpr_read_b32 v140, a104\n"
"v_accvgpr_read_b32 v141, a105\n"
"v_accvgpr_read_b32 v142, a106\n"
"v_accvgpr_read_b32 v143, a107\n"
"v_accvgpr_read_b32 v144, a88\n"
"v_accvgpr_read_b32 v145, a89\n"
"v_accvgpr_read_b32 v146, a90\n"
"v_accvgpr_read_b32 v147, a91\n"
"v_accvgpr_read_b32 v148, a108\n"
"v_accvgpr_read_b32 v149, a109\n"
"v_accvgpr_read_b32 v150, a110\n"
"v_accvgpr_read_b32 v151, a111\n"
"v_accvgpr_read_b32 v152, a92\n"
"v_accvgpr_read_b32 v153, a93\n"
"v_accvgpr_read_b32 v154, a94\n"
"v_accvgpr_read_b32 v155, a95\n"
"v_accvgpr_read_b32 v156, a112\n"
"v_accvgpr_read_b32 v157, a113\n"
"v_accvgpr_read_b32 v158, a114\n"
"v_accvgpr_read_b32 v159, a115\n"
"v_accvgpr_read_b32 v160, a96\n"
"v_accvgpr_read_b32 v161, a97\n"
"v_accvgpr_read_b32 v162, a98\n"
"v_accvgpr_read_b32 v163, a99\n"
"v_accvgpr_read_b32 v164, a116\n"
"v_accvgpr_read_b32 v165, a117\n"
"v_accvgpr_read_b32 v166, a118\n"
"v_accvgpr_read_b32 v167, a119\n"
"v_accvgpr_read_b32 v168, a120\n"
"v_accvgpr_read_b32 v169, a121\n"
"v_accvgpr_read_b32 v170, a122\n"
"v_accvgpr_read_b32 v171, a123\n"
"v_accvgpr_read_b32 v172, a140\n"
"v_accvgpr_read_b32 v173, a141\n"
"v_accvgpr_read_b32 v174, a142\n"
"v_accvgpr_read_b32 v175, a143\n"
"s_and_b32 s60, s70, 1\n"
"s_mul_i32 s59, s60, 20480\n"
"v_add_u32 v5, s59, v21\n"
"s_add_u32 s59, s59, 114688\n"
"v_add_u32 v20, s59, v21\n"
"v_and_b32 v4, 15, v0\n"
"v_lshlrev_b32 v4, 3, v4\n"
"v_add_u32 v4, 40960, v4\n"
"v_mov_b32 v3, 0\n"
"s_barrier\n"
"s_waitcnt vmcnt(0)\n"
"global_load_dword v2, v3, s[30:31] sc1\n"
"s_sleep 5\n"
"global_load_dword v26, v3, s[30:31] sc1\n"
"s_sleep 5\n"
"global_load_dword v27, v3, s[30:31] sc1\n"
"s_sleep 5\n"
"global_load_dword v28, v3, s[30:31] sc1\n"
"s_mov_b32 s86, 0x20000\n"
"POLL_%=:\n"
"s_waitcnt vmcnt(3)\n"
"v_cmp_ne_u32 vcc, s77, v2\n"
"s_nop 0\n"
"s_cbranch_vccz POLLx_%=\n"
"global_load_dword v2, v3, s[30:31] sc1\n"
"s_waitcnt vmcnt(3)\n"
"v_cmp_ne_u32 vcc, s77, v26\n"
"s_nop 0\n"
"s_cbranch_vccz POLLx_%=\n"
"global_load_dword v26, v3, s[30:31] sc1\n"
"s_waitcnt vmcnt(3)\n"
"v_cmp_ne_u32 vcc, s77, v27\n"
"s_nop 0\n"
"s_cbranch_vccz POLLx_%=\n"
"global_load_dword v27, v3, s[30:31] sc1\n"
"s_waitcnt vmcnt(3)\n"
"v_cmp_ne_u32 vcc, s77, v28\n"
"s_nop 0\n"
"s_cbranch_vccz POLLx_%=\n"
"global_load_dword v28, v3, s[30:31] sc1\n"
"s_sub_u32 s86, s86, 1\n"
"s_cmp_lg_u32 s86, 0\n"
"s_cbranch_scc1 POLL_%=\n"
"POLLx_%=:\n"
"global_load_dwordx2 v[236:237], v4, s[18:19] offset:0 sc1\n"
"global_load_dword v246, v4, s[18:19] offset:1028 sc1\n"
"global_load_dwordx2 v[238:239], v4, s[18:19] offset:128 sc1\n"
"global_load_dword v247, v4, s[18:19] offset:1156 sc1\n"
"global_load_dwordx2 v[240:241], v4, s[18:19] offset:256 sc1\n"
"global_load_dword v248, v4, s[18:19] offset:1284 sc1\n"
"global_load_dwordx2 v[242:243], v4, s[18:19] offset:384 sc1\n"
"global_load_dword v249, v4, s[18:19] offset:1412 sc1\n"
"global_load_dwordx2 v[244:245], v4, s[18:19] offset:512 sc1\n"
"global_load_dword v250, v4, s[18:19] offset:1540 sc1\n"
"global_load_dwordx4 v[176:179], v5, s[18:19] offset:0 sc1\n"
"global_load_dwordx4 v[180:183], v5, s[18:19] offset:1024 sc1\n"
"global_load_dwordx4 v[184:187], v5, s[18:19] offset:2048 sc1\n"
"global_load_dwordx4 v[188:191], v5, s[18:19] offset:3072 sc1\n"
"v_add_u32 v5, 4096, v5\n"
"global_load_dwordx4 v[192:195], v5, s[18:19] offset:0 sc1\n"
"global_load_dwordx4 v[196:199], v5, s[18:19] offset:1024 sc1\n"
"global_load_dwordx4 v[200:203], v5, s[18:19] offset:2048 sc1\n"
"global_load_dwordx4 v[204:207], v5, s[18:19] offset:3072 sc1\n"
"v_add_u32 v5, 4096, v5\n"
"global_load_dwordx4 v[208:211], v5, s[18:19] offset:0 sc1\n"
"global_load_dwordx4 v[212:215], v5, s[18:19] offset:1024 sc1\n"
"global_load_dwordx4 v[216:219], v5, s[18:19] offset:2048 sc1\n"
"global_load_dwordx4 v[220:223], v5, s[18:19] offset:3072 sc1\n"
"v_add_u32 v5, 4096, v5\n"
"global_load_dwordx4 v[224:227], v5, s[18:19] offset:0 sc1\n"
"global_load_dwordx4 v[228:231], v5, s[18:19] offset:1024 sc1\n"
"global_load_dwordx4 v[232:235], v5, s[18:19] offset:2048 sc1\n"
"s_waitcnt vmcnt(24)\n"
"v_max_f32 v26, v6, v236\n"
"v_sub_f32 v27, v6, v26\n"
"v_sub_f32 v28, v236, v26\n"
"v_exp_f32 v27, v27\n"
"v_exp_f32 v28, v28\n"
"s_waitcnt vmcnt(23)\n"
"v_add_f32 v29, v237, v246\n"
"v_mul_f32 v26, v7, v27\n"
"v_fmac_f32 v26, v29, v28\n"
"v_rcp_f32 v26, v26\n"
"s_nop 0\n"
"v_mul_f32 v6, v27, v26\n"
"v_mul_f32 v7, v28, v26\n"
"s_waitcnt vmcnt(22)\n"
"v_max_f32 v26, v8, v238\n"
"v_sub_f32 v27, v8, v26\n"
"v_sub_f32 v28, v238, v26\n"
"v_exp_f32 v27, v27\n"
"v_exp_f32 v28, v28\n"
"s_waitcnt vmcnt(21)\n"
"v_add_f32 v29, v239, v247\n"
"v_mul_f32 v26, v9, v27\n"
"v_fmac_f32 v26, v29, v28\n"
"v_rcp_f32 v26, v26\n"
"s_nop 0\n"
"v_mul_f32 v8, v27, v26\n"
"v_mul_f32 v9, v28, v26\n"
"s_waitcnt vmcnt(20)\n"
"v_max_f32 v26, v10, v240\n"
"v_sub_f32 v27, v10, v26\n"
"v_sub_f32 v28, v240, v26\n"
"v_exp_f32 v27, v27\n"
"v_exp_f32 v28, v28\n"
"s_waitcnt vmcnt(19)\n"
"v_add_f32 v29, v241, v248\n"
"v_mul_f32 v26, v11, v27\n"
"v_fmac_f32 v26, v29, v28\n"
"v_rcp_f32 v26, v26\n"
"s_nop 0\n"
"v_mul_f32 v10, v27, v26\n"
"v_mul_f32 v11, v28, v26\n"
"s_waitcnt vmcnt(18)\n"
"v_max_f32 v26, v22, v242\n"
"v_sub_f32 v27, v22, v26\n"
"v_sub_f32 v28, v242, v26\n"
"v_exp_f32 v27, v27\n"
"v_exp_f32 v28, v28\n"
"s_waitcnt vmcnt(17)\n"
"v_add_f32 v29, v243, v249\n"
"v_mul_f32 v26, v23, v27\n"
"v_fmac_f32 v26, v29, v28\n"
"v_rcp_f32 v26, v26\n"
"s_nop 0\n"
"v_mul_f32 v22, v27, v26\n"
"v_mul_f32 v23, v28, v26\n"
"s_waitcnt vmcnt(16)\n"
"v_max_f32 v26, v24, v244\n"
"v_sub_f32 v27, v24, v26\n"
"v_sub_f32 v28, v244, v26\n"
"v_exp_f32 v27, v27\n"
"v_exp_f32 v28, v28\n"
"s_waitcnt vmcnt(15)\n"
"v_add_f32 v29, v245, v250\n"
"v_mul_f32 v26, v25, v27\n"
"v_fmac_f32 v26, v29, v28\n"
"v_rcp_f32 v26, v26\n"
"s_nop 0\n"
"v_mul_f32 v24, v27, v26\n"
"v_mul_f32 v25, v28, v26\n"
"global_load_dwordx4 v[236:239], v5, s[18:19] offset:3072 sc1\n"
"v_add_u32 v5, 4096, v5\n"
"global_load_dwordx4 v[240:243], v5, s[18:19] offset:0 sc1\n"
"global_load_dwordx4 v[244:247], v5, s[18:19] offset:1024 sc1\n"
"global_load_dwordx4 v[248:251], v5, s[18:19] offset:2048 sc1\n"
"global_load_dwordx4 v[252:255], v5, s[18:19] offset:3072 sc1\n"
"v_pk_mul_f32 v[48:49], v[48:49], v[6:7] op_sel_hi:[1,0]\n"
"v_pk_mul_f32 v[50:51], v[50:51], v[6:7] op_sel_hi:[1,0]\n"
"v_pk_mul_f32 v[52:53], v[52:53], v[6:7] op_sel_hi:[1,0]\n"
"v_pk_mul_f32 v[54:55], v[54:55], v[6:7] op_sel_hi:[1,0]\n"
"v_pk_mul_f32 v[56:57], v[56:57], v[8:9] op_sel_hi:[1,0]\n"
"v_pk_mul_f32 v[58:59], v[58:59], v[8:9] op_sel_hi:[1,0]\n"
"v_pk_mul_f32 v[60:61], v[60:61], v[8:9] op_sel_hi:[1,0]\n"
"v_pk_mul_f32 v[62:63], v[62:63], v[8:9] op_sel_hi:[1,0]\n"
"v_pk_mul_f32 v[64:65], v[64:65], v[10:11] op_sel_hi:[1,0]\n"
"v_pk_mul_f32 v[66:67], v[66:67], v[10:11] op_sel_hi:[1,0]\n"
"v_pk_mul_f32 v[68:69], v[68:69], v[10:11] op_sel_hi:[1,0]\n"
"v_pk_mul_f32 v[70:71], v[70:71], v[10:11] op_sel_hi:[1,0]\n"
"v_pk_mul_f32 v[72:73], v[72:73], v[22:23] op_sel_hi:[1,0]\n"
"v_pk_mul_f32 v[74:75], v[74:75], v[22:23] op_sel_hi:[1,0]\n"
"v_pk_mul_f32 v[76:77], v[76:77], v[22:23] op_sel_hi:[1,0]\n"
"v_pk_mul_f32 v[78:79], v[78:79], v[22:23] op_sel_hi:[1,0]\n"
"v_pk_mul_f32 v[80:81], v[80:81], v[24:25] op_sel_hi:[1,0]\n"
"v_pk_mul_f32 v[82:83], v[82:83], v[24:25] op_sel_hi:[1,0]\n"
"v_pk_mul_f32 v[84:85], v[84:85], v[24:25] op_sel_hi:[1,0]\n"
"v_pk_mul_f32 v[86:87], v[86:87], v[24:25] op_sel_hi:[1,0]\n"
"v_pk_mul_f32 v[88:89], v[88:89], v[6:7] op_sel_hi:[1,0]\n"
"v_pk_mul_f32 v[90:91], v[90:91], v[6:7] op_sel_hi:[1,0]\n"
"v_pk_mul_f32 v[92:93], v[92:93], v[6:7] op_sel_hi:[1,0]\n"
"v_pk_mul_f32 v[94:95], v[94:95], v[6:7] op_sel_hi:[1,0]\n"
"v_pk_mul_f32 v[96:97], v[96:97], v[8:9] op_sel_hi:[1,0]\n"
"v_pk_mul_f32 v[98:99], v[98:99], v[8:9] op_sel_hi:[1,0]\n"
"v_pk_mul_f32 v[100:101], v[100:101], v[8:9] op_sel_hi:[1,0]\n"
"v_pk_mul_f32 v[102:103], v[102:103], v[8:9] op_sel_hi:[1,0]\n"
"v_pk_mul_f32 v[104:105], v[104:105], v[10:11] op_sel_hi:[1,0]\n"
"v_pk_mul_f32 v[106:107], v[106:107], v[10:11] op_sel_hi:[1,0]\n"
"v_pk_mul_f32 v[108:109], v[108:109], v[10:11] op_sel_hi:[1,0]\n"
"v_pk_mul_f32 v[110:111], v[110:111], v[10:11] op_sel_hi:[1,0]\n"
"v_pk_mul_f32 v[112:113], v[112:113], v[22:23] op_sel_hi:[1,0]\n"
"v_pk_mul_f32 v[114:115], v[114:115], v[22:23] op_sel_hi:[1,0]\n"
"v_pk_mul_f32 v[116:117], v[116:117], v[22:23] op_sel_hi:[1,0]\n"
"v_pk_mul_f32 v[118:119], v[118:119], v[22:23] op_sel_hi:[1,0]\n"
"v_pk_mul_f32 v[120:121], v[120:121], v[24:25] op_sel_hi:[1,0]\n"
"v_pk_mul_f32 v[122:123], v[122:123], v[24:25] op_sel_hi:[1,0]\n"
"v_pk_mul_f32 v[124:125], v[124:125], v[24:25] op_sel_hi:[1,0]\n"
"v_pk_mul_f32 v[126:127], v[126:127], v[24:25] op_sel_hi:[1,0]\n"
"v_pk_mul_f32 v[128:129], v[128:129], v[6:7] op_sel_hi:[1,0]\n"
"v_pk_mul_f32 v[130:131], v[130:131], v[6:7] op_sel_hi:[1,0]\n"
"v_pk_mul_f32 v[132:133], v[132:133], v[6:7] op_sel_hi:[1,0]\n"
"v_pk_mul_f32 v[134:135], v[134:135], v[6:7] op_sel_hi:[1,0]\n"
"v_pk_mul_f32 v[136:137], v[136:137], v[8:9] op_sel_hi:[1,0]\n"
"v_pk_mul_f32 v[138:139], v[138:139], v[8:9] op_sel_hi:[1,0]\n"
"v_pk_mul_f32 v[140:141], v[140:141], v[8:9] op_sel_hi:[1,0]\n"
"v_pk_mul_f32 v[142:143], v[142:143], v[8:9] op_sel_hi:[1,0]\n"
"v_pk_mul_f32 v[144:145], v[144:145], v[10:11] op_sel_hi:[1,0]\n"
"v_pk_mul_f32 v[146:147], v[146:147], v[10:11] op_sel_hi:[1,0]\n"
"v_pk_mul_f32 v[148:149], v[148:149], v[10:11] op_sel_hi:[1,0]\n"
"v_pk_mul_f32 v[150:151], v[150:151], v[10:11] op_sel_hi:[1,0]\n"
"v_pk_mul_f32 v[152:153], v[152:153], v[22:23] op_sel_hi:[1,0]\n"
"v_pk_mul_f32 v[154:155], v[154:155], v[22:23] op_sel_hi:[1,0]\n"
"v_pk_mul_f32 v[156:157], v[156:157], v[22:23] op_sel_hi:[1,0]\n"
"v_pk_mul_f32 v[158:159], v[158:159], v[22:23] op_sel_hi:[1,0]\n"
"v_pk_mul_f32 v[160:161], v[160:161], v[24:25] op_sel_hi:[1,0]\n"
"v_pk_mul_f32 v[162:163], v[162:163], v[24:25] op_sel_hi:[1,0]\n"
"v_pk_mul_f32 v[164:165], v[164:165], v[24:25] op_sel_hi:[1,0]\n"
"v_pk_mul_f32 v[166:167], v[166:167], v[24:25] op_sel_hi:[1,0]\n"
"v_pk_mul_f32 v[168:169], v[168:169], v[6:7] op_sel_hi:[1,0]\n"
"v_pk_mul_f32 v[170:171], v[170:171], v[6:7] op_sel_hi:[1,0]\n"
"v_pk_mul_f32 v[172:173], v[172:173], v[6:7] op_sel_hi:[1,0]\n"
"v_pk_mul_f32 v[174:175], v[174:175], v[6:7] op_sel_hi:[1,0]\n"
"s_waitcnt vmcnt(19)\n"
"v_lshlrev_b32 v26, 16, v176\n"
"v_and_b32 v27, 0xffff0000, v176\n"
"v_pk_fma_f32 v[48:49], v[26:27], v[6:7], v[48:49] op_sel:[0,1,0]\n"
"v_cvt_pk_bf16_f32 v176, v48, v49\n"
"v_lshlrev_b32 v28, 16, v177\n"
"v_and_b32 v29, 0xffff0000, v177\n"
"v_pk_fma_f32 v[50:51], v[28:29], v[6:7], v[50:51] op_sel:[0,1,0]\n"
"v_cvt_pk_bf16_f32 v177, v50, v51\n"
"v_lshlrev_b32 v26, 16, v178\n"
"v_and_b32 v27, 0xffff0000, v178\n"
"v_pk_fma_f32 v[52:53], v[26:27], v[6:7], v[52:53] op_sel:[0,1,0]\n"
"v_cvt_pk_bf16_f32 v178, v52, v53\n"
"v_lshlrev_b32 v28, 16, v179\n"
"v_and_b32 v29, 0xffff0000, v179\n"
"v_pk_fma_f32 v[54:55], v[28:29], v[6:7], v[54:55] op_sel:[0,1,0]\n"
"v_cvt_pk_bf16_f32 v179, v54, v55\n"
"ds_write_b128 v20, v[176:179] offset:0\n"
"s_waitcnt vmcnt(18)\n"
"v_lshlrev_b32 v26, 16, v180\n"
"v_and_b32 v27, 0xffff0000, v180\n"
"v_pk_fma_f32 v[56:57], v[26:27], v[8:9], v[56:57] op_sel:[0,1,0]\n"
"v_cvt_pk_bf16_f32 v180, v56, v57\n"
"v_lshlrev_b32 v28, 16, v181\n"
"v_and_b32 v29, 0xffff0000, v181\n"
"v_pk_fma_f32 v[58:59], v[28:29], v[8:9], v[58:59] op_sel:[0,1,0]\n"
"v_cvt_pk_bf16_f32 v181, v58, v59\n"
"v_lshlrev_b32 v26, 16, v182\n"
"v_and_b32 v27, 0xffff0000, v182\n"
"v_pk_fma_f32 v[60:61], v[26:27], v[8:9], v[60:61] op_sel:[0,1,0]\n"
"v_cvt_pk_bf16_f32 v182, v60, v61\n"
"v_lshlrev_b32 v28, 16, v183\n"
"v_and_b32 v29, 0xffff0000, v183\n"
"v_pk_fma_f32 v[62:63], v[28:29], v[8:9], v[62:63] op_sel:[0,1,0]\n"
"v_cvt_pk_bf16_f32 v183, v62, v63\n"
"ds_write_b128 v20, v[180:183] offset:1024\n"
"s_waitcnt vmcnt(17)\n"
"v_lshlrev_b32 v26, 16, v184\n"
"v_and_b32 v27, 0xffff0000, v184\n"
"v_pk_fma_f32 v[64:65], v[26:27], v[10:11], v[64:65] op_sel:[0,1,0]\n"
"v_cvt_pk_bf16_f32 v184, v64, v65\n"
"v_lshlrev_b32 v28, 16, v185\n"
"v_and_b32 v29, 0xffff0000, v185\n"
"v_pk_fma_f32 v[66:67], v[28:29], v[10:11], v[66:67] op_sel:[0,1,0]\n"
"v_cvt_pk_bf16_f32 v185, v66, v67\n"
"v_lshlrev_b32 v26, 16, v186\n"
"v_and_b32 v27, 0xffff0000, v186\n"
"v_pk_fma_f32 v[68:69], v[26:27], v[10:11], v[68:69] op_sel:[0,1,0]\n"
"v_cvt_pk_bf16_f32 v186, v68, v69\n"
"v_lshlrev_b32 v28, 16, v187\n"
"v_and_b32 v29, 0xffff0000, v187\n"
"v_pk_fma_f32 v[70:71], v[28:29], v[10:11], v[70:71] op_sel:[0,1,0]\n"
"v_cvt_pk_bf16_f32 v187, v70, v71\n"
"ds_write_b128 v20, v[184:187] offset:2048\n"
"s_waitcnt vmcnt(16)\n"
"v_lshlrev_b32 v26, 16, v188\n"
"v_and_b32 v27, 0xffff0000, v188\n"
"v_pk_fma_f32 v[72:73], v[26:27], v[22:23], v[72:73] op_sel:[0,1,0]\n"
"v_cvt_pk_bf16_f32 v188, v72, v73\n"
"v_lshlrev_b32 v28, 16, v189\n"
"v_and_b32 v29, 0xffff0000, v189\n"
"v_pk_fma_f32 v[74:75], v[28:29], v[22:23], v[74:75] op_sel:[0,1,0]\n"
"v_cvt_pk_bf16_f32 v189, v74, v75\n"
"v_lshlrev_b32 v26, 16, v190\n"
"v_and_b32 v27, 0xffff0000, v190\n"
"v_pk_fma_f32 v[76:77], v[26:27], v[22:23], v[76:77] op_sel:[0,1,0]\n"
"v_cvt_pk_bf16_f32 v190, v76, v77\n"
"v_lshlrev_b32 v28, 16, v191\n"
"v_and_b32 v29, 0xffff0000, v191\n"
"v_pk_fma_f32 v[78:79], v[28:29], v[22:23], v[78:79] op_sel:[0,1,0]\n"
"v_cvt_pk_bf16_f32 v191, v78, v79\n"
"ds_write_b128 v20, v[188:191] offset:3072\n"
"s_waitcnt vmcnt(15)\n"
"v_lshlrev_b32 v26, 16, v192\n"
"v_and_b32 v27, 0xffff0000, v192\n"
"v_pk_fma_f32 v[80:81], v[26:27], v[24:25], v[80:81] op_sel:[0,1,0]\n"
"v_cvt_pk_bf16_f32 v192, v80, v81\n"
"v_lshlrev_b32 v28, 16, v193\n"
"v_and_b32 v29, 0xffff0000, v193\n"
"v_pk_fma_f32 v[82:83], v[28:29], v[24:25], v[82:83] op_sel:[0,1,0]\n"
"v_cvt_pk_bf16_f32 v193, v82, v83\n"
"v_lshlrev_b32 v26, 16, v194\n"
"v_and_b32 v27, 0xffff0000, v194\n"
"v_pk_fma_f32 v[84:85], v[26:27], v[24:25], v[84:85] op_sel:[0,1,0]\n"
"v_cvt_pk_bf16_f32 v194, v84, v85\n"
"v_lshlrev_b32 v28, 16, v195\n"
"v_and_b32 v29, 0xffff0000, v195\n"
"v_pk_fma_f32 v[86:87], v[28:29], v[24:25], v[86:87] op_sel:[0,1,0]\n"
"v_cvt_pk_bf16_f32 v195, v86, v87\n"
"ds_write_b128 v20, v[192:195] offset:4096\n"
"s_waitcnt vmcnt(14)\n"
"v_lshlrev_b32 v26, 16, v196\n"
"v_and_b32 v27, 0xffff0000, v196\n"
"v_pk_fma_f32 v[88:89], v[26:27], v[6:7], v[88:89] op_sel:[0,1,0]\n"
"v_cvt_pk_bf16_f32 v196, v88, v89\n"
"v_lshlrev_b32 v28, 16, v197\n"
"v_and_b32 v29, 0xffff0000, v197\n"
"v_pk_fma_f32 v[90:91], v[28:29], v[6:7], v[90:91] op_sel:[0,1,0]\n"
"v_cvt_pk_bf16_f32 v197, v90, v91\n"
"v_lshlrev_b32 v26, 16, v198\n"
"v_and_b32 v27, 0xffff0000, v198\n"
"v_pk_fma_f32 v[92:93], v[26:27], v[6:7], v[92:93] op_sel:[0,1,0]\n"
"v_cvt_pk_bf16_f32 v198, v92, v93\n"
"v_lshlrev_b32 v28, 16, v199\n"
"v_and_b32 v29, 0xffff0000, v199\n"
"v_pk_fma_f32 v[94:95], v[28:29], v[6:7], v[94:95] op_sel:[0,1,0]\n"
"v_cvt_pk_bf16_f32 v199, v94, v95\n"
"ds_write_b128 v20, v[196:199] offset:5120\n"
"s_waitcnt vmcnt(13)\n"
"v_lshlrev_b32 v26, 16, v200\n"
"v_and_b32 v27, 0xffff0000, v200\n"
"v_pk_fma_f32 v[96:97], v[26:27], v[8:9], v[96:97] op_sel:[0,1,0]\n"
"v_cvt_pk_bf16_f32 v200, v96, v97\n"
"v_lshlrev_b32 v28, 16, v201\n"
"v_and_b32 v29, 0xffff0000, v201\n"
"v_pk_fma_f32 v[98:99], v[28:29], v[8:9], v[98:99] op_sel:[0,1,0]\n"
"v_cvt_pk_bf16_f32 v201, v98, v99\n"
"v_lshlrev_b32 v26, 16, v202\n"
"v_and_b32 v27, 0xffff0000, v202\n"
"v_pk_fma_f32 v[100:101], v[26:27], v[8:9], v[100:101] op_sel:[0,1,0]\n"
"v_cvt_pk_bf16_f32 v202, v100, v101\n"
"v_lshlrev_b32 v28, 16, v203\n"
"v_and_b32 v29, 0xffff0000, v203\n"
"v_pk_fma_f32 v[102:103], v[28:29], v[8:9], v[102:103] op_sel:[0,1,0]\n"
"v_cvt_pk_bf16_f32 v203, v102, v103\n"
"ds_write_b128 v20, v[200:203] offset:6144\n"
"s_waitcnt vmcnt(12)\n"
"v_lshlrev_b32 v26, 16, v204\n"
"v_and_b32 v27, 0xffff0000, v204\n"
"v_pk_fma_f32 v[104:105], v[26:27], v[10:11], v[104:105] op_sel:[0,1,0]\n"
"v_cvt_pk_bf16_f32 v204, v104, v105\n"
"v_lshlrev_b32 v28, 16, v205\n"
"v_and_b32 v29, 0xffff0000, v205\n"
"v_pk_fma_f32 v[106:107], v[28:29], v[10:11], v[106:107] op_sel:[0,1,0]\n"
"v_cvt_pk_bf16_f32 v205, v106, v107\n"
"v_lshlrev_b32 v26, 16, v206\n"
"v_and_b32 v27, 0xffff0000, v206\n"
"v_pk_fma_f32 v[108:109], v[26:27], v[10:11], v[108:109] op_sel:[0,1,0]\n"
"v_cvt_pk_bf16_f32 v206, v108, v109\n"
"v_lshlrev_b32 v28, 16, v207\n"
"v_and_b32 v29, 0xffff0000, v207\n"
"v_pk_fma_f32 v[110:111], v[28:29], v[10:11], v[110:111] op_sel:[0,1,0]\n"
"v_cvt_pk_bf16_f32 v207, v110, v111\n"
"ds_write_b128 v20, v[204:207] offset:7168\n"
"s_waitcnt vmcnt(11)\n"
"v_lshlrev_b32 v26, 16, v208\n"
"v_and_b32 v27, 0xffff0000, v208\n"
"v_pk_fma_f32 v[112:113], v[26:27], v[22:23], v[112:113] op_sel:[0,1,0]\n"
"v_cvt_pk_bf16_f32 v208, v112, v113\n"
"v_lshlrev_b32 v28, 16, v209\n"
"v_and_b32 v29, 0xffff0000, v209\n"
"v_pk_fma_f32 v[114:115], v[28:29], v[22:23], v[114:115] op_sel:[0,1,0]\n"
"v_cvt_pk_bf16_f32 v209, v114, v115\n"
"v_lshlrev_b32 v26, 16, v210\n"
"v_and_b32 v27, 0xffff0000, v210\n"
"v_pk_fma_f32 v[116:117], v[26:27], v[22:23], v[116:117] op_sel:[0,1,0]\n"
"v_cvt_pk_bf16_f32 v210, v116, v117\n"
"v_lshlrev_b32 v28, 16, v211\n"
"v_and_b32 v29, 0xffff0000, v211\n"
"v_pk_fma_f32 v[118:119], v[28:29], v[22:23], v[118:119] op_sel:[0,1,0]\n"
"v_cvt_pk_bf16_f32 v211, v118, v119\n"
"ds_write_b128 v20, v[208:211] offset:8192\n"
"s_waitcnt vmcnt(10)\n"
"v_lshlrev_b32 v26, 16, v212\n"
"v_and_b32 v27, 0xffff0000, v212\n"
"v_pk_fma_f32 v[120:121], v[26:27], v[24:25], v[120:121] op_sel:[0,1,0]\n"
"v_cvt_pk_bf16_f32 v212, v120, v121\n"
"v_lshlrev_b32 v28, 16, v213\n"
"v_and_b32 v29, 0xffff0000, v213\n"
"v_pk_fma_f32 v[122:123], v[28:29], v[24:25], v[122:123] op_sel:[0,1,0]\n"
"v_cvt_pk_bf16_f32 v213, v122, v123\n"
"v_lshlrev_b32 v26, 16, v214\n"
"v_and_b32 v27, 0xffff0000, v214\n"
"v_pk_fma_f32 v[124:125], v[26:27], v[24:25], v[124:125] op_sel:[0,1,0]\n"
"v_cvt_pk_bf16_f32 v214, v124, v125\n"
"v_lshlrev_b32 v28, 16, v215\n"
"v_and_b32 v29, 0xffff0000, v215\n"
"v_pk_fma_f32 v[126:127], v[28:29], v[24:25], v[126:127] op_sel:[0,1,0]\n"
"v_cvt_pk_bf16_f32 v215, v126, v127\n"
"ds_write_b128 v20, v[212:215] offset:9216\n"
"s_waitcnt vmcnt(9)\n"
"v_lshlrev_b32 v26, 16, v216\n"
"v_and_b32 v27, 0xffff0000, v216\n"
"v_pk_fma_f32 v[128:129], v[26:27], v[6:7], v[128:129] op_sel:[0,1,0]\n"
"v_cvt_pk_bf16_f32 v216, v128, v129\n"
"v_lshlrev_b32 v28, 16, v217\n"
"v_and_b32 v29, 0xffff0000, v217\n"
"v_pk_fma_f32 v[130:131], v[28:29], v[6:7], v[130:131] op_sel:[0,1,0]\n"
"v_cvt_pk_bf16_f32 v217, v130, v131\n"
"v_lshlrev_b32 v26, 16, v218\n"
"v_and_b32 v27, 0xffff0000, v218\n"
"v_pk_fma_f32 v[132:133], v[26:27], v[6:7], v[132:133] op_sel:[0,1,0]\n"
"v_cvt_pk_bf16_f32 v218, v132, v133\n"
"v_lshlrev_b32 v28, 16, v219\n"
"v_and_b32 v29, 0xffff0000, v219\n"
"v_pk_fma_f32 v[134:135], v[28:29], v[6:7], v[134:135] op_sel:[0,1,0]\n"
"v_cvt_pk_bf16_f32 v219, v134, v135\n"
"ds_write_b128 v20, v[216:219] offset:10240\n"
"s_waitcnt vmcnt(8)\n"
"v_lshlrev_b32 v26, 16, v220\n"
"v_and_b32 v27, 0xffff0000, v220\n"
"v_pk_fma_f32 v[136:137], v[26:27], v[8:9], v[136:137] op_sel:[0,1,0]\n"
"v_cvt_pk_bf16_f32 v220, v136, v137\n"
"v_lshlrev_b32 v28, 16, v221\n"
"v_and_b32 v29, 0xffff0000, v221\n"
"v_pk_fma_f32 v[138:139], v[28:29], v[8:9], v[138:139] op_sel:[0,1,0]\n"
"v_cvt_pk_bf16_f32 v221, v138, v139\n"
"v_lshlrev_b32 v26, 16, v222\n"
"v_and_b32 v27, 0xffff0000, v222\n"
"v_pk_fma_f32 v[140:141], v[26:27], v[8:9], v[140:141] op_sel:[0,1,0]\n"
"v_cvt_pk_bf16_f32 v222, v140, v141\n"
"v_lshlrev_b32 v28, 16, v223\n"
"v_and_b32 v29, 0xffff0000, v223\n"
"v_pk_fma_f32 v[142:143], v[28:29], v[8:9], v[142:143] op_sel:[0,1,0]\n"
"v_cvt_pk_bf16_f32 v223, v142, v143\n"
"ds_write_b128 v20, v[220:223] offset:11264\n"
"s_waitcnt vmcnt(7)\n"
"v_lshlrev_b32 v26, 16, v224\n"
"v_and_b32 v27, 0xffff0000, v224\n"
"v_pk_fma_f32 v[144:145], v[26:27], v[10:11], v[144:145] op_sel:[0,1,0]\n"
"v_cvt_pk_bf16_f32 v224, v144, v145\n"
"v_lshlrev_b32 v28, 16, v225\n"
"v_and_b32 v29, 0xffff0000, v225\n"
"v_pk_fma_f32 v[146:147], v[28:29], v[10:11], v[146:147] op_sel:[0,1,0]\n"
"v_cvt_pk_bf16_f32 v225, v146, v147\n"
"v_lshlrev_b32 v26, 16, v226\n"
"v_and_b32 v27, 0xffff0000, v226\n"
"v_pk_fma_f32 v[148:149], v[26:27], v[10:11], v[148:149] op_sel:[0,1,0]\n"
"v_cvt_pk_bf16_f32 v226, v148, v149\n"
"v_lshlrev_b32 v28, 16, v227\n"
"v_and_b32 v29, 0xffff0000, v227\n"
"v_pk_fma_f32 v[150:151], v[28:29], v[10:11], v[150:151] op_sel:[0,1,0]\n"
"v_cvt_pk_bf16_f32 v227, v150, v151\n"
"ds_write_b128 v20, v[224:227] offset:12288\n"
"s_waitcnt vmcnt(6)\n"
"v_lshlrev_b32 v26, 16, v228\n"
"v_and_b32 v27, 0xffff0000, v228\n"
"v_pk_fma_f32 v[152:153], v[26:27], v[22:23], v[152:153] op_sel:[0,1,0]\n"
"v_cvt_pk_bf16_f32 v228, v152, v153\n"
"v_lshlrev_b32 v28, 16, v229\n"
"v_and_b32 v29, 0xffff0000, v229\n"
"v_pk_fma_f32 v[154:155], v[28:29], v[22:23], v[154:155] op_sel:[0,1,0]\n"
"v_cvt_pk_bf16_f32 v229, v154, v155\n"
"v_lshlrev_b32 v26, 16, v230\n"
"v_and_b32 v27, 0xffff0000, v230\n"
"v_pk_fma_f32 v[156:157], v[26:27], v[22:23], v[156:157] op_sel:[0,1,0]\n"
"v_cvt_pk_bf16_f32 v230, v156, v157\n"
"v_lshlrev_b32 v28, 16, v231\n"
"v_and_b32 v29, 0xffff0000, v231\n"
"v_pk_fma_f32 v[158:159], v[28:29], v[22:23], v[158:159] op_sel:[0,1,0]\n"
"v_cvt_pk_bf16_f32 v231, v158, v159\n"
"ds_write_b128 v20, v[228:231] offset:13312\n"
"s_waitcnt vmcnt(5)\n"
"v_lshlrev_b32 v26, 16, v232\n"
"v_and_b32 v27, 0xffff0000, v232\n"
"v_pk_fma_f32 v[160:161], v[26:27], v[24:25], v[160:161] op_sel:[0,1,0]\n"
"v_cvt_pk_bf16_f32 v232, v160, v161\n"
"v_lshlrev_b32 v28, 16, v233\n"
"v_and_b32 v29, 0xffff0000, v233\n"
"v_pk_fma_f32 v[162:163], v[28:29], v[24:25], v[162:163] op_sel:[0,1,0]\n"
"v_cvt_pk_bf16_f32 v233, v162, v163\n"
"v_lshlrev_b32 v26, 16, v234\n"
"v_and_b32 v27, 0xffff0000, v234\n"
"v_pk_fma_f32 v[164:165], v[26:27], v[24:25], v[164:165] op_sel:[0,1,0]\n"
"v_cvt_pk_bf16_f32 v234, v164, v165\n"
"v_lshlrev_b32 v28, 16, v235\n"
"v_and_b32 v29, 0xffff0000, v235\n"
"v_pk_fma_f32 v[166:167], v[28:29], v[24:25], v[166:167] op_sel:[0,1,0]\n"
"v_cvt_pk_bf16_f32 v235, v166, v167\n"
"ds_write_b128 v20, v[232:235] offset:14336\n"
"s_waitcnt vmcnt(4)\n"
"v_lshlrev_b32 v26, 16, v236\n"
"v_and_b32 v27, 0xffff0000, v236\n"
"v_pk_fma_f32 v[168:169], v[26:27], v[6:7], v[168:169] op_sel:[0,1,0]\n"
"v_cvt_pk_bf16_f32 v236, v168, v169\n"
"v_lshlrev_b32 v28, 16, v237\n"
"v_and_b32 v29, 0xffff0000, v237\n"
"v_pk_fma_f32 v[170:171], v[28:29], v[6:7], v[170:171] op_sel:[0,1,0]\n"
"v_cvt_pk_bf16_f32 v237, v170, v171\n"
"v_lshlrev_b32 v26, 16, v238\n"
"v_and_b32 v27, 0xffff0000, v238\n"
"v_pk_fma_f32 v[172:173], v[26:27], v[6:7], v[172:173] op_sel:[0,1,0]\n"
"v_cvt_pk_bf16_f32 v238, v172, v173\n"
"v_lshlrev_b32 v28, 16, v239\n"
"v_and_b32 v29, 0xffff0000, v239\n"
"v_pk_fma_f32 v[174:175], v[28:29], v[6:7], v[174:175] op_sel:[0,1,0]\n"
"v_cvt_pk_bf16_f32 v239, v174, v175\n"
"ds_write_b128 v20, v[236:239] offset:15360\n"
"v_accvgpr_read_b32 v48, a124\n"
"v_accvgpr_read_b32 v49, a125\n"
"v_accvgpr_read_b32 v50, a126\n"
"v_accvgpr_read_b32 v51, a127\n"
"v_accvgpr_read_b32 v52, a144\n"
"v_accvgpr_read_b32 v53, a145\n"
"v_accvgpr_read_b32 v54, a146\n"
"v_accvgpr_read_b32 v55, a147\n"
"v_pk_mul_f32 v[48:49], v[48:49], v[8:9] op_sel_hi:[1,0]\n"
"v_pk_mul_f32 v[50:51], v[50:51], v[8:9] op_sel_hi:[1,0]\n"
"v_pk_mul_f32 v[52:53], v[52:53], v[8:9] op_sel_hi:[1,0]\n"
"v_pk_mul_f32 v[54:55], v[54:55], v[8:9] op_sel_hi:[1,0]\n"
"s_waitcnt vmcnt(3)\n"
"v_lshlrev_b32 v26, 16, v240\n"
"v_and_b32 v27, 0xffff0000, v240\n"
"v_pk_fma_f32 v[48:49], v[26:27], v[8:9], v[48:49] op_sel:[0,1,0]\n"
"v_cvt_pk_bf16_f32 v240, v48, v49\n"
"v_lshlrev_b32 v28, 16, v241\n"
"v_and_b32 v29, 0xffff0000, v241\n"
"v_pk_fma_f32 v[50:51], v[28:29], v[8:9], v[50:51] op_sel:[0,1,0]\n"
"v_cvt_pk_bf16_f32 v241, v50, v51\n"
"v_lshlrev_b32 v26, 16, v242\n"
"v_and_b32 v27, 0xffff0000, v242\n"
"v_pk_fma_f32 v[52:53], v[26:27], v[8:9], v[52:53] op_sel:[0,1,0]\n"
"v_cvt_pk_bf16_f32 v242, v52, v53\n"
"v_lshlrev_b32 v28, 16, v243\n"
"v_and_b32 v29, 0xffff0000, v243\n"
"v_pk_fma_f32 v[54:55], v[28:29], v[8:9], v[54:55] op_sel:[0,1,0]\n"
"v_cvt_pk_bf16_f32 v243, v54, v55\n"
"ds_write_b128 v20, v[240:243] offset:16384\n"
"v_accvgpr_read_b32 v56, a128\n"
"v_accvgpr_read_b32 v57, a129\n"
"v_accvgpr_read_b32 v58, a130\n"
"v_accvgpr_read_b32 v59, a131\n"
"v_accvgpr_read_b32 v60, a148\n"
"v_accvgpr_read_b32 v61, a149\n"
"v_accvgpr_read_b32 v62, a150\n"
"v_accvgpr_read_b32 v63, a151\n"
"v_pk_mul_f32 v[56:57], v[56:57], v[10:11] op_sel_hi:[1,0]\n"
"v_pk_mul_f32 v[58:59], v[58:59], v[10:11] op_sel_hi:[1,0]\n"
"v_pk_mul_f32 v[60:61], v[60:61], v[10:11] op_sel_hi:[1,0]\n"
"v_pk_mul_f32 v[62:63], v[62:63], v[10:11] op_sel_hi:[1,0]\n"
"s_waitcnt vmcnt(2)\n"
"v_lshlrev_b32 v26, 16, v244\n"
"v_and_b32 v27, 0xffff0000, v244\n"
"v_pk_fma_f32 v[56:57], v[26:27], v[10:11], v[56:57] op_sel:[0,1,0]\n"
"v_cvt_pk_bf16_f32 v244, v56, v57\n"
"v_lshlrev_b32 v28, 16, v245\n"
"v_and_b32 v29, 0xffff0000, v245\n"
"v_pk_fma_f32 v[58:59], v[28:29], v[10:11], v[58:59] op_sel:[0,1,0]\n"
"v_cvt_pk_bf16_f32 v245, v58, v59\n"
"v_lshlrev_b32 v26, 16, v246\n"
"v_and_b32 v27, 0xffff0000, v246\n"
"v_pk_fma_f32 v[60:61], v[26:27], v[10:11], v[60:61] op_sel:[0,1,0]\n"
"v_cvt_pk_bf16_f32 v246, v60, v61\n"
"v_lshlrev_b32 v28, 16, v247\n"
"v_and_b32 v29, 0xffff0000, v247\n"
"v_pk_fma_f32 v[62:63], v[28:29], v[10:11], v[62:63] op_sel:[0,1,0]\n"
"v_cvt_pk_bf16_f32 v247, v62, v63\n"
"ds_write_b128 v20, v[244:247] offset:17408\n"
"v_accvgpr_read_b32 v64, a132\n"
"v_accvgpr_read_b32 v65, a133\n"
"v_accvgpr_read_b32 v66, a134\n"
"v_accvgpr_read_b32 v67, a135\n"
"v_accvgpr_read_b32 v68, a152\n"
"v_accvgpr_read_b32 v69, a153\n"
"v_accvgpr_read_b32 v70, a154\n"
"v_accvgpr_read_b32 v71, a155\n"
"v_pk_mul_f32 v[64:65], v[64:65], v[22:23] op_sel_hi:[1,0]\n"
"v_pk_mul_f32 v[66:67], v[66:67], v[22:23] op_sel_hi:[1,0]\n"
"v_pk_mul_f32 v[68:69], v[68:69], v[22:23] op_sel_hi:[1,0]\n"
"v_pk_mul_f32 v[70:71], v[70:71], v[22:23] op_sel_hi:[1,0]\n"
"s_waitcnt vmcnt(1)\n"
"v_lshlrev_b32 v26, 16, v248\n"
"v_and_b32 v27, 0xffff0000, v248\n"
"v_pk_fma_f32 v[64:65], v[26:27], v[22:23], v[64:65] op_sel:[0,1,0]\n"
"v_cvt_pk_bf16_f32 v248, v64, v65\n"
"v_lshlrev_b32 v28, 16, v249\n"
"v_and_b32 v29, 0xffff0000, v249\n"
"v_pk_fma_f32 v[66:67], v[28:29], v[22:23], v[66:67] op_sel:[0,1,0]\n"
"v_cvt_pk_bf16_f32 v249, v66, v67\n"
"v_lshlrev_b32 v26, 16, v250\n"
"v_and_b32 v27, 0xffff0000, v250\n"
"v_pk_fma_f32 v[68:69], v[26:27], v[22:23], v[68:69] op_sel:[0,1,0]\n"
"v_cvt_pk_bf16_f32 v250, v68, v69\n"
"v_lshlrev_b32 v28, 16, v251\n"
"v_and_b32 v29, 0xffff0000, v251\n"
"v_pk_fma_f32 v[70:71], v[28:29], v[22:23], v[70:71] op_sel:[0,1,0]\n"
"v_cvt_pk_bf16_f32 v251, v70, v71\n"
"ds_write_b128 v20, v[248:251] offset:18432\n"
"v_accvgpr_read_b32 v72, a136\n"
"v_accvgpr_read_b32 v73, a137\n"
"v_accvgpr_read_b32 v74, a138\n"
"v_accvgpr_read_b32 v75, a139\n"
"v_accvgpr_read_b32 v76, a156\n"
"v_accvgpr_read_b32 v77, a157\n"
"v_accvgpr_read_b32 v78, a158\n"
"v_accvgpr_read_b32 v79, a159\n"
"v_pk_mul_f32 v[72:73], v[72:73], v[24:25] op_sel_hi:[1,0]\n"
"v_pk_mul_f32 v[74:75], v[74:75], v[24:25] op_sel_hi:[1,0]\n"
"v_pk_mul_f32 v[76:77], v[76:77], v[24:25] op_sel_hi:[1,0]\n"
"v_pk_mul_f32 v[78:79], v[78:79], v[24:25] op_sel_hi:[1,0]\n"
"s_waitcnt vmcnt(0)\n"
"v_lshlrev_b32 v26, 16, v252\n"
"v_and_b32 v27, 0xffff0000, v252\n"
"v_pk_fma_f32 v[72:73], v[26:27], v[24:25], v[72:73] op_sel:[0,1,0]\n"
"v_cvt_pk_bf16_f32 v252, v72, v73\n"
"v_lshlrev_b32 v28, 16, v253\n"
"v_and_b32 v29, 0xffff0000, v253\n"
"v_pk_fma_f32 v[74:75], v[28:29], v[24:25], v[74:75] op_sel:[0,1,0]\n"
"v_cvt_pk_bf16_f32 v253, v74, v75\n"
"v_lshlrev_b32 v26, 16, v254\n"
"v_and_b32 v27, 0xffff0000, v254\n"
"v_pk_fma_f32 v[76:77], v[26:27], v[24:25], v[76:77] op_sel:[0,1,0]\n"
"v_cvt_pk_bf16_f32 v254, v76, v77\n"
"v_lshlrev_b32 v28, 16, v255\n"
"v_and_b32 v29, 0xffff0000, v255\n"
"v_pk_fma_f32 v[78:79], v[28:29], v[24:25], v[78:79] op_sel:[0,1,0]\n"
"v_cvt_pk_bf16_f32 v255, v78, v79\n"
"ds_write_b128 v20, v[252:255] offset:19456\n"
"s_waitcnt lgkmcnt(0)\n"
"s_barrier\n"
"v_add_u32 v20, 114688, v21\n"
"ds_read_b128 a[0:3], v20 offset:0\n"
"ds_read_b128 a[20:23], v20 offset:5120\n"
"ds_read_b128 a[40:43], v20 offset:10240\n"
"ds_read_b128 a[60:63], v20 offset:15360\n"
"v_add_u32 v137, 57344, v13\n"
"v_add_u32 v138, 57344, v14\n"
"v_add_u32 v139, 57344, v15\n"
"v_add_u32 v140, 57344, v16\n"
"v_add_u32 v141, 57344, v17\n"
"v_add_u32 v142, 57344, v18\n"
"v_add_u32 v143, 57344, v19\n"
"ds_read_b64_tr_b16 v[88:89], v13 offset:0\n"
"ds_read_b64_tr_b16 v[90:91], v13 offset:14336\n"
"ds_read_b128 a[80:83], v20 offset:20480\n"
"ds_read_b64_tr_b16 v[92:93], v13 offset:28672\n"
"ds_read_b64_tr_b16 v[94:95], v13 offset:43008\n"
"ds_read_b128 a[100:103], v20 offset:25600\n"
"ds_read_b64_tr_b16 v[96:97], v137 offset:0\n"
"ds_read_b64_tr_b16 v[98:99], v137 offset:14336\n"
"ds_read_b64_tr_b16 v[100:101], v137 offset:28672\n"
"ds_read_b64_tr_b16 v[102:103], v137 offset:43008\n"
"ds_read_b128 a[120:123], v20 offset:30720\n"
"ds_read_b128 a[140:143], v20 offset:35840\n"
"ds_read_b128 a[4:7], v20 offset:1024\n"
"ds_read_b128 a[24:27], v20 offset:6144\n"
"ds_read_b128 a[44:47], v20 offset:11264\n"
"ds_read_b128 a[64:67], v20 offset:16384\n"
"v_lshlrev_b32 v136, 3, v0\n"
"s_waitcnt lgkmcnt(12)\n"
"v_mfma_f32_16x16x32_bf16 v[48:51], a[160:163], a[0:3], 0\n"
"ds_read_b128 a[84:87], v20 offset:21504\n"
"v_mfma_f32_16x16x32_bf16 v[48:51], a[164:167], a[20:23], v[48:51]\n"
"ds_read_b128 a[104:107], v20 offset:26624\n"
"v_mfma_f32_16x16x32_bf16 v[48:51], a[168:171], a[40:43], v[48:51]\n"
"ds_read_b128 a[124:127], v20 offset:31744\n"
"v_mfma_f32_16x16x32_bf16 v[48:51], a[172:175], a[60:63], v[48:51]\n"
"ds_read_b128 a[144:147], v20 offset:36864\n"
"v_mfma_f32_16x16x32_bf16 v[48:51], v[88:91], a[80:83], v[48:51]\n"
"s_waitcnt lgkmcnt(6)\n"
"ds_read_b128 a[8:11], v20 offset:2048\n"
"v_mfma_f32_16x16x32_bf16 v[48:51], v[92:95], a[100:103], v[48:51]\n"
"ds_read_b128 a[28:31], v20 offset:7168\n"
"v_mfma_f32_16x16x32_bf16 v[48:51], v[96:99], a[120:123], v[48:51]\n"
"ds_read_b128 a[48:51], v20 offset:12288\n"
"v_mfma_f32_16x16x32_bf16 v[48:51], v[100:103], a[140:143], v[48:51]\n"
"ds_read_b128 a[68:71], v20 offset:17408\n"
"v_mfma_f32_16x16x32_bf16 v[52:55], a[160:163], a[4:7], 0\n"
"ds_read_b128 a[88:91], v20 offset:22528\n"
"v_mfma_f32_16x16x32_bf16 v[52:55], a[164:167], a[24:27], v[52:55]\n"
"s_waitcnt lgkmcnt(6)\n"
"ds_read_b128 a[108:111], v20 offset:27648\n"
"v_mfma_f32_16x16x32_bf16 v[52:55], a[168:171], a[44:47], v[52:55]\n"
"ds_read_b128 a[128:131], v20 offset:32768\n"
"v_mfma_f32_16x16x32_bf16 v[52:55], a[172:175], a[64:67], v[52:55]\n"
"ds_read_b128 a[148:151], v20 offset:37888\n"
"v_mfma_f32_16x16x32_bf16 v[52:55], v[88:91], a[84:87], v[52:55]\n"
"ds_read_b128 a[12:15], v20 offset:3072\n"
"v_mfma_f32_16x16x32_bf16 v[52:55], v[92:95], a[104:107], v[52:55]\n"
"ds_read_b128 a[32:35], v20 offset:8192\n"
"v_mfma_f32_16x16x32_bf16 v[52:55], v[96:99], a[124:127], v[52:55]\n"
"s_waitcnt lgkmcnt(6)\n"
"ds_read_b128 a[52:55], v20 offset:13312\n"
"v_cvt_pk_bf16_f32 v120, v48, v49\n"
"v_mfma_f32_16x16x32_bf16 v[52:55], v[100:103], a[144:147], v[52:55]\n"
"ds_read_b128 a[72:75], v20 offset:18432\n"
"v_cvt_pk_bf16_f32 v121, v50, v51\n"
"v_mfma_f32_16x16x32_bf16 v[56:59], a[160:163], a[8:11], 0\n"
"global_store_dwordx2 v136, v[120:121], s[22:23] offset:0 sc1\n"
"v_mfma_f32_16x16x32_bf16 v[56:59], a[164:167], a[28:31], v[56:59]\n"
"ds_read_b128 a[92:95], v20 offset:23552\n"
"v_mfma_f32_16x16x32_bf16 v[56:59], a[168:171], a[48:51], v[56:59]\n"
"ds_read_b128 a[112:115], v20 offset:28672\n"
"v_mfma_f32_16x16x32_bf16 v[56:59], a[172:175], a[68:71], v[56:59]\n"
"s_waitcnt lgkmcnt(4)\n"
"ds_read_b128 a[132:135], v20 offset:33792\n"
"v_mfma_f32_16x16x32_bf16 v[56:59], v[88:91], a[88:91], v[56:59]\n"
"ds_read_b128 a[152:155], v20 offset:38912\n"
"v_mfma_f32_16x16x32_bf16 v[56:59], v[92:95], a[108:111], v[56:59]\n"
"ds_read_b128 a[16:19], v20 offset:4096\n"
"v_mfma_f32_16x16x32_bf16 v[56:59], v[96:99], a[128:131], v[56:59]\n"
"ds_read_b128 a[36:39], v20 offset:9216\n"
"v_cvt_pk_bf16_f32 v122, v52, v53\n"
"v_mfma_f32_16x16x32_bf16 v[56:59], v[100:103], a[148:151], v[56:59]\n"
"ds_read_b128 a[56:59], v20 offset:14336\n"
"v_cvt_pk_bf16_f32 v123, v54, v55\n"
"v_mfma_f32_16x16x32_bf16 v[60:63], a[160:163], a[12:15], 0\n"
"global_store_dwordx2 v136, v[122:123], s[22:23] offset:512 sc1\n"
"v_mfma_f32_16x16x32_bf16 v[60:63], a[164:167], a[32:35], v[60:63]\n"
"s_waitcnt lgkmcnt(4)\n"
"ds_read_b128 a[76:79], v20 offset:19456\n"
"v_mfma_f32_16x16x32_bf16 v[60:63], a[168:171], a[52:55], v[60:63]\n"
"ds_read_b128 a[96:99], v20 offset:24576\n"
"v_mfma_f32_16x16x32_bf16 v[60:63], a[172:175], a[72:75], v[60:63]\n"
"ds_read_b128 a[116:119], v20 offset:29696\n"
"v_mfma_f32_16x16x32_bf16 v[60:63], v[88:91], a[92:95], v[60:63]\n"
"ds_read_b128 a[136:139], v20 offset:34816\n"
"v_mfma_f32_16x16x32_bf16 v[60:63], v[92:95], a[112:115], v[60:63]\n"
"ds_read_b128 a[156:159], v20 offset:39936\n"
"v_mfma_f32_16x16x32_bf16 v[60:63], v[96:99], a[132:135], v[60:63]\n"
"s_waitcnt lgkmcnt(5)\n"
"ds_read_b64_tr_b16 v[104:105], v14 offset:0\n"
"v_cvt_pk_bf16_f32 v124, v56, v57\n"
"v_mfma_f32_16x16x32_bf16 v[60:63], v[100:103], a[152:155], v[60:63]\n"
"ds_read_b64_tr_b16 v[106:107], v14 offset:14336\n"
"v_cvt_pk_bf16_f32 v125, v58, v59\n"
"v_mfma_f32_16x16x32_bf16 v[64:67], a[160:163], a[16:19], 0\n"
"global_store_dwordx2 v136, v[124:125], s[22:23] offset:1024 sc1\n"
"v_mfma_f32_16x16x32_bf16 v[64:67], a[164:167], a[36:39], v[64:67]\n"
"ds_read_b64_tr_b16 v[108:109], v14 offset:28672\n"
"v_mfma_f32_16x16x32_bf16 v[64:67], a[168:171], a[56:59], v[64:67]\n"
"s_waitcnt lgkmcnt(4)\n"
"ds_read_b64_tr_b16 v[110:111], v14 offset:43008\n"
"v_mfma_f32_16x16x32_bf16 v[64:67], a[172:175], a[76:79], v[64:67]\n"
"ds_read_b64_tr_b16 v[112:113], v138 offset:0\n"
"v_mfma_f32_16x16x32_bf16 v[64:67], v[88:91], a[96:99], v[64:67]\n"
"ds_read_b64_tr_b16 v[114:115], v138 offset:14336\n"
"v_mfma_f32_16x16x32_bf16 v[64:67], v[92:95], a[116:119], v[64:67]\n"
"ds_read_b64_tr_b16 v[116:117], v138 offset:28672\n"
"v_mfma_f32_16x16x32_bf16 v[64:67], v[96:99], a[136:139], v[64:67]\n"
"s_waitcnt lgkmcnt(5)\n"
"ds_read_b64_tr_b16 v[118:119], v138 offset:43008\n"
"v_cvt_pk_bf16_f32 v126, v60, v61\n"
"v_mfma_f32_16x16x32_bf16 v[64:67], v[100:103], a[156:159], v[64:67]\n"
"v_cvt_pk_bf16_f32 v127, v62, v63\n"
"v_mfma_f32_16x16x32_bf16 v[68:71], a[176:179], a[0:3], 0\n"
"global_store_dwordx2 v136, v[126:127], s[22:23] offset:1536 sc1\n"
"v_mfma_f32_16x16x32_bf16 v[68:71], a[180:183], a[20:23], v[68:71]\n"
"ds_read_b64_tr_b16 v[88:89], v15 offset:0\n"
"v_mfma_f32_16x16x32_bf16 v[68:71], a[184:187], a[40:43], v[68:71]\n"
"ds_read_b64_tr_b16 v[90:91], v15 offset:14336\n"
"v_mfma_f32_16x16x32_bf16 v[68:71], a[188:191], a[60:63], v[68:71]\n"
"ds_read_b64_tr_b16 v[92:93], v15 offset:28672\n"
"v_mfma_f32_16x16x32_bf16 v[68:71], v[104:107], a[80:83], v[68:71]\n"
"s_waitcnt lgkmcnt(3)\n"
"ds_read_b64_tr_b16 v[94:95], v15 offset:43008\n"
"v_mfma_f32_16x16x32_bf16 v[68:71], v[108:111], a[100:103], v[68:71]\n"
"ds_read_b64_tr_b16 v[96:97], v139 offset:0\n"
"v_mfma_f32_16x16x32_bf16 v[68:71], v[112:115], a[120:123], v[68:71]\n"
"ds_read_b64_tr_b16 v[98:99], v139 offset:14336\n"
"v_cvt_pk_bf16_f32 v128, v64, v65\n"
"v_mfma_f32_16x16x32_bf16 v[68:71], v[116:119], a[140:143], v[68:71]\n"
"ds_read_b64_tr_b16 v[100:101], v139 offset:28672\n"
"v_cvt_pk_bf16_f32 v129, v66, v67\n"
"v_mfma_f32_16x16x32_bf16 v[72:75], a[176:179], a[4:7], 0\n"
"global_store_dwordx2 v136, v[128:129], s[22:23] offset:2048 sc1\n"
"s_nop 1\n"
"v_add_u32 v136, 2560, v136\n"
"v_mfma_f32_16x16x32_bf16 v[72:75], a[180:183], a[24:27], v[72:75]\n"
"ds_read_b64_tr_b16 v[102:103], v139 offset:43008\n"
"v_mfma_f32_16x16x32_bf16 v[72:75], a[184:187], a[44:47], v[72:75]\n"
"v_mfma_f32_16x16x32_bf16 v[72:75], a[188:191], a[64:67], v[72:75]\n"
"v_mfma_f32_16x16x32_bf16 v[72:75], v[104:107], a[84:87], v[72:75]\n"
"v_mfma_f32_16x16x32_bf16 v[72:75], v[108:111], a[104:107], v[72:75]\n"
"v_mfma_f32_16x16x32_bf16 v[72:75], v[112:115], a[124:127], v[72:75]\n"
"v_cvt_pk_bf16_f32 v130, v68, v69\n"
"v_cvt_pk_bf16_f32 v131, v70, v71\n"
"v_mfma_f32_16x16x32_bf16 v[72:75], v[116:119], a[144:147], v[72:75]\n"
"global_store_dwordx2 v136, v[130:131], s[22:23] offset:0 sc1\n"
"v_mfma_f32_16x16x32_bf16 v[76:79], a[176:179], a[8:11], 0\n"
"v_mfma_f32_16x16x32_bf16 v[76:79], a[180:183], a[28:31], v[76:79]\n"
"v_mfma_f32_16x16x32_bf16 v[76:79], a[184:187], a[48:51], v[76:79]\n"
"v_mfma_f32_16x16x32_bf16 v[76:79], a[188:191], a[68:71], v[76:79]\n"
"v_mfma_f32_16x16x32_bf16 v[76:79], v[104:107], a[88:91], v[76:79]\n"
"v_mfma_f32_16x16x32_bf16 v[76:79], v[108:111], a[108:111], v[76:79]\n"
"v_mfma_f32_16x16x32_bf16 v[76:79], v[112:115], a[128:131], v[76:79]\n"
"v_cvt_pk_bf16_f32 v132, v72, v73\n"
"v_cvt_pk_bf16_f32 v133, v74, v75\n"
"v_mfma_f32_16x16x32_bf16 v[76:79], v[116:119], a[148:151], v[76:79]\n"
"global_store_dwordx2 v136, v[132:133], s[22:23] offset:512 sc1\n"
"v_mfma_f32_16x16x32_bf16 v[80:83], a[176:179], a[12:15], 0\n"
"v_mfma_f32_16x16x32_bf16 v[80:83], a[180:183], a[32:35], v[80:83]\n"
"v_mfma_f32_16x16x32_bf16 v[80:83], a[184:187], a[52:55], v[80:83]\n"
"v_mfma_f32_16x16x32_bf16 v[80:83], a[188:191], a[72:75], v[80:83]\n"
"v_mfma_f32_16x16x32_bf16 v[80:83], v[104:107], a[92:95], v[80:83]\n"
"v_mfma_f32_16x16x32_bf16 v[80:83], v[108:111], a[112:115], v[80:83]\n"
"v_mfma_f32_16x16x32_bf16 v[80:83], v[112:115], a[132:135], v[80:83]\n"
"v_cvt_pk_bf16_f32 v134, v76, v77\n"
"v_cvt_pk_bf16_f32 v135, v78, v79\n"
"v_mfma_f32_16x16x32_bf16 v[80:83], v[116:119], a[152:155], v[80:83]\n"
"global_store_dwordx2 v136, v[134:135], s[22:23] offset:1024 sc1\n"
"v_mfma_f32_16x16x32_bf16 v[84:87], a[176:179], a[16:19], 0\n"
"v_mfma_f32_16x16x32_bf16 v[84:87], a[180:183], a[36:39], v[84:87]\n"
"v_mfma_f32_16x16x32_bf16 v[84:87], a[184:187], a[56:59], v[84:87]\n"
"v_mfma_f32_16x16x32_bf16 v[84:87], a[188:191], a[76:79], v[84:87]\n"
"v_mfma_f32_16x16x32_bf16 v[84:87], v[104:107], a[96:99], v[84:87]\n"
"v_mfma_f32_16x16x32_bf16 v[84:87], v[108:111], a[116:119], v[84:87]\n"
"v_mfma_f32_16x16x32_bf16 v[84:87], v[112:115], a[136:139], v[84:87]\n"
"v_cvt_pk_bf16_f32 v120, v80, v81\n"
"v_cvt_pk_bf16_f32 v121, v82, v83\n"
"v_mfma_f32_16x16x32_bf16 v[84:87], v[116:119], a[156:159], v[84:87]\n"
"global_store_dwordx2 v136, v[120:121], s[22:23] offset:1536 sc1\n"
"v_mfma_f32_16x16x32_bf16 v[48:51], a[192:195], a[0:3], 0\n"
"ds_read_b64_tr_b16 v[104:105], v16 offset:0\n"
"v_mfma_f32_16x16x32_bf16 v[48:51], a[196:199], a[20:23], v[48:51]\n"
"ds_read_b64_tr_b16 v[106:107], v16 offset:14336\n"
"v_mfma_f32_16x16x32_bf16 v[48:51], a[200:203], a[40:43], v[48:51]\n"
"ds_read_b64_tr_b16 v[108:109], v16 offset:28672\n"
"v_mfma_f32_16x16x32_bf16 v[48:51], a[204:207], a[60:63], v[48:51]\n"
"s_waitcnt lgkmcnt(3)\n"
"ds_read_b64_tr_b16 v[110:111], v16 offset:43008\n"
"v_mfma_f32_16x16x32_bf16 v[48:51], v[88:91], a[80:83], v[48:51]\n"
"ds_read_b64_tr_b16 v[112:113], v140 offset:0\n"
"v_mfma_f32_16x16x32_bf16 v[48:51], v[92:95], a[100:103], v[48:51]\n"
"ds_read_b64_tr_b16 v[114:115], v140 offset:14336\n"
"v_mfma_f32_16x16x32_bf16 v[48:51], v[96:99], a[120:123], v[48:51]\n"
"ds_read_b64_tr_b16 v[116:117], v140 offset:28672\n"
"v_cvt_pk_bf16_f32 v122, v84, v85\n"
"v_mfma_f32_16x16x32_bf16 v[48:51], v[100:103], a[140:143], v[48:51]\n"
"ds_read_b64_tr_b16 v[118:119], v140 offset:43008\n"
"v_cvt_pk_bf16_f32 v123, v86, v87\n"
"v_mfma_f32_16x16x32_bf16 v[52:55], a[192:195], a[4:7], 0\n"
"global_store_dwordx2 v136, v[122:123], s[22:23] offset:2048 sc1\n"
"s_nop 1\n"
"v_add_u32 v136, 2560, v136\n"
"v_mfma_f32_16x16x32_bf16 v[52:55], a[196:199], a[24:27], v[52:55]\n"
"v_mfma_f32_16x16x32_bf16 v[52:55], a[200:203], a[44:47], v[52:55]\n"
"v_mfma_f32_16x16x32_bf16 v[52:55], a[204:207], a[64:67], v[52:55]\n"
"v_mfma_f32_16x16x32_bf16 v[52:55], v[88:91], a[84:87], v[52:55]\n"
"v_mfma_f32_16x16x32_bf16 v[52:55], v[92:95], a[104:107], v[52:55]\n"
"v_mfma_f32_16x16x32_bf16 v[52:55], v[96:99], a[124:127], v[52:55]\n"
"v_cvt_pk_bf16_f32 v124, v48, v49\n"
"v_cvt_pk_bf16_f32 v125, v50, v51\n"
"v_mfma_f32_16x16x32_bf16 v[52:55], v[100:103], a[144:147], v[52:55]\n"
"global_store_dwordx2 v136, v[124:125], s[22:23] offset:0 sc1\n"
"v_mfma_f32_16x16x32_bf16 v[56:59], a[192:195], a[8:11], 0\n"
"v_mfma_f32_16x16x32_bf16 v[56:59], a[196:199], a[28:31], v[56:59]\n"
"v_mfma_f32_16x16x32_bf16 v[56:59], a[200:203], a[48:51], v[56:59]\n"
"v_mfma_f32_16x16x32_bf16 v[56:59], a[204:207], a[68:71], v[56:59]\n"
"v_mfma_f32_16x16x32_bf16 v[56:59], v[88:91], a[88:91], v[56:59]\n"
"v_mfma_f32_16x16x32_bf16 v[56:59], v[92:95], a[108:111], v[56:59]\n"
"v_mfma_f32_16x16x32_bf16 v[56:59], v[96:99], a[128:131], v[56:59]\n"
"v_cvt_pk_bf16_f32 v126, v52, v53\n"
"v_cvt_pk_bf16_f32 v127, v54, v55\n"
"v_mfma_f32_16x16x32_bf16 v[56:59], v[100:103], a[148:151], v[56:59]\n"
"global_store_dwordx2 v136, v[126:127], s[22:23] offset:512 sc1\n"
"v_mfma_f32_16x16x32_bf16 v[60:63], a[192:195], a[12:15], 0\n"
"v_mfma_f32_16x16x32_bf16 v[60:63], a[196:199], a[32:35], v[60:63]\n"
"v_mfma_f32_16x16x32_bf16 v[60:63], a[200:203], a[52:55], v[60:63]\n"
"v_mfma_f32_16x16x32_bf16 v[60:63], a[204:207], a[72:75], v[60:63]\n"
"v_mfma_f32_16x16x32_bf16 v[60:63], v[88:91], a[92:95], v[60:63]\n"
"v_mfma_f32_16x16x32_bf16 v[60:63], v[92:95], a[112:115], v[60:63]\n"
"v_mfma_f32_16x16x32_bf16 v[60:63], v[96:99], a[132:135], v[60:63]\n"
"v_cvt_pk_bf16_f32 v128, v56, v57\n"
"v_cvt_pk_bf16_f32 v129, v58, v59\n"
"v_mfma_f32_16x16x32_bf16 v[60:63], v[100:103], a[152:155], v[60:63]\n"
"global_store_dwordx2 v136, v[128:129], s[22:23] offset:1024 sc1\n"
"v_mfma_f32_16x16x32_bf16 v[64:67], a[192:195], a[16:19], 0\n"
"v_mfma_f32_16x16x32_bf16 v[64:67], a[196:199], a[36:39], v[64:67]\n"
"v_mfma_f32_16x16x32_bf16 v[64:67], a[200:203], a[56:59], v[64:67]\n"
"v_mfma_f32_16x16x32_bf16 v[64:67], a[204:207], a[76:79], v[64:67]\n"
"v_mfma_f32_16x16x32_bf16 v[64:67], v[88:91], a[96:99], v[64:67]\n"
"v_mfma_f32_16x16x32_bf16 v[64:67], v[92:95], a[116:119], v[64:67]\n"
"v_mfma_f32_16x16x32_bf16 v[64:67], v[96:99], a[136:139], v[64:67]\n"
"v_cvt_pk_bf16_f32 v130, v60, v61\n"
"v_cvt_pk_bf16_f32 v131, v62, v63\n"
"v_mfma_f32_16x16x32_bf16 v[64:67], v[100:103], a[156:159], v[64:67]\n"
"global_store_dwordx2 v136, v[130:131], s[22:23] offset:1536 sc1\n"
"v_mfma_f32_16x16x32_bf16 v[68:71], a[208:211], a[0:3], 0\n"
"ds_read_b64_tr_b16 v[88:89], v17 offset:0\n"
"v_mfma_f32_16x16x32_bf16 v[68:71], a[212:215], a[20:23], v[68:71]\n"
"ds_read_b64_tr_b16 v[90:91], v17 offset:14336\n"
"v_mfma_f32_16x16x32_bf16 v[68:71], a[216:219], a[40:43], v[68:71]\n"
"ds_read_b64_tr_b16 v[92:93], v17 offset:28672\n"
"v_mfma_f32_16x16x32_bf16 v[68:71], a[220:223], a[60:63], v[68:71]\n"
"s_waitcnt lgkmcnt(3)\n"
"ds_read_b64_tr_b16 v[94:95], v17 offset:43008\n"
"v_mfma_f32_16x16x32_bf16 v[68:71], v[104:107], a[80:83], v[68:71]\n"
"ds_read_b64_tr_b16 v[96:97], v141 offset:0\n"
"v_mfma_f32_16x16x32_bf16 v[68:71], v[108:111], a[100:103], v[68:71]\n"
"ds_read_b64_tr_b16 v[98:99], v141 offset:14336\n"
"v_mfma_f32_16x16x32_bf16 v[68:71], v[112:115], a[120:123], v[68:71]\n"
"ds_read_b64_tr_b16 v[100:101], v141 offset:28672\n"
"v_cvt_pk_bf16_f32 v132, v64, v65\n"
"v_mfma_f32_16x16x32_bf16 v[68:71], v[116:119], a[140:143], v[68:71]\n"
"ds_read_b64_tr_b16 v[102:103], v141 offset:43008\n"
"v_cvt_pk_bf16_f32 v133, v66, v67\n"
"v_mfma_f32_16x16x32_bf16 v[72:75], a[208:211], a[4:7], 0\n"
"global_store_dwordx2 v136, v[132:133], s[22:23] offset:2048 sc1\n"
"s_nop 1\n"
"v_add_u32 v136, 2560, v136\n"
"v_mfma_f32_16x16x32_bf16 v[72:75], a[212:215], a[24:27], v[72:75]\n"
"v_mfma_f32_16x16x32_bf16 v[72:75], a[216:219], a[44:47], v[72:75]\n"
"v_mfma_f32_16x16x32_bf16 v[72:75], a[220:223], a[64:67], v[72:75]\n"
"v_mfma_f32_16x16x32_bf16 v[72:75], v[104:107], a[84:87], v[72:75]\n"
"v_mfma_f32_16x16x32_bf16 v[72:75], v[108:111], a[104:107], v[72:75]\n"
"v_mfma_f32_16x16x32_bf16 v[72:75], v[112:115], a[124:127], v[72:75]\n"
"v_cvt_pk_bf16_f32 v134, v68, v69\n"
"v_cvt_pk_bf16_f32 v135, v70, v71\n"
"v_mfma_f32_16x16x32_bf16 v[72:75], v[116:119], a[144:147], v[72:75]\n"
"global_store_dwordx2 v136, v[134:135], s[22:23] offset:0 sc1\n"
"v_mfma_f32_16x16x32_bf16 v[76:79], a[208:211], a[8:11], 0\n"
"v_mfma_f32_16x16x32_bf16 v[76:79], a[212:215], a[28:31], v[76:79]\n"
"v_mfma_f32_16x16x32_bf16 v[76:79], a[216:219], a[48:51], v[76:79]\n"
"v_mfma_f32_16x16x32_bf16 v[76:79], a[220:223], a[68:71], v[76:79]\n"
"v_mfma_f32_16x16x32_bf16 v[76:79], v[104:107], a[88:91], v[76:79]\n"
"v_mfma_f32_16x16x32_bf16 v[76:79], v[108:111], a[108:111], v[76:79]\n"
"v_mfma_f32_16x16x32_bf16 v[76:79], v[112:115], a[128:131], v[76:79]\n"
"v_cvt_pk_bf16_f32 v120, v72, v73\n"
"v_cvt_pk_bf16_f32 v121, v74, v75\n"
"v_mfma_f32_16x16x32_bf16 v[76:79], v[116:119], a[148:151], v[76:79]\n"
"global_store_dwordx2 v136, v[120:121], s[22:23] offset:512 sc1\n"
"v_mfma_f32_16x16x32_bf16 v[80:83], a[208:211], a[12:15], 0\n"
"v_mfma_f32_16x16x32_bf16 v[80:83], a[212:215], a[32:35], v[80:83]\n"
"v_mfma_f32_16x16x32_bf16 v[80:83], a[216:219], a[52:55], v[80:83]\n"
"v_mfma_f32_16x16x32_bf16 v[80:83], a[220:223], a[72:75], v[80:83]\n"
"v_mfma_f32_16x16x32_bf16 v[80:83], v[104:107], a[92:95], v[80:83]\n"
"v_mfma_f32_16x16x32_bf16 v[80:83], v[108:111], a[112:115], v[80:83]\n"
"v_mfma_f32_16x16x32_bf16 v[80:83], v[112:115], a[132:135], v[80:83]\n"
"v_cvt_pk_bf16_f32 v122, v76, v77\n"
"v_cvt_pk_bf16_f32 v123, v78, v79\n"
"v_mfma_f32_16x16x32_bf16 v[80:83], v[116:119], a[152:155], v[80:83]\n"
"global_store_dwordx2 v136, v[122:123], s[22:23] offset:1024 sc1\n"
"v_mfma_f32_16x16x32_bf16 v[84:87], a[208:211], a[16:19], 0\n"
"v_mfma_f32_16x16x32_bf16 v[84:87], a[212:215], a[36:39], v[84:87]\n"
"v_mfma_f32_16x16x32_bf16 v[84:87], a[216:219], a[56:59], v[84:87]\n"
"v_mfma_f32_16x16x32_bf16 v[84:87], a[220:223], a[76:79], v[84:87]\n"
"v_mfma_f32_16x16x32_bf16 v[84:87], v[104:107], a[96:99], v[84:87]\n"
"v_mfma_f32_16x16x32_bf16 v[84:87], v[108:111], a[116:119], v[84:87]\n"
"v_mfma_f32_16x16x32_bf16 v[84:87], v[112:115], a[136:139], v[84:87]\n"
"v_cvt_pk_bf16_f32 v124, v80, v81\n"
"v_cvt_pk_bf16_f32 v125, v82, v83\n"
"v_mfma_f32_16x16x32_bf16 v[84:87], v[116:119], a[156:159], v[84:87]\n"
"global_store_dwordx2 v136, v[124:125], s[22:23] offset:1536 sc1\n"
"v_mfma_f32_16x16x32_bf16 v[48:51], a[224:227], a[0:3], 0\n"
"ds_read_b64_tr_b16 v[104:105], v18 offset:0\n"
"v_mfma_f32_16x16x32_bf16 v[48:51], a[228:231], a[20:23], v[48:51]\n"
"ds_read_b64_tr_b16 v[106:107], v18 offset:14336\n"
"v_mfma_f32_16x16x32_bf16 v[48:51], a[232:235], a[40:43], v[48:51]\n"
"ds_read_b64_tr_b16 v[108:109], v18 offset:28672\n"
"v_mfma_f32_16x16x32_bf16 v[48:51], a[236:239], a[60:63], v[48:51]\n"
"s_waitcnt lgkmcnt(3)\n"
"ds_read_b64_tr_b16 v[110:111], v18 offset:43008\n"
"v_mfma_f32_16x16x32_bf16 v[48:51], v[88:91], a[80:83], v[48:51]\n"
"ds_read_b64_tr_b16 v[112:113], v142 offset:0\n"
"v_mfma_f32_16x16x32_bf16 v[48:51], v[92:95], a[100:103], v[48:51]\n"
"ds_read_b64_tr_b16 v[114:115], v142 offset:14336\n"
"v_mfma_f32_16x16x32_bf16 v[48:51], v[96:99], a[120:123], v[48:51]\n"
"ds_read_b64_tr_b16 v[116:117], v142 offset:28672\n"
"v_cvt_pk_bf16_f32 v126, v84, v85\n"
"v_mfma_f32_16x16x32_bf16 v[48:51], v[100:103], a[140:143], v[48:51]\n"
"ds_read_b64_tr_b16 v[118:119], v142 offset:43008\n"
"v_cvt_pk_bf16_f32 v127, v86, v87\n"
"v_mfma_f32_16x16x32_bf16 v[52:55], a[224:227], a[4:7], 0\n"
"global_store_dwordx2 v136, v[126:127], s[22:23] offset:2048 sc1\n"
"s_nop 1\n"
"v_add_u32 v136, 2560, v136\n"
"v_mfma_f32_16x16x32_bf16 v[52:55], a[228:231], a[24:27], v[52:55]\n"
"v_mfma_f32_16x16x32_bf16 v[52:55], a[232:235], a[44:47], v[52:55]\n"
"v_mfma_f32_16x16x32_bf16 v[52:55], a[236:239], a[64:67], v[52:55]\n"
"v_mfma_f32_16x16x32_bf16 v[52:55], v[88:91], a[84:87], v[52:55]\n"
"v_mfma_f32_16x16x32_bf16 v[52:55], v[92:95], a[104:107], v[52:55]\n"
"v_mfma_f32_16x16x32_bf16 v[52:55], v[96:99], a[124:127], v[52:55]\n"
"v_cvt_pk_bf16_f32 v128, v48, v49\n"
"v_cvt_pk_bf16_f32 v129, v50, v51\n"
"v_mfma_f32_16x16x32_bf16 v[52:55], v[100:103], a[144:147], v[52:55]\n"
"global_store_dwordx2 v136, v[128:129], s[22:23] offset:0 sc1\n"
"v_mfma_f32_16x16x32_bf16 v[56:59], a[224:227], a[8:11], 0\n"
"v_mfma_f32_16x16x32_bf16 v[56:59], a[228:231], a[28:31], v[56:59]\n"
"v_mfma_f32_16x16x32_bf16 v[56:59], a[232:235], a[48:51], v[56:59]\n"
"v_mfma_f32_16x16x32_bf16 v[56:59], a[236:239], a[68:71], v[56:59]\n"
"v_mfma_f32_16x16x32_bf16 v[56:59], v[88:91], a[88:91], v[56:59]\n"
"v_mfma_f32_16x16x32_bf16 v[56:59], v[92:95], a[108:111], v[56:59]\n"
"v_mfma_f32_16x16x32_bf16 v[56:59], v[96:99], a[128:131], v[56:59]\n"
"v_cvt_pk_bf16_f32 v130, v52, v53\n"
"v_cvt_pk_bf16_f32 v131, v54, v55\n"
"v_mfma_f32_16x16x32_bf16 v[56:59], v[100:103], a[148:151], v[56:59]\n"
"global_store_dwordx2 v136, v[130:131], s[22:23] offset:512 sc1\n"
"v_mfma_f32_16x16x32_bf16 v[60:63], a[224:227], a[12:15], 0\n"
"v_mfma_f32_16x16x32_bf16 v[60:63], a[228:231], a[32:35], v[60:63]\n"
"v_mfma_f32_16x16x32_bf16 v[60:63], a[232:235], a[52:55], v[60:63]\n"
"v_mfma_f32_16x16x32_bf16 v[60:63], a[236:239], a[72:75], v[60:63]\n"
"v_mfma_f32_16x16x32_bf16 v[60:63], v[88:91], a[92:95], v[60:63]\n"
"v_mfma_f32_16x16x32_bf16 v[60:63], v[92:95], a[112:115], v[60:63]\n"
"v_mfma_f32_16x16x32_bf16 v[60:63], v[96:99], a[132:135], v[60:63]\n"
"v_cvt_pk_bf16_f32 v132, v56, v57\n"
"v_cvt_pk_bf16_f32 v133, v58, v59\n"
"v_mfma_f32_16x16x32_bf16 v[60:63], v[100:103], a[152:155], v[60:63]\n"
"global_store_dwordx2 v136, v[132:133], s[22:23] offset:1024 sc1\n"
"v_mfma_f32_16x16x32_bf16 v[64:67], a[224:227], a[16:19], 0\n"
"v_mfma_f32_16x16x32_bf16 v[64:67], a[228:231], a[36:39], v[64:67]\n"
"v_mfma_f32_16x16x32_bf16 v[64:67], a[232:235], a[56:59], v[64:67]\n"
"v_mfma_f32_16x16x32_bf16 v[64:67], a[236:239], a[76:79], v[64:67]\n"
"v_mfma_f32_16x16x32_bf16 v[64:67], v[88:91], a[96:99], v[64:67]\n"
"v_mfma_f32_16x16x32_bf16 v[64:67], v[92:95], a[116:119], v[64:67]\n"
"v_mfma_f32_16x16x32_bf16 v[64:67], v[96:99], a[136:139], v[64:67]\n"
"v_cvt_pk_bf16_f32 v134, v60, v61\n"
"v_cvt_pk_bf16_f32 v135, v62, v63\n"
"v_mfma_f32_16x16x32_bf16 v[64:67], v[100:103], a[156:159], v[64:67]\n"
"global_store_dwordx2 v136, v[134:135], s[22:23] offset:1536 sc1\n"
"v_mfma_f32_16x16x32_bf16 v[68:71], a[240:243], a[0:3], 0\n"
"ds_read_b64_tr_b16 v[88:89], v19 offset:0\n"
"v_mfma_f32_16x16x32_bf16 v[68:71], a[244:247], a[20:23], v[68:71]\n"
"ds_read_b64_tr_b16 v[90:91], v19 offset:14336\n"
"v_mfma_f32_16x16x32_bf16 v[68:71], a[248:251], a[40:43], v[68:71]\n"
"ds_read_b64_tr_b16 v[92:93], v19 offset:28672\n"
"v_mfma_f32_16x16x32_bf16 v[68:71], a[252:255], a[60:63], v[68:71]\n"
"s_waitcnt lgkmcnt(3)\n"
"ds_read_b64_tr_b16 v[94:95], v19 offset:43008\n"
"v_mfma_f32_16x16x32_bf16 v[68:71], v[104:107], a[80:83], v[68:71]\n"
"ds_read_b64_tr_b16 v[96:97], v143 offset:0\n"
"v_mfma_f32_16x16x32_bf16 v[68:71], v[108:111], a[100:103], v[68:71]\n"
"ds_read_b64_tr_b16 v[98:99], v143 offset:14336\n"
"v_mfma_f32_16x16x32_bf16 v[68:71], v[112:115], a[120:123], v[68:71]\n"
"ds_read_b64_tr_b16 v[100:101], v143 offset:28672\n"
"v_cvt_pk_bf16_f32 v120, v64, v65\n"
"v_mfma_f32_16x16x32_bf16 v[68:71], v[116:119], a[140:143], v[68:71]\n"
"ds_read_b64_tr_b16 v[102:103], v143 offset:43008\n"
"v_cvt_pk_bf16_f32 v121, v66, v67\n"
"v_mfma_f32_16x16x32_bf16 v[72:75], a[240:243], a[4:7], 0\n"
"global_store_dwordx2 v136, v[120:121], s[22:23] offset:2048 sc1\n"
"s_nop 1\n"
"v_add_u32 v136, 2560, v136\n"
"v_mfma_f32_16x16x32_bf16 v[72:75], a[244:247], a[24:27], v[72:75]\n"
"v_mfma_f32_16x16x32_bf16 v[72:75], a[248:251], a[44:47], v[72:75]\n"
"v_mfma_f32_16x16x32_bf16 v[72:75], a[252:255], a[64:67], v[72:75]\n"
"v_mfma_f32_16x16x32_bf16 v[72:75], v[104:107], a[84:87], v[72:75]\n"
"v_mfma_f32_16x16x32_bf16 v[72:75], v[108:111], a[104:107], v[72:75]\n"
"v_mfma_f32_16x16x32_bf16 v[72:75], v[112:115], a[124:127], v[72:75]\n"
"v_cvt_pk_bf16_f32 v122, v68, v69\n"
"v_cvt_pk_bf16_f32 v123, v70, v71\n"
"v_mfma_f32_16x16x32_bf16 v[72:75], v[116:119], a[144:147], v[72:75]\n"
"global_store_dwordx2 v136, v[122:123], s[22:23] offset:0 sc1\n"
"v_mfma_f32_16x16x32_bf16 v[76:79], a[240:243], a[8:11], 0\n"
"v_mfma_f32_16x16x32_bf16 v[76:79], a[244:247], a[28:31], v[76:79]\n"
"v_mfma_f32_16x16x32_bf16 v[76:79], a[248:251], a[48:51], v[76:79]\n"
"v_mfma_f32_16x16x32_bf16 v[76:79], a[252:255], a[68:71], v[76:79]\n"
"v_mfma_f32_16x16x32_bf16 v[76:79], v[104:107], a[88:91], v[76:79]\n"
"v_mfma_f32_16x16x32_bf16 v[76:79], v[108:111], a[108:111], v[76:79]\n"
"v_mfma_f32_16x16x32_bf16 v[76:79], v[112:115], a[128:131], v[76:79]\n"
"v_cvt_pk_bf16_f32 v124, v72, v73\n"
"v_cvt_pk_bf16_f32 v125, v74, v75\n"
"v_mfma_f32_16x16x32_bf16 v[76:79], v[116:119], a[148:151], v[76:79]\n"
"global_store_dwordx2 v136, v[124:125], s[22:23] offset:512 sc1\n"
"v_mfma_f32_16x16x32_bf16 v[80:83], a[240:243], a[12:15], 0\n"
"v_mfma_f32_16x16x32_bf16 v[80:83], a[244:247], a[32:35], v[80:83]\n"
"v_mfma_f32_16x16x32_bf16 v[80:83], a[248:251], a[52:55], v[80:83]\n"
"v_mfma_f32_16x16x32_bf16 v[80:83], a[252:255], a[72:75], v[80:83]\n"
"v_mfma_f32_16x16x32_bf16 v[80:83], v[104:107], a[92:95], v[80:83]\n"
"v_mfma_f32_16x16x32_bf16 v[80:83], v[108:111], a[112:115], v[80:83]\n"
"v_mfma_f32_16x16x32_bf16 v[80:83], v[112:115], a[132:135], v[80:83]\n"
"v_cvt_pk_bf16_f32 v126, v76, v77\n"
"v_cvt_pk_bf16_f32 v127, v78, v79\n"
"v_mfma_f32_16x16x32_bf16 v[80:83], v[116:119], a[152:155], v[80:83]\n"
"global_store_dwordx2 v136, v[126:127], s[22:23] offset:1024 sc1\n"
"v_mfma_f32_16x16x32_bf16 v[84:87], a[240:243], a[16:19], 0\n"
"v_mfma_f32_16x16x32_bf16 v[84:87], a[244:247], a[36:39], v[84:87]\n"
"v_mfma_f32_16x16x32_bf16 v[84:87], a[248:251], a[56:59], v[84:87]\n"
"v_mfma_f32_16x16x32_bf16 v[84:87], a[252:255], a[76:79], v[84:87]\n"
"v_mfma_f32_16x16x32_bf16 v[84:87], v[104:107], a[96:99], v[84:87]\n"
"v_mfma_f32_16x16x32_bf16 v[84:87], v[108:111], a[116:119], v[84:87]\n"
"v_mfma_f32_16x16x32_bf16 v[84:87], v[112:115], a[136:139], v[84:87]\n"
"v_cvt_pk_bf16_f32 v128, v80, v81\n"
"v_cvt_pk_bf16_f32 v129, v82, v83\n"
"v_mfma_f32_16x16x32_bf16 v[84:87], v[116:119], a[156:159], v[84:87]\n"
"global_store_dwordx2 v136, v[128:129], s[22:23] offset:1536 sc1\n"
"v_mfma_f32_16x16x32_bf16 v[48:51], v[32:35], a[0:3], 0\n"
"v_mfma_f32_16x16x32_bf16 v[48:51], v[36:39], a[20:23], v[48:51]\n"
"v_mfma_f32_16x16x32_bf16 v[48:51], v[40:43], a[40:43], v[48:51]\n"
"v_mfma_f32_16x16x32_bf16 v[48:51], v[44:47], a[60:63], v[48:51]\n"
"s_waitcnt lgkmcnt(0)\n"
"v_mfma_f32_16x16x32_bf16 v[48:51], v[88:91], a[80:83], v[48:51]\n"
"v_mfma_f32_16x16x32_bf16 v[48:51], v[92:95], a[100:103], v[48:51]\n"
"v_mfma_f32_16x16x32_bf16 v[48:51], v[96:99], a[120:123], v[48:51]\n"
"v_cvt_pk_bf16_f32 v130, v84, v85\n"
"v_cvt_pk_bf16_f32 v131, v86, v87\n"
"v_mfma_f32_16x16x32_bf16 v[48:51], v[100:103], a[140:143], v[48:51]\n"
"global_store_dwordx2 v136, v[130:131], s[22:23] offset:2048 sc1\n"
"s_nop 1\n"
"v_add_u32 v136, 2560, v136\n"
"v_mfma_f32_16x16x32_bf16 v[52:55], v[32:35], a[4:7], 0\n"
"v_mfma_f32_16x16x32_bf16 v[52:55], v[36:39], a[24:27], v[52:55]\n"
"v_mfma_f32_16x16x32_bf16 v[52:55], v[40:43], a[44:47], v[52:55]\n"
"v_mfma_f32_16x16x32_bf16 v[52:55], v[44:47], a[64:67], v[52:55]\n"
"v_mfma_f32_16x16x32_bf16 v[52:55], v[88:91], a[84:87], v[52:55]\n"
"v_mfma_f32_16x16x32_bf16 v[52:55], v[92:95], a[104:107], v[52:55]\n"
"v_mfma_f32_16x16x32_bf16 v[52:55], v[96:99], a[124:127], v[52:55]\n"
"v_cvt_pk_bf16_f32 v132, v48, v49\n"
"v_cvt_pk_bf16_f32 v133, v50, v51\n"
"v_mfma_f32_16x16x32_bf16 v[52:55], v[100:103], a[144:147], v[52:55]\n"
"global_store_dwordx2 v136, v[132:133], s[22:23] offset:0 sc1\n"
"v_mfma_f32_16x16x32_bf16 v[56:59], v[32:35], a[8:11], 0\n"
"v_mfma_f32_16x16x32_bf16 v[56:59], v[36:39], a[28:31], v[56:59]\n"
"v_mfma_f32_16x16x32_bf16 v[56:59], v[40:43], a[48:51], v[56:59]\n"
"v_mfma_f32_16x16x32_bf16 v[56:59], v[44:47], a[68:71], v[56:59]\n"
"v_mfma_f32_16x16x32_bf16 v[56:59], v[88:91], a[88:91], v[56:59]\n"
"v_mfma_f32_16x16x32_bf16 v[56:59], v[92:95], a[108:111], v[56:59]\n"
"v_mfma_f32_16x16x32_bf16 v[56:59], v[96:99], a[128:131], v[56:59]\n"
"v_cvt_pk_bf16_f32 v134, v52, v53\n"
"v_cvt_pk_bf16_f32 v135, v54, v55\n"
"v_mfma_f32_16x16x32_bf16 v[56:59], v[100:103], a[148:151], v[56:59]\n"
"global_store_dwordx2 v136, v[134:135], s[22:23] offset:512 sc1\n"
"v_mfma_f32_16x16x32_bf16 v[60:63], v[32:35], a[12:15], 0\n"
"v_mfma_f32_16x16x32_bf16 v[60:63], v[36:39], a[32:35], v[60:63]\n"
"v_mfma_f32_16x16x32_bf16 v[60:63], v[40:43], a[52:55], v[60:63]\n"
"v_mfma_f32_16x16x32_bf16 v[60:63], v[44:47], a[72:75], v[60:63]\n"
"v_mfma_f32_16x16x32_bf16 v[60:63], v[88:91], a[92:95], v[60:63]\n"
"v_mfma_f32_16x16x32_bf16 v[60:63], v[92:95], a[112:115], v[60:63]\n"
"v_mfma_f32_16x16x32_bf16 v[60:63], v[96:99], a[132:135], v[60:63]\n"
"v_cvt_pk_bf16_f32 v120, v56, v57\n"
"v_cvt_pk_bf16_f32 v121, v58, v59\n"
"v_mfma_f32_16x16x32_bf16 v[60:63], v[100:103], a[152:155], v[60:63]\n"
"global_store_dwordx2 v136, v[120:121], s[22:23] offset:1024 sc1\n"
"v_mfma_f32_16x16x32_bf16 v[64:67], v[32:35], a[16:19], 0\n"
"v_mfma_f32_16x16x32_bf16 v[64:67], v[36:39], a[36:39], v[64:67]\n"
"v_mfma_f32_16x16x32_bf16 v[64:67], v[40:43], a[56:59], v[64:67]\n"
"v_mfma_f32_16x16x32_bf16 v[64:67], v[44:47], a[76:79], v[64:67]\n"
"v_mfma_f32_16x16x32_bf16 v[64:67], v[88:91], a[96:99], v[64:67]\n"
"v_mfma_f32_16x16x32_bf16 v[64:67], v[92:95], a[116:119], v[64:67]\n"
"v_mfma_f32_16x16x32_bf16 v[64:67], v[96:99], a[136:139], v[64:67]\n"
"v_cvt_pk_bf16_f32 v122, v60, v61\n"
"v_cvt_pk_bf16_f32 v123, v62, v63\n"
"v_mfma_f32_16x16x32_bf16 v[64:67], v[100:103], a[156:159], v[64:67]\n"
"global_store_dwordx2 v136, v[122:123], s[22:23] offset:1536 sc1\n"
"s_nop 7\n"
"v_cvt_pk_bf16_f32 v124, v64, v65\n"
"v_cvt_pk_bf16_f32 v125, v66, v67\n"
"global_store_dwordx2 v136, v[124:125], s[22:23] offset:2048 sc1\n"
"s_nop 1\n"
"v_add_u32 v136, 2560, v136\n"
"v_and_b32 v8, 15, v0\n"
"v_lshlrev_b32 v9, 2, v8\n"
"v_add_u32 v9, 48128, v9\n"
"global_load_dword v7, v9, s[16:17] sc1\n"
"v_lshlrev_b32 v8, 2, v8\n"
"s_mul_i32 s60, s98, 70\n"
"s_mul_i32 s59, s70, 70\n"
"s_add_u32 s60, s60, s59\n"
"v_add_u32 v10, s60, v0\n"
"s_add_u32 s60, s60, 64\n"
"v_lshlrev_b32 v2, 4, v10\n"
"v_lshrrev_b32 v12, 5, v10\n"
"v_mul_u32_u24 v13, 205, v12\n"
"v_lshrrev_b32 v13, 10, v13\n"
"v_mul_u32_u24 v14, 5, v13\n"
"v_sub_u32 v12, v12, v14\n"
"v_and_b32 v14, 7, v10\n"
"v_lshl_add_u32 v12, v12, 3, v14\n"
"v_mul_u32_u24 v12, 1792, v12\n"
"v_bfe_u32 v14, v10, 3, 2\n"
"v_lshl_add_u32 v13, v13, 2, v14\n"
"v_lshl_add_u32 v4, v13, 3, v12\n"
"v_lshrrev_b32 v11, 2, v0\n"
"v_add_u32 v11, s60, v11\n"
"v_lshlrev_b32 v3, 4, v11\n"
"v_lshrrev_b32 v12, 5, v11\n"
"v_mul_u32_u24 v13, 205, v12\n"
"v_lshrrev_b32 v13, 10, v13\n"
"v_mul_u32_u24 v14, 5, v13\n"
"v_sub_u32 v12, v12, v14\n"
"v_and_b32 v14, 7, v11\n"
"v_lshl_add_u32 v12, v12, 3, v14\n"
"v_mul_u32_u24 v12, 1792, v12\n"
"v_bfe_u32 v14, v11, 3, 2\n"
"v_lshl_add_u32 v13, v13, 2, v14\n"
"v_lshl_add_u32 v5, v13, 3, v12\n"
"v_and_b32 v12, 3, v0\n"
"v_lshl_add_u32 v3, v12, 2, v3\n"
"v_bfe_u32 v13, v0, 1, 1\n"
"v_mul_u32_u24 v13, 896, v13\n"
"v_add_u32 v5, v5, v13\n"
"v_and_b32 v12, 1, v0\n"
"v_lshl_add_u32 v5, v12, 2, v5\n"
"v_mov_b32 v12, s78\n"
"v_mov_b32 v13, s98\n"
"s_waitcnt vmcnt(0)\n"
"s_barrier\n"
"s_mov_b32 s66, 0\n"
"s_mov_b32 s86, 0x40000\n"
"v_mov_b32 v9, 159744\n"
"s_cmp_lg_u32 s70, 0\n"
"s_cbranch_scc1 RDr_%=\n"
"s_mov_b64 exec, 1\n"
"global_store_dword v13, v12, s[94:95] sc1\n"
"s_mov_b64 exec, -1\n"
"RDr_%=:\n"
"s_cmp_lg_u32 s70, 0\n"
"s_cbranch_scc1 NPr_%=\n"
"PGr_%=:\n"
"global_load_dword v6, v8, s[94:95] sc1\n"
"s_waitcnt vmcnt(0)\n"
"v_cmp_eq_u32 vcc, v7, v6\n"
"s_sub_u32 s86, s86, 1\n"
"s_cselect_b32 s67, 0xffff, 0\n"
"s_or_b32 s67, s67, vcc_lo\n"
"s_and_b32 s67, s67, 0xffff\n"
"s_cmp_lg_u32 s67, s66\n"
"s_cbranch_scc1 PXr_%=\n"
"s_sleep 1\n"
"s_branch PGr_%=\n"
"PXr_%=:\n"
"v_mov_b32 v6, s67\n"
"ds_write_b32 v9, v6\n"
"s_waitcnt lgkmcnt(0)\n"
"NPr_%=:\n"
"s_barrier\n"
"ds_read_b32 v6, v9\n"
"v_xor_b32 v9, 4, v9\n"
"s_waitcnt lgkmcnt(0)\n"
"v_readfirstlane_b32 s67, v6\n"
"s_andn2_b32 s60, s67, s66\n"
"s_or_b32 s66, s66, s67\n"
"s_cmp_eq_u32 s66, 0xffff\n"
"s_cselect_b32 s59, 0xffff, 0\n"
"s_cmp_eq_u32 s70, 0\n"
"s_cselect_b32 s60, s59, s60\n"
"s_cmp_lg_u32 s60, 0xffff\n"
"s_cbranch_scc1 SELr_%=\n"
"v_add_u32 v16, 0, v2\n"
"global_load_dwordx4 v[128:131], v16, s[34:35] sc1\n"
"v_add_u32 v17, 2293760, v2\n"
"global_load_dwordx4 v[132:135], v17, s[34:35] sc1\n"
"v_add_u32 v16, 4587520, v2\n"
"global_load_dwordx4 v[136:139], v16, s[34:35] sc1\n"
"v_add_u32 v17, 6881280, v2\n"
"global_load_dwordx4 v[140:143], v17, s[34:35] sc1\n"
"v_add_u32 v16, 9175040, v2\n"
"global_load_dwordx4 v[144:147], v16, s[34:35] sc1\n"
"v_add_u32 v17, 11468800, v2\n"
"global_load_dwordx4 v[148:151], v17, s[34:35] sc1\n"
"v_add_u32 v16, 13762560, v2\n"
"global_load_dwordx4 v[152:155], v16, s[34:35] sc1\n"
"v_add_u32 v17, 16056320, v2\n"
"global_load_dwordx4 v[156:159], v17, s[34:35] sc1\n"
"v_add_u32 v16, 1146880, v2\n"
"global_load_dwordx4 v[160:163], v16, s[34:35] sc1\n"
"v_add_u32 v17, 3440640, v2\n"
"global_load_dwordx4 v[164:167], v17, s[34:35] sc1\n"
"v_add_u32 v16, 5734400, v2\n"
"global_load_dwordx4 v[168:171], v16, s[34:35] sc1\n"
"v_add_u32 v17, 8028160, v2\n"
"global_load_dwordx4 v[172:175], v17, s[34:35] sc1\n"
"v_add_u32 v16, 10321920, v2\n"
"global_load_dwordx4 v[176:179], v16, s[34:35] sc1\n"
"v_add_u32 v17, 12615680, v2\n"
"global_load_dwordx4 v[180:183], v17, s[34:35] sc1\n"
"v_add_u32 v16, 14909440, v2\n"
"global_load_dwordx4 v[184:187], v16, s[34:35] sc1\n"
"v_add_u32 v17, 17203200, v2\n"
"global_load_dwordx4 v[188:191], v17, s[34:35] sc1\n"
"s_mov_b64 exec, 0xffffff\n"
"v_add_u32 v16, 0, v3\n"
"global_load_dword v192, v16, s[34:35] sc1\n"
"v_add_u32 v17, 2293760, v3\n"
"global_load_dword v193, v17, s[34:35] sc1\n"
"v_add_u32 v16, 4587520, v3\n"
"global_load_dword v194, v16, s[34:35] sc1\n"
"v_add_u32 v17, 6881280, v3\n"
"global_load_dword v195, v17, s[34:35] sc1\n"
"v_add_u32 v16, 9175040, v3\n"
"global_load_dword v196, v16, s[34:35] sc1\n"
"v_add_u32 v17, 11468800, v3\n"
"global_load_dword v197, v17, s[34:35] sc1\n"
"v_add_u32 v16, 13762560, v3\n"
"global_load_dword v198, v16, s[34:35] sc1\n"
"v_add_u32 v17, 16056320, v3\n"
"global_load_dword v199, v17, s[34:35] sc1\n"
"v_add_u32 v16, 1146880, v3\n"
"global_load_dword v200, v16, s[34:35] sc1\n"
"v_add_u32 v17, 3440640, v3\n"
"global_load_dword v201, v17, s[34:35] sc1\n"
"v_add_u32 v16, 5734400, v3\n"
"global_load_dword v202, v16, s[34:35] sc1\n"
"v_add_u32 v17, 8028160, v3\n"
"global_load_dword v203, v17, s[34:35] sc1\n"
"v_add_u32 v16, 10321920, v3\n"
"global_load_dword v204, v16, s[34:35] sc1\n"
"v_add_u32 v17, 12615680, v3\n"
"global_load_dword v205, v17, s[34:35] sc1\n"
"v_add_u32 v16, 14909440, v3\n"
"global_load_dword v206, v16, s[34:35] sc1\n"
"v_add_u32 v17, 17203200, v3\n"
"global_load_dword v207, v17, s[34:35] sc1\n"
"s_mov_b64 exec, -1\n"
"s_branch ALLr_%=\n"
"SELr_%=:\n"
"s_bitcmp1_b32 s60, 0\n"
"s_cbranch_scc0 SKr0_%=\n"
"s_mov_b64 s[68:69], s[34:35]\n"
"s_add_u32 s68, s68, 0\n"
"s_addc_u32 s69, s69, 0\n"
"global_load_dwordx4 v[128:131], v2, s[68:69] sc1\n"
"s_mov_b64 exec, 0xffffff\n"
"global_load_dword v192, v3, s[68:69] sc1\n"
"s_mov_b64 exec, -1\n"
"SKr0_%=:\n"
"s_bitcmp1_b32 s60, 1\n"
"s_cbranch_scc0 SKr1_%=\n"
"s_mov_b64 s[68:69], s[34:35]\n"
"s_add_u32 s68, s68, 2293760\n"
"s_addc_u32 s69, s69, 0\n"
"global_load_dwordx4 v[132:135], v2, s[68:69] sc1\n"
"s_mov_b64 exec, 0xffffff\n"
"global_load_dword v193, v3, s[68:69] sc1\n"
"s_mov_b64 exec, -1\n"
"SKr1_%=:\n"
"s_bitcmp1_b32 s60, 2\n"
"s_cbranch_scc0 SKr2_%=\n"
"s_mov_b64 s[68:69], s[34:35]\n"
"s_add_u32 s68, s68, 4587520\n"
"s_addc_u32 s69, s69, 0\n"
"global_load_dwordx4 v[136:139], v2, s[68:69] sc1\n"
"s_mov_b64 exec, 0xffffff\n"
"global_load_dword v194, v3, s[68:69] sc1\n"
"s_mov_b64 exec, -1\n"
"SKr2_%=:\n"
"s_bitcmp1_b32 s60, 3\n"
"s_cbranch_scc0 SKr3_%=\n"
"s_mov_b64 s[68:69], s[34:35]\n"
"s_add_u32 s68, s68, 6881280\n"
"s_addc_u32 s69, s69, 0\n"
"global_load_dwordx4 v[140:143], v2, s[68:69] sc1\n"
"s_mov_b64 exec, 0xffffff\n"
"global_load_dword v195, v3, s[68:69] sc1\n"
"s_mov_b64 exec, -1\n"
"SKr3_%=:\n"
"s_bitcmp1_b32 s60, 4\n"
"s_cbranch_scc0 SKr4_%=\n"
"s_mov_b64 s[68:69], s[34:35]\n"
"s_add_u32 s68, s68, 9175040\n"
"s_addc_u32 s69, s69, 0\n"
"global_load_dwordx4 v[144:147], v2, s[68:69] sc1\n"
"s_mov_b64 exec, 0xffffff\n"
"global_load_dword v196, v3, s[68:69] sc1\n"
"s_mov_b64 exec, -1\n"
"SKr4_%=:\n"
"s_bitcmp1_b32 s60, 5\n"
"s_cbranch_scc0 SKr5_%=\n"
"s_mov_b64 s[68:69], s[34:35]\n"
"s_add_u32 s68, s68, 11468800\n"
"s_addc_u32 s69, s69, 0\n"
"global_load_dwordx4 v[148:151], v2, s[68:69] sc1\n"
"s_mov_b64 exec, 0xffffff\n"
"global_load_dword v197, v3, s[68:69] sc1\n"
"s_mov_b64 exec, -1\n"
"SKr5_%=:\n"
"s_bitcmp1_b32 s60, 6\n"
"s_cbranch_scc0 SKr6_%=\n"
"s_mov_b64 s[68:69], s[34:35]\n"
"s_add_u32 s68, s68, 13762560\n"
"s_addc_u32 s69, s69, 0\n"
"global_load_dwordx4 v[152:155], v2, s[68:69] sc1\n"
"s_mov_b64 exec, 0xffffff\n"
"global_load_dword v198, v3, s[68:69] sc1\n"
"s_mov_b64 exec, -1\n"
"SKr6_%=:\n"
"s_bitcmp1_b32 s60, 7\n"
"s_cbranch_scc0 SKr7_%=\n"
"s_mov_b64 s[68:69], s[34:35]\n"
"s_add_u32 s68, s68, 16056320\n"
"s_addc_u32 s69, s69, 0\n"
"global_load_dwordx4 v[156:159], v2, s[68:69] sc1\n"
"s_mov_b64 exec, 0xffffff\n"
"global_load_dword v199, v3, s[68:69] sc1\n"
"s_mov_b64 exec, -1\n"
"SKr7_%=:\n"
"s_bitcmp1_b32 s60, 8\n"
"s_cbranch_scc0 SKr8_%=\n"
"s_mov_b64 s[68:69], s[34:35]\n"
"s_add_u32 s68, s68, 1146880\n"
"s_addc_u32 s69, s69, 0\n"
"global_load_dwordx4 v[160:163], v2, s[68:69] sc1\n"
"s_mov_b64 exec, 0xffffff\n"
"global_load_dword v200, v3, s[68:69] sc1\n"
"s_mov_b64 exec, -1\n"
"SKr8_%=:\n"
"s_bitcmp1_b32 s60, 9\n"
"s_cbranch_scc0 SKr9_%=\n"
"s_mov_b64 s[68:69], s[34:35]\n"
"s_add_u32 s68, s68, 3440640\n"
"s_addc_u32 s69, s69, 0\n"
"global_load_dwordx4 v[164:167], v2, s[68:69] sc1\n"
"s_mov_b64 exec, 0xffffff\n"
"global_load_dword v201, v3, s[68:69] sc1\n"
"s_mov_b64 exec, -1\n"
"SKr9_%=:\n"
"s_bitcmp1_b32 s60, 10\n"
"s_cbranch_scc0 SKr10_%=\n"
"s_mov_b64 s[68:69], s[34:35]\n"
"s_add_u32 s68, s68, 5734400\n"
"s_addc_u32 s69, s69, 0\n"
"global_load_dwordx4 v[168:171], v2, s[68:69] sc1\n"
"s_mov_b64 exec, 0xffffff\n"
"global_load_dword v202, v3, s[68:69] sc1\n"
"s_mov_b64 exec, -1\n"
"SKr10_%=:\n"
"s_bitcmp1_b32 s60, 11\n"
"s_cbranch_scc0 SKr11_%=\n"
"s_mov_b64 s[68:69], s[34:35]\n"
"s_add_u32 s68, s68, 8028160\n"
"s_addc_u32 s69, s69, 0\n"
"global_load_dwordx4 v[172:175], v2, s[68:69] sc1\n"
"s_mov_b64 exec, 0xffffff\n"
"global_load_dword v203, v3, s[68:69] sc1\n"
"s_mov_b64 exec, -1\n"
"SKr11_%=:\n"
"s_bitcmp1_b32 s60, 12\n"
"s_cbranch_scc0 SKr12_%=\n"
"s_mov_b64 s[68:69], s[34:35]\n"
"s_add_u32 s68, s68, 10321920\n"
"s_addc_u32 s69, s69, 0\n"
"global_load_dwordx4 v[176:179], v2, s[68:69] sc1\n"
"s_mov_b64 exec, 0xffffff\n"
"global_load_dword v204, v3, s[68:69] sc1\n"
"s_mov_b64 exec, -1\n"
"SKr12_%=:\n"
"s_bitcmp1_b32 s60, 13\n"
"s_cbranch_scc0 SKr13_%=\n"
"s_mov_b64 s[68:69], s[34:35]\n"
"s_add_u32 s68, s68, 12615680\n"
"s_addc_u32 s69, s69, 0\n"
"global_load_dwordx4 v[180:183], v2, s[68:69] sc1\n"
"s_mov_b64 exec, 0xffffff\n"
"global_load_dword v205, v3, s[68:69] sc1\n"
"s_mov_b64 exec, -1\n"
"SKr13_%=:\n"
"s_bitcmp1_b32 s60, 14\n"
"s_cbranch_scc0 SKr14_%=\n"
"s_mov_b64 s[68:69], s[34:35]\n"
"s_add_u32 s68, s68, 14909440\n"
"s_addc_u32 s69, s69, 0\n"
"global_load_dwordx4 v[184:187], v2, s[68:69] sc1\n"
"s_mov_b64 exec, 0xffffff\n"
"global_load_dword v206, v3, s[68:69] sc1\n"
"s_mov_b64 exec, -1\n"
"SKr14_%=:\n"
"s_bitcmp1_b32 s60, 15\n"
"s_cbranch_scc0 SKr15_%=\n"
"s_mov_b64 s[68:69], s[34:35]\n"
"s_add_u32 s68, s68, 17203200\n"
"s_addc_u32 s69, s69, 0\n"
"global_load_dwordx4 v[188:191], v2, s[68:69] sc1\n"
"s_mov_b64 exec, 0xffffff\n"
"global_load_dword v207, v3, s[68:69] sc1\n"
"s_mov_b64 exec, -1\n"
"SKr15_%=:\n"
"s_cmp_lg_u32 s66, 0xffff\n"
"s_cbranch_scc1 RDr_%=\n"
"ALLr_%=:\n"
"s_waitcnt vmcnt(0)\n"
"v_lshlrev_b32 v48, 16, v128\n"
"v_and_b32 v49, 0xffff0000, v128\n"
"v_lshlrev_b32 v50, 16, v129\n"
"v_and_b32 v51, 0xffff0000, v129\n"
"v_lshlrev_b32 v52, 16, v130\n"
"v_and_b32 v53, 0xffff0000, v130\n"
"v_lshlrev_b32 v54, 16, v131\n"
"v_and_b32 v55, 0xffff0000, v131\n"
"v_lshlrev_b32 v72, 16, v132\n"
"v_and_b32 v73, 0xffff0000, v132\n"
"v_pk_add_f32 v[48:49], v[48:49], v[72:73]\n"
"v_lshlrev_b32 v74, 16, v133\n"
"v_and_b32 v75, 0xffff0000, v133\n"
"v_pk_add_f32 v[50:51], v[50:51], v[74:75]\n"
"v_lshlrev_b32 v76, 16, v134\n"
"v_and_b32 v77, 0xffff0000, v134\n"
"v_pk_add_f32 v[52:53], v[52:53], v[76:77]\n"
"v_lshlrev_b32 v78, 16, v135\n"
"v_and_b32 v79, 0xffff0000, v135\n"
"v_pk_add_f32 v[54:55], v[54:55], v[78:79]\n"
"v_lshlrev_b32 v72, 16, v136\n"
"v_and_b32 v73, 0xffff0000, v136\n"
"v_pk_add_f32 v[48:49], v[48:49], v[72:73]\n"
"v_lshlrev_b32 v74, 16, v137\n"
"v_and_b32 v75, 0xffff0000, v137\n"
"v_pk_add_f32 v[50:51], v[50:51], v[74:75]\n"
"v_lshlrev_b32 v76, 16, v138\n"
"v_and_b32 v77, 0xffff0000, v138\n"
"v_pk_add_f32 v[52:53], v[52:53], v[76:77]\n"
"v_lshlrev_b32 v78, 16, v139\n"
"v_and_b32 v79, 0xffff0000, v139\n"
"v_pk_add_f32 v[54:55], v[54:55], v[78:79]\n"
"v_lshlrev_b32 v72, 16, v140\n"
"v_and_b32 v73, 0xffff0000, v140\n"
"v_pk_add_f32 v[48:49], v[48:49], v[72:73]\n"
"v_lshlrev_b32 v74, 16, v141\n"
"v_and_b32 v75, 0xffff0000, v141\n"
"v_pk_add_f32 v[50:51], v[50:51], v[74:75]\n"
"v_lshlrev_b32 v76, 16, v142\n"
"v_and_b32 v77, 0xffff0000, v142\n"
"v_pk_add_f32 v[52:53], v[52:53], v[76:77]\n"
"v_lshlrev_b32 v78, 16, v143\n"
"v_and_b32 v79, 0xffff0000, v143\n"
"v_pk_add_f32 v[54:55], v[54:55], v[78:79]\n"
"v_lshlrev_b32 v72, 16, v144\n"
"v_and_b32 v73, 0xffff0000, v144\n"
"v_pk_add_f32 v[48:49], v[48:49], v[72:73]\n"
"v_lshlrev_b32 v74, 16, v145\n"
"v_and_b32 v75, 0xffff0000, v145\n"
"v_pk_add_f32 v[50:51], v[50:51], v[74:75]\n"
"v_lshlrev_b32 v76, 16, v146\n"
"v_and_b32 v77, 0xffff0000, v146\n"
"v_pk_add_f32 v[52:53], v[52:53], v[76:77]\n"
"v_lshlrev_b32 v78, 16, v147\n"
"v_and_b32 v79, 0xffff0000, v147\n"
"v_pk_add_f32 v[54:55], v[54:55], v[78:79]\n"
"v_lshlrev_b32 v72, 16, v148\n"
"v_and_b32 v73, 0xffff0000, v148\n"
"v_pk_add_f32 v[48:49], v[48:49], v[72:73]\n"
"v_lshlrev_b32 v74, 16, v149\n"
"v_and_b32 v75, 0xffff0000, v149\n"
"v_pk_add_f32 v[50:51], v[50:51], v[74:75]\n"
"v_lshlrev_b32 v76, 16, v150\n"
"v_and_b32 v77, 0xffff0000, v150\n"
"v_pk_add_f32 v[52:53], v[52:53], v[76:77]\n"
"v_lshlrev_b32 v78, 16, v151\n"
"v_and_b32 v79, 0xffff0000, v151\n"
"v_pk_add_f32 v[54:55], v[54:55], v[78:79]\n"
"v_lshlrev_b32 v72, 16, v152\n"
"v_and_b32 v73, 0xffff0000, v152\n"
"v_pk_add_f32 v[48:49], v[48:49], v[72:73]\n"
"v_lshlrev_b32 v74, 16, v153\n"
"v_and_b32 v75, 0xffff0000, v153\n"
"v_pk_add_f32 v[50:51], v[50:51], v[74:75]\n"
"v_lshlrev_b32 v76, 16, v154\n"
"v_and_b32 v77, 0xffff0000, v154\n"
"v_pk_add_f32 v[52:53], v[52:53], v[76:77]\n"
"v_lshlrev_b32 v78, 16, v155\n"
"v_and_b32 v79, 0xffff0000, v155\n"
"v_pk_add_f32 v[54:55], v[54:55], v[78:79]\n"
"v_lshlrev_b32 v72, 16, v156\n"
"v_and_b32 v73, 0xffff0000, v156\n"
"v_pk_add_f32 v[48:49], v[48:49], v[72:73]\n"
"v_lshlrev_b32 v74, 16, v157\n"
"v_and_b32 v75, 0xffff0000, v157\n"
"v_pk_add_f32 v[50:51], v[50:51], v[74:75]\n"
"v_lshlrev_b32 v76, 16, v158\n"
"v_and_b32 v77, 0xffff0000, v158\n"
"v_pk_add_f32 v[52:53], v[52:53], v[76:77]\n"
"v_lshlrev_b32 v78, 16, v159\n"
"v_and_b32 v79, 0xffff0000, v159\n"
"v_pk_add_f32 v[54:55], v[54:55], v[78:79]\n"
"v_lshlrev_b32 v72, 16, v160\n"
"v_and_b32 v73, 0xffff0000, v160\n"
"v_pk_add_f32 v[48:49], v[48:49], v[72:73]\n"
"v_lshlrev_b32 v74, 16, v161\n"
"v_and_b32 v75, 0xffff0000, v161\n"
"v_pk_add_f32 v[50:51], v[50:51], v[74:75]\n"
"v_lshlrev_b32 v76, 16, v162\n"
"v_and_b32 v77, 0xffff0000, v162\n"
"v_pk_add_f32 v[52:53], v[52:53], v[76:77]\n"
"v_lshlrev_b32 v78, 16, v163\n"
"v_and_b32 v79, 0xffff0000, v163\n"
"v_pk_add_f32 v[54:55], v[54:55], v[78:79]\n"
"v_lshlrev_b32 v72, 16, v164\n"
"v_and_b32 v73, 0xffff0000, v164\n"
"v_pk_add_f32 v[48:49], v[48:49], v[72:73]\n"
"v_lshlrev_b32 v74, 16, v165\n"
"v_and_b32 v75, 0xffff0000, v165\n"
"v_pk_add_f32 v[50:51], v[50:51], v[74:75]\n"
"v_lshlrev_b32 v76, 16, v166\n"
"v_and_b32 v77, 0xffff0000, v166\n"
"v_pk_add_f32 v[52:53], v[52:53], v[76:77]\n"
"v_lshlrev_b32 v78, 16, v167\n"
"v_and_b32 v79, 0xffff0000, v167\n"
"v_pk_add_f32 v[54:55], v[54:55], v[78:79]\n"
"v_lshlrev_b32 v72, 16, v168\n"
"v_and_b32 v73, 0xffff0000, v168\n"
"v_pk_add_f32 v[48:49], v[48:49], v[72:73]\n"
"v_lshlrev_b32 v74, 16, v169\n"
"v_and_b32 v75, 0xffff0000, v169\n"
"v_pk_add_f32 v[50:51], v[50:51], v[74:75]\n"
"v_lshlrev_b32 v76, 16, v170\n"
"v_and_b32 v77, 0xffff0000, v170\n"
"v_pk_add_f32 v[52:53], v[52:53], v[76:77]\n"
"v_lshlrev_b32 v78, 16, v171\n"
"v_and_b32 v79, 0xffff0000, v171\n"
"v_pk_add_f32 v[54:55], v[54:55], v[78:79]\n"
"v_lshlrev_b32 v72, 16, v172\n"
"v_and_b32 v73, 0xffff0000, v172\n"
"v_pk_add_f32 v[48:49], v[48:49], v[72:73]\n"
"v_lshlrev_b32 v74, 16, v173\n"
"v_and_b32 v75, 0xffff0000, v173\n"
"v_pk_add_f32 v[50:51], v[50:51], v[74:75]\n"
"v_lshlrev_b32 v76, 16, v174\n"
"v_and_b32 v77, 0xffff0000, v174\n"
"v_pk_add_f32 v[52:53], v[52:53], v[76:77]\n"
"v_lshlrev_b32 v78, 16, v175\n"
"v_and_b32 v79, 0xffff0000, v175\n"
"v_pk_add_f32 v[54:55], v[54:55], v[78:79]\n"
"v_lshlrev_b32 v72, 16, v176\n"
"v_and_b32 v73, 0xffff0000, v176\n"
"v_pk_add_f32 v[48:49], v[48:49], v[72:73]\n"
"v_lshlrev_b32 v74, 16, v177\n"
"v_and_b32 v75, 0xffff0000, v177\n"
"v_pk_add_f32 v[50:51], v[50:51], v[74:75]\n"
"v_lshlrev_b32 v76, 16, v178\n"
"v_and_b32 v77, 0xffff0000, v178\n"
"v_pk_add_f32 v[52:53], v[52:53], v[76:77]\n"
"v_lshlrev_b32 v78, 16, v179\n"
"v_and_b32 v79, 0xffff0000, v179\n"
"v_pk_add_f32 v[54:55], v[54:55], v[78:79]\n"
"v_lshlrev_b32 v72, 16, v180\n"
"v_and_b32 v73, 0xffff0000, v180\n"
"v_pk_add_f32 v[48:49], v[48:49], v[72:73]\n"
"v_lshlrev_b32 v74, 16, v181\n"
"v_and_b32 v75, 0xffff0000, v181\n"
"v_pk_add_f32 v[50:51], v[50:51], v[74:75]\n"
"v_lshlrev_b32 v76, 16, v182\n"
"v_and_b32 v77, 0xffff0000, v182\n"
"v_pk_add_f32 v[52:53], v[52:53], v[76:77]\n"
"v_lshlrev_b32 v78, 16, v183\n"
"v_and_b32 v79, 0xffff0000, v183\n"
"v_pk_add_f32 v[54:55], v[54:55], v[78:79]\n"
"v_lshlrev_b32 v72, 16, v184\n"
"v_and_b32 v73, 0xffff0000, v184\n"
"v_pk_add_f32 v[48:49], v[48:49], v[72:73]\n"
"v_lshlrev_b32 v74, 16, v185\n"
"v_and_b32 v75, 0xffff0000, v185\n"
"v_pk_add_f32 v[50:51], v[50:51], v[74:75]\n"
"v_lshlrev_b32 v76, 16, v186\n"
"v_and_b32 v77, 0xffff0000, v186\n"
"v_pk_add_f32 v[52:53], v[52:53], v[76:77]\n"
"v_lshlrev_b32 v78, 16, v187\n"
"v_and_b32 v79, 0xffff0000, v187\n"
"v_pk_add_f32 v[54:55], v[54:55], v[78:79]\n"
"v_lshlrev_b32 v72, 16, v188\n"
"v_and_b32 v73, 0xffff0000, v188\n"
"v_pk_add_f32 v[48:49], v[48:49], v[72:73]\n"
"v_lshlrev_b32 v74, 16, v189\n"
"v_and_b32 v75, 0xffff0000, v189\n"
"v_pk_add_f32 v[50:51], v[50:51], v[74:75]\n"
"v_lshlrev_b32 v76, 16, v190\n"
"v_and_b32 v77, 0xffff0000, v190\n"
"v_pk_add_f32 v[52:53], v[52:53], v[76:77]\n"
"v_lshlrev_b32 v78, 16, v191\n"
"v_and_b32 v79, 0xffff0000, v191\n"
"v_pk_add_f32 v[54:55], v[54:55], v[78:79]\n"
"v_cvt_pk_bf16_f32 v64, v48, v49\n"
"v_cvt_pk_bf16_f32 v65, v50, v51\n"
"v_cvt_pk_bf16_f32 v66, v52, v53\n"
"v_cvt_pk_bf16_f32 v67, v54, v55\n"
"global_store_dwordx2 v4, v[64:65], s[28:29]\n"
"global_store_dwordx2 v4, v[66:67], s[28:29] offset:896\n"
"v_lshlrev_b32 v56, 16, v192\n"
"v_and_b32 v57, 0xffff0000, v192\n"
"v_lshlrev_b32 v74, 16, v193\n"
"v_and_b32 v75, 0xffff0000, v193\n"
"v_pk_add_f32 v[56:57], v[56:57], v[74:75]\n"
"v_lshlrev_b32 v76, 16, v194\n"
"v_and_b32 v77, 0xffff0000, v194\n"
"v_pk_add_f32 v[56:57], v[56:57], v[76:77]\n"
"v_lshlrev_b32 v78, 16, v195\n"
"v_and_b32 v79, 0xffff0000, v195\n"
"v_pk_add_f32 v[56:57], v[56:57], v[78:79]\n"
"v_lshlrev_b32 v72, 16, v196\n"
"v_and_b32 v73, 0xffff0000, v196\n"
"v_pk_add_f32 v[56:57], v[56:57], v[72:73]\n"
"v_lshlrev_b32 v74, 16, v197\n"
"v_and_b32 v75, 0xffff0000, v197\n"
"v_pk_add_f32 v[56:57], v[56:57], v[74:75]\n"
"v_lshlrev_b32 v76, 16, v198\n"
"v_and_b32 v77, 0xffff0000, v198\n"
"v_pk_add_f32 v[56:57], v[56:57], v[76:77]\n"
"v_lshlrev_b32 v78, 16, v199\n"
"v_and_b32 v79, 0xffff0000, v199\n"
"v_pk_add_f32 v[56:57], v[56:57], v[78:79]\n"
"v_lshlrev_b32 v72, 16, v200\n"
"v_and_b32 v73, 0xffff0000, v200\n"
"v_pk_add_f32 v[56:57], v[56:57], v[72:73]\n"
"v_lshlrev_b32 v74, 16, v201\n"
"v_and_b32 v75, 0xffff0000, v201\n"
"v_pk_add_f32 v[56:57], v[56:57], v[74:75]\n"
"v_lshlrev_b32 v76, 16, v202\n"
"v_and_b32 v77, 0xffff0000, v202\n"
"v_pk_add_f32 v[56:57], v[56:57], v[76:77]\n"
"v_lshlrev_b32 v78, 16, v203\n"
"v_and_b32 v79, 0xffff0000, v203\n"
"v_pk_add_f32 v[56:57], v[56:57], v[78:79]\n"
"v_lshlrev_b32 v72, 16, v204\n"
"v_and_b32 v73, 0xffff0000, v204\n"
"v_pk_add_f32 v[56:57], v[56:57], v[72:73]\n"
"v_lshlrev_b32 v74, 16, v205\n"
"v_and_b32 v75, 0xffff0000, v205\n"
"v_pk_add_f32 v[56:57], v[56:57], v[74:75]\n"
"v_lshlrev_b32 v76, 16, v206\n"
"v_and_b32 v77, 0xffff0000, v206\n"
"v_pk_add_f32 v[56:57], v[56:57], v[76:77]\n"
"v_lshlrev_b32 v78, 16, v207\n"
"v_and_b32 v79, 0xffff0000, v207\n"
"v_pk_add_f32 v[56:57], v[56:57], v[78:79]\n"
"v_cvt_pk_bf16_f32 v68, v56, v57\n"
"s_mov_b64 exec, 0xffffff\n"
"global_store_dword v5, v68, s[28:29]\n"
"s_mov_b64 exec, -1\n"
"s_branch END_%=\n"
"HOT_END_%=:\n"
"R1_0_%=:\n"
"s_call_b64 s[74:75], R1_%=\n"
"s_branch R1_0_r_%=\n"
"S1_0_%=:\n"
"s_call_b64 s[74:75], S1_%=\n"
"s_branch S1_0_r_%=\n"
"S2_1_%=:\n"
"s_call_b64 s[74:75], S2_%=\n"
"s_branch S2_1_r_%=\n"
"R0_1_%=:\n"
"s_call_b64 s[74:75], R0_%=\n"
"s_branch R0_1_r_%=\n"
"S3_1_%=:\n"
"s_call_b64 s[74:75], S3_%=\n"
"s_branch S3_1_r_%=\n"
"R1_1_%=:\n"
"s_call_b64 s[74:75], R1_%=\n"
"s_branch R1_1_r_%=\n"
"S1_1_%=:\n"
"s_call_b64 s[74:75], S1_%=\n"
"s_branch S1_1_r_%=\n"
"S2_14_%=:\n"
"s_call_b64 s[74:75], S2_%=\n"
"s_branch S2_14_r_%=\n"
"R0_14_%=:\n"
"s_call_b64 s[74:75], R0_%=\n"
"s_branch R0_14_r_%=\n"
"S3_14_%=:\n"
"s_call_b64 s[74:75], S3_%=\n"
"s_branch S3_14_r_%=\n"
"R1_14_%=:\n"
"s_call_b64 s[74:75], R1_%=\n"
"s_branch R1_14_r_%=\n"
"S1_14_%=:\n"
"s_call_b64 s[74:75], S1_%=\n"
"s_branch S1_14_r_%=\n"
"S2_15_%=:\n"
"s_call_b64 s[74:75], S2_%=\n"
"s_branch S2_15_r_%=\n"
"R0_15_%=:\n"
"s_call_b64 s[74:75], R0_%=\n"
"s_branch R0_15_r_%=\n"
"S3_15_%=:\n"
"s_call_b64 s[74:75], S3_%=\n"
"s_branch S3_15_r_%=\n"
"R1_15_%=:\n"
"s_call_b64 s[74:75], R1_%=\n"
"s_branch R1_15_r_%=\n"
"S1_15_%=:\n"
"s_call_b64 s[74:75], S1_%=\n"
"s_branch S1_15_r_%=\n"
"S2_16_%=:\n"
"s_call_b64 s[74:75], S2_%=\n"
"s_branch S2_16_r_%=\n"
"S3_16_%=:\n"
"s_call_b64 s[74:75], S3_%=\n"
"s_branch S3_16_r_%=\n"
"S2_2v0_%=:\n"
"s_call_b64 s[74:75], S2_%=\n"
"s_branch S2_2v0_r_%=\n"
"R0_2v0_%=:\n"
"s_call_b64 s[74:75], R0_%=\n"
"s_branch R0_2v0_r_%=\n"
"S3_2v0_%=:\n"
"s_call_b64 s[74:75], S3_%=\n"
"s_branch S3_2v0_r_%=\n"
"R1_2v0_%=:\n"
"s_call_b64 s[74:75], R1_%=\n"
"s_branch R1_2v0_r_%=\n"
"S1_2v0_%=:\n"
"s_call_b64 s[74:75], S1_%=\n"
"s_branch S1_2v0_r_%=\n"
"S2_3v0_%=:\n"
"s_call_b64 s[74:75], S2_%=\n"
"s_branch S2_3v0_r_%=\n"
"R0_3v0_%=:\n"
"s_call_b64 s[74:75], R0_%=\n"
"s_branch R0_3v0_r_%=\n"
"S3_3v0_%=:\n"
"s_call_b64 s[74:75], S3_%=\n"
"s_branch S3_3v0_r_%=\n"
"R1_3v0_%=:\n"
"s_call_b64 s[74:75], R1_%=\n"
"s_branch R1_3v0_r_%=\n"
"S1_3v0_%=:\n"
"s_call_b64 s[74:75], S1_%=\n"
"s_branch S1_3v0_r_%=\n"
"R0_%=:\n"
"s_nop 15\n"
"v_max3_f32 v1, v136, v137, v138\n"
"v_max_f32 v1, v1, v139\n"
"v_mov_b32 v2, v1\n"
"v_mov_b32 v3, v1\n"
"s_nop 1\n"
"v_permlane16_swap_b32 v2, v3\n"
"s_nop 0\n"
"v_max_f32 v1, v2, v3\n"
"v_mov_b32 v2, v1\n"
"v_mov_b32 v3, v1\n"
"s_nop 1\n"
"v_permlane32_swap_b32 v2, v3\n"
"s_nop 0\n"
"v_max_f32 v1, v2, v3\n"
"v_max_f32 v1, 0, v1\n"
"v_sub_f32 v136, v136, v1\n"
"v_sub_f32 v137, v137, v1\n"
"v_sub_f32 v138, v138, v1\n"
"v_sub_f32 v139, v139, v1\n"
"v_sub_f32 v76, v76, v1\n"
"v_sub_f32 v77, v77, v1\n"
"v_sub_f32 v78, v78, v1\n"
"v_sub_f32 v79, v79, v1\n"
"v_exp_f32 v2, -v1\n"
"s_nop 0\n"
"v_mul_f32 v33, v33, v2\n"
"v_max3_f32 v1, v140, v141, v142\n"
"v_max_f32 v1, v1, v143\n"
"v_mov_b32 v2, v1\n"
"v_mov_b32 v3, v1\n"
"s_nop 1\n"
"v_permlane16_swap_b32 v2, v3\n"
"s_nop 0\n"
"v_max_f32 v1, v2, v3\n"
"v_mov_b32 v2, v1\n"
"v_mov_b32 v3, v1\n"
"s_nop 1\n"
"v_permlane32_swap_b32 v2, v3\n"
"s_nop 0\n"
"v_max_f32 v1, v2, v3\n"
"v_max_f32 v1, 0, v1\n"
"v_sub_f32 v140, v140, v1\n"
"v_sub_f32 v141, v141, v1\n"
"v_sub_f32 v142, v142, v1\n"
"v_sub_f32 v143, v143, v1\n"
"v_sub_f32 v80, v80, v1\n"
"v_sub_f32 v81, v81, v1\n"
"v_sub_f32 v82, v82, v1\n"
"v_sub_f32 v83, v83, v1\n"
"v_exp_f32 v2, -v1\n"
"s_nop 0\n"
"v_mul_f32 v34, v34, v2\n"
"v_max3_f32 v1, v144, v145, v146\n"
"v_max_f32 v1, v1, v147\n"
"v_mov_b32 v2, v1\n"
"v_mov_b32 v3, v1\n"
"s_nop 1\n"
"v_permlane16_swap_b32 v2, v3\n"
"s_nop 0\n"
"v_max_f32 v1, v2, v3\n"
"v_mov_b32 v2, v1\n"
"v_mov_b32 v3, v1\n"
"s_nop 1\n"
"v_permlane32_swap_b32 v2, v3\n"
"s_nop 0\n"
"v_max_f32 v1, v2, v3\n"
"v_max_f32 v1, 0, v1\n"
"v_sub_f32 v144, v144, v1\n"
"v_sub_f32 v145, v145, v1\n"
"v_sub_f32 v146, v146, v1\n"
"v_sub_f32 v147, v147, v1\n"
"v_sub_f32 v84, v84, v1\n"
"v_sub_f32 v85, v85, v1\n"
"v_sub_f32 v86, v86, v1\n"
"v_sub_f32 v87, v87, v1\n"
"v_exp_f32 v2, -v1\n"
"s_nop 0\n"
"v_mul_f32 v35, v35, v2\n"
"v_max3_f32 v1, v148, v149, v150\n"
"v_max_f32 v1, v1, v151\n"
"v_mov_b32 v2, v1\n"
"v_mov_b32 v3, v1\n"
"s_nop 1\n"
"v_permlane16_swap_b32 v2, v3\n"
"s_nop 0\n"
"v_max_f32 v1, v2, v3\n"
"v_mov_b32 v2, v1\n"
"v_mov_b32 v3, v1\n"
"s_nop 1\n"
"v_permlane32_swap_b32 v2, v3\n"
"s_nop 0\n"
"v_max_f32 v1, v2, v3\n"
"v_max_f32 v1, 0, v1\n"
"v_sub_f32 v148, v148, v1\n"
"v_sub_f32 v149, v149, v1\n"
"v_sub_f32 v150, v150, v1\n"
"v_sub_f32 v151, v151, v1\n"
"v_sub_f32 v88, v88, v1\n"
"v_sub_f32 v89, v89, v1\n"
"v_sub_f32 v90, v90, v1\n"
"v_sub_f32 v91, v91, v1\n"
"v_exp_f32 v2, -v1\n"
"s_nop 0\n"
"v_mul_f32 v36, v36, v2\n"
"v_max3_f32 v1, v152, v153, v154\n"
"v_max_f32 v1, v1, v155\n"
"v_mov_b32 v2, v1\n"
"v_mov_b32 v3, v1\n"
"s_nop 1\n"
"v_permlane16_swap_b32 v2, v3\n"
"s_nop 0\n"
"v_max_f32 v1, v2, v3\n"
"v_mov_b32 v2, v1\n"
"v_mov_b32 v3, v1\n"
"s_nop 1\n"
"v_permlane32_swap_b32 v2, v3\n"
"s_nop 0\n"
"v_max_f32 v1, v2, v3\n"
"v_max_f32 v1, 0, v1\n"
"v_sub_f32 v152, v152, v1\n"
"v_sub_f32 v153, v153, v1\n"
"v_sub_f32 v154, v154, v1\n"
"v_sub_f32 v155, v155, v1\n"
"v_sub_f32 v92, v92, v1\n"
"v_sub_f32 v93, v93, v1\n"
"v_sub_f32 v94, v94, v1\n"
"v_sub_f32 v95, v95, v1\n"
"v_exp_f32 v2, -v1\n"
"s_nop 0\n"
"v_mul_f32 v37, v37, v2\n"
"s_max_u32 s56, s56, 4\n"
"s_add_u32 s57, s57, 1\n"
"s_nop 3\n"
"s_setpc_b64 s[74:75]\n"
"R1_%=:\n"
"s_nop 15\n"
"v_max3_f32 v1, v156, v157, v158\n"
"v_max_f32 v1, v1, v159\n"
"v_mov_b32 v2, v1\n"
"v_mov_b32 v3, v1\n"
"s_nop 1\n"
"v_permlane16_swap_b32 v2, v3\n"
"s_nop 0\n"
"v_max_f32 v1, v2, v3\n"
"v_mov_b32 v2, v1\n"
"v_mov_b32 v3, v1\n"
"s_nop 1\n"
"v_permlane32_swap_b32 v2, v3\n"
"s_nop 0\n"
"v_max_f32 v1, v2, v3\n"
"v_max_f32 v1, 0, v1\n"
"v_sub_f32 v136, v136, v1\n"
"v_sub_f32 v137, v137, v1\n"
"v_sub_f32 v138, v138, v1\n"
"v_sub_f32 v139, v139, v1\n"
"v_sub_f32 v156, v156, v1\n"
"v_sub_f32 v157, v157, v1\n"
"v_sub_f32 v158, v158, v1\n"
"v_sub_f32 v159, v159, v1\n"
"v_sub_f32 v76, v76, v1\n"
"v_sub_f32 v77, v77, v1\n"
"v_sub_f32 v78, v78, v1\n"
"v_sub_f32 v79, v79, v1\n"
"v_exp_f32 v2, -v1\n"
"s_nop 0\n"
"v_mul_f32 v33, v33, v2\n"
"v_exp_f32 v48, v136\n"
"v_exp_f32 v49, v137\n"
"v_exp_f32 v50, v138\n"
"v_exp_f32 v51, v139\n"
"s_nop 0\n"
"v_cvt_pk_bf16_f32 v116, v48, v49\n"
"v_cvt_pk_bf16_f32 v117, v50, v51\n"
"v_max3_f32 v1, v160, v161, v162\n"
"v_max_f32 v1, v1, v163\n"
"v_mov_b32 v2, v1\n"
"v_mov_b32 v3, v1\n"
"s_nop 1\n"
"v_permlane16_swap_b32 v2, v3\n"
"s_nop 0\n"
"v_max_f32 v1, v2, v3\n"
"v_mov_b32 v2, v1\n"
"v_mov_b32 v3, v1\n"
"s_nop 1\n"
"v_permlane32_swap_b32 v2, v3\n"
"s_nop 0\n"
"v_max_f32 v1, v2, v3\n"
"v_max_f32 v1, 0, v1\n"
"v_sub_f32 v140, v140, v1\n"
"v_sub_f32 v141, v141, v1\n"
"v_sub_f32 v142, v142, v1\n"
"v_sub_f32 v143, v143, v1\n"
"v_sub_f32 v160, v160, v1\n"
"v_sub_f32 v161, v161, v1\n"
"v_sub_f32 v162, v162, v1\n"
"v_sub_f32 v163, v163, v1\n"
"v_sub_f32 v80, v80, v1\n"
"v_sub_f32 v81, v81, v1\n"
"v_sub_f32 v82, v82, v1\n"
"v_sub_f32 v83, v83, v1\n"
"v_exp_f32 v2, -v1\n"
"s_nop 0\n"
"v_mul_f32 v34, v34, v2\n"
"v_exp_f32 v48, v140\n"
"v_exp_f32 v49, v141\n"
"v_exp_f32 v50, v142\n"
"v_exp_f32 v51, v143\n"
"s_nop 0\n"
"v_cvt_pk_bf16_f32 v120, v48, v49\n"
"v_cvt_pk_bf16_f32 v121, v50, v51\n"
"v_max3_f32 v1, v164, v165, v166\n"
"v_max_f32 v1, v1, v167\n"
"v_mov_b32 v2, v1\n"
"v_mov_b32 v3, v1\n"
"s_nop 1\n"
"v_permlane16_swap_b32 v2, v3\n"
"s_nop 0\n"
"v_max_f32 v1, v2, v3\n"
"v_mov_b32 v2, v1\n"
"v_mov_b32 v3, v1\n"
"s_nop 1\n"
"v_permlane32_swap_b32 v2, v3\n"
"s_nop 0\n"
"v_max_f32 v1, v2, v3\n"
"v_max_f32 v1, 0, v1\n"
"v_sub_f32 v144, v144, v1\n"
"v_sub_f32 v145, v145, v1\n"
"v_sub_f32 v146, v146, v1\n"
"v_sub_f32 v147, v147, v1\n"
"v_sub_f32 v164, v164, v1\n"
"v_sub_f32 v165, v165, v1\n"
"v_sub_f32 v166, v166, v1\n"
"v_sub_f32 v167, v167, v1\n"
"v_sub_f32 v84, v84, v1\n"
"v_sub_f32 v85, v85, v1\n"
"v_sub_f32 v86, v86, v1\n"
"v_sub_f32 v87, v87, v1\n"
"v_exp_f32 v2, -v1\n"
"s_nop 0\n"
"v_mul_f32 v35, v35, v2\n"
"v_exp_f32 v48, v144\n"
"v_exp_f32 v49, v145\n"
"v_exp_f32 v50, v146\n"
"v_exp_f32 v51, v147\n"
"s_nop 0\n"
"v_cvt_pk_bf16_f32 v124, v48, v49\n"
"v_cvt_pk_bf16_f32 v125, v50, v51\n"
"v_max3_f32 v1, v168, v169, v170\n"
"v_max_f32 v1, v1, v171\n"
"v_mov_b32 v2, v1\n"
"v_mov_b32 v3, v1\n"
"s_nop 1\n"
"v_permlane16_swap_b32 v2, v3\n"
"s_nop 0\n"
"v_max_f32 v1, v2, v3\n"
"v_mov_b32 v2, v1\n"
"v_mov_b32 v3, v1\n"
"s_nop 1\n"
"v_permlane32_swap_b32 v2, v3\n"
"s_nop 0\n"
"v_max_f32 v1, v2, v3\n"
"v_max_f32 v1, 0, v1\n"
"v_sub_f32 v148, v148, v1\n"
"v_sub_f32 v149, v149, v1\n"
"v_sub_f32 v150, v150, v1\n"
"v_sub_f32 v151, v151, v1\n"
"v_sub_f32 v168, v168, v1\n"
"v_sub_f32 v169, v169, v1\n"
"v_sub_f32 v170, v170, v1\n"
"v_sub_f32 v171, v171, v1\n"
"v_sub_f32 v88, v88, v1\n"
"v_sub_f32 v89, v89, v1\n"
"v_sub_f32 v90, v90, v1\n"
"v_sub_f32 v91, v91, v1\n"
"v_exp_f32 v2, -v1\n"
"s_nop 0\n"
"v_mul_f32 v36, v36, v2\n"
"v_exp_f32 v48, v148\n"
"v_exp_f32 v49, v149\n"
"v_exp_f32 v50, v150\n"
"v_exp_f32 v51, v151\n"
"s_nop 0\n"
"v_cvt_pk_bf16_f32 v128, v48, v49\n"
"v_cvt_pk_bf16_f32 v129, v50, v51\n"
"v_max3_f32 v1, v172, v173, v174\n"
"v_max_f32 v1, v1, v175\n"
"v_mov_b32 v2, v1\n"
"v_mov_b32 v3, v1\n"
"s_nop 1\n"
"v_permlane16_swap_b32 v2, v3\n"
"s_nop 0\n"
"v_max_f32 v1, v2, v3\n"
"v_mov_b32 v2, v1\n"
"v_mov_b32 v3, v1\n"
"s_nop 1\n"
"v_permlane32_swap_b32 v2, v3\n"
"s_nop 0\n"
"v_max_f32 v1, v2, v3\n"
"v_max_f32 v1, 0, v1\n"
"v_sub_f32 v152, v152, v1\n"
"v_sub_f32 v153, v153, v1\n"
"v_sub_f32 v154, v154, v1\n"
"v_sub_f32 v155, v155, v1\n"
"v_sub_f32 v172, v172, v1\n"
"v_sub_f32 v173, v173, v1\n"
"v_sub_f32 v174, v174, v1\n"
"v_sub_f32 v175, v175, v1\n"
"v_sub_f32 v92, v92, v1\n"
"v_sub_f32 v93, v93, v1\n"
"v_sub_f32 v94, v94, v1\n"
"v_sub_f32 v95, v95, v1\n"
"v_exp_f32 v2, -v1\n"
"s_nop 0\n"
"v_mul_f32 v37, v37, v2\n"
"v_exp_f32 v48, v152\n"
"v_exp_f32 v49, v153\n"
"v_exp_f32 v50, v154\n"
"v_exp_f32 v51, v155\n"
"s_nop 0\n"
"v_cvt_pk_bf16_f32 v132, v48, v49\n"
"v_cvt_pk_bf16_f32 v133, v50, v51\n"
"s_max_u32 s56, s56, 4\n"
"s_add_u32 s57, s57, 1\n"
"s_nop 3\n"
"s_setpc_b64 s[74:75]\n"
"S1_%=:\n"
"s_nop 15\n"
"v_sub_f32 v38, 0, v76\n"
"v_max_f32 v1, v38, v43\n"
"v_sub_f32 v76, 0, v1\n"
"v_sub_f32 v77, 0, v1\n"
"v_sub_f32 v78, 0, v1\n"
"v_sub_f32 v79, 0, v1\n"
"v_sub_f32 v39, 0, v80\n"
"v_max_f32 v1, v39, v44\n"
"v_sub_f32 v80, 0, v1\n"
"v_sub_f32 v81, 0, v1\n"
"v_sub_f32 v82, 0, v1\n"
"v_sub_f32 v83, 0, v1\n"
"v_sub_f32 v40, 0, v84\n"
"v_max_f32 v1, v40, v45\n"
"v_sub_f32 v84, 0, v1\n"
"v_sub_f32 v85, 0, v1\n"
"v_sub_f32 v86, 0, v1\n"
"v_sub_f32 v87, 0, v1\n"
"v_sub_f32 v41, 0, v88\n"
"v_max_f32 v1, v41, v46\n"
"v_sub_f32 v88, 0, v1\n"
"v_sub_f32 v89, 0, v1\n"
"v_sub_f32 v90, 0, v1\n"
"v_sub_f32 v91, 0, v1\n"
"v_sub_f32 v42, 0, v92\n"
"v_max_f32 v1, v42, v47\n"
"v_sub_f32 v92, 0, v1\n"
"v_sub_f32 v93, 0, v1\n"
"v_sub_f32 v94, 0, v1\n"
"v_sub_f32 v95, 0, v1\n"
"s_sub_u32 s56, s56, 1\n"
"s_nop 3\n"
"s_setpc_b64 s[74:75]\n"
"S2_%=:\n"
"s_nop 15\n"
"v_add_f32 v1, v38, v76\n"
"v_exp_f32 v1, v1\n"
"s_nop 0\n"
"v_mul_f32 v33, v33, v1\n"
"v_add_f32 v1, v39, v80\n"
"v_exp_f32 v1, v1\n"
"s_nop 0\n"
"v_mul_f32 v34, v34, v1\n"
"v_add_f32 v1, v40, v84\n"
"v_exp_f32 v1, v1\n"
"s_nop 0\n"
"v_mul_f32 v35, v35, v1\n"
"v_add_f32 v1, v41, v88\n"
"v_exp_f32 v1, v1\n"
"s_nop 0\n"
"v_mul_f32 v36, v36, v1\n"
"v_add_f32 v1, v42, v92\n"
"v_exp_f32 v1, v1\n"
"s_nop 0\n"
"v_mul_f32 v37, v37, v1\n"
"v_max_f32 v1, v43, v38\n"
"v_sub_f32 v2, v43, v1\n"
"v_sub_f32 v3, v38, v1\n"
"v_exp_f32 v2, v2\n"
"v_exp_f32 v3, v3\n"
"v_mov_b32 v43, v1\n"
"v_accvgpr_read_b32 v4, a0\n"
"v_mul_f32 v4, v4, v2\n"
"v_accvgpr_write_b32 a0, v4\n"
"v_accvgpr_read_b32 v4, a1\n"
"v_mul_f32 v4, v4, v2\n"
"v_accvgpr_write_b32 a1, v4\n"
"v_accvgpr_read_b32 v4, a2\n"
"v_mul_f32 v4, v4, v2\n"
"v_accvgpr_write_b32 a2, v4\n"
"v_accvgpr_read_b32 v4, a3\n"
"v_mul_f32 v4, v4, v2\n"
"v_accvgpr_write_b32 a3, v4\n"
"v_accvgpr_read_b32 v4, a20\n"
"v_mul_f32 v4, v4, v2\n"
"v_accvgpr_write_b32 a20, v4\n"
"v_accvgpr_read_b32 v4, a21\n"
"v_mul_f32 v4, v4, v2\n"
"v_accvgpr_write_b32 a21, v4\n"
"v_accvgpr_read_b32 v4, a22\n"
"v_mul_f32 v4, v4, v2\n"
"v_accvgpr_write_b32 a22, v4\n"
"v_accvgpr_read_b32 v4, a23\n"
"v_mul_f32 v4, v4, v2\n"
"v_accvgpr_write_b32 a23, v4\n"
"v_accvgpr_read_b32 v4, a40\n"
"v_mul_f32 v4, v4, v2\n"
"v_accvgpr_write_b32 a40, v4\n"
"v_accvgpr_read_b32 v4, a41\n"
"v_mul_f32 v4, v4, v2\n"
"v_accvgpr_write_b32 a41, v4\n"
"v_accvgpr_read_b32 v4, a42\n"
"v_mul_f32 v4, v4, v2\n"
"v_accvgpr_write_b32 a42, v4\n"
"v_accvgpr_read_b32 v4, a43\n"
"v_mul_f32 v4, v4, v2\n"
"v_accvgpr_write_b32 a43, v4\n"
"v_accvgpr_read_b32 v4, a60\n"
"v_mul_f32 v4, v4, v2\n"
"v_accvgpr_write_b32 a60, v4\n"
"v_accvgpr_read_b32 v4, a61\n"
"v_mul_f32 v4, v4, v2\n"
"v_accvgpr_write_b32 a61, v4\n"
"v_accvgpr_read_b32 v4, a62\n"
"v_mul_f32 v4, v4, v2\n"
"v_accvgpr_write_b32 a62, v4\n"
"v_accvgpr_read_b32 v4, a63\n"
"v_mul_f32 v4, v4, v2\n"
"v_accvgpr_write_b32 a63, v4\n"
"v_accvgpr_read_b32 v4, a80\n"
"v_mul_f32 v4, v4, v2\n"
"v_accvgpr_write_b32 a80, v4\n"
"v_accvgpr_read_b32 v4, a81\n"
"v_mul_f32 v4, v4, v2\n"
"v_accvgpr_write_b32 a81, v4\n"
"v_accvgpr_read_b32 v4, a82\n"
"v_mul_f32 v4, v4, v2\n"
"v_accvgpr_write_b32 a82, v4\n"
"v_accvgpr_read_b32 v4, a83\n"
"v_mul_f32 v4, v4, v2\n"
"v_accvgpr_write_b32 a83, v4\n"
"v_accvgpr_read_b32 v4, a100\n"
"v_mul_f32 v4, v4, v2\n"
"v_accvgpr_write_b32 a100, v4\n"
"v_accvgpr_read_b32 v4, a101\n"
"v_mul_f32 v4, v4, v2\n"
"v_accvgpr_write_b32 a101, v4\n"
"v_accvgpr_read_b32 v4, a102\n"
"v_mul_f32 v4, v4, v2\n"
"v_accvgpr_write_b32 a102, v4\n"
"v_accvgpr_read_b32 v4, a103\n"
"v_mul_f32 v4, v4, v2\n"
"v_accvgpr_write_b32 a103, v4\n"
"v_accvgpr_read_b32 v4, a120\n"
"v_mul_f32 v4, v4, v2\n"
"v_accvgpr_write_b32 a120, v4\n"
"v_accvgpr_read_b32 v4, a121\n"
"v_mul_f32 v4, v4, v2\n"
"v_accvgpr_write_b32 a121, v4\n"
"v_accvgpr_read_b32 v4, a122\n"
"v_mul_f32 v4, v4, v2\n"
"v_accvgpr_write_b32 a122, v4\n"
"v_accvgpr_read_b32 v4, a123\n"
"v_mul_f32 v4, v4, v2\n"
"v_accvgpr_write_b32 a123, v4\n"
"v_accvgpr_read_b32 v4, a140\n"
"v_mul_f32 v4, v4, v2\n"
"v_accvgpr_write_b32 a140, v4\n"
"v_accvgpr_read_b32 v4, a141\n"
"v_mul_f32 v4, v4, v2\n"
"v_accvgpr_write_b32 a141, v4\n"
"v_accvgpr_read_b32 v4, a142\n"
"v_mul_f32 v4, v4, v2\n"
"v_accvgpr_write_b32 a142, v4\n"
"v_accvgpr_read_b32 v4, a143\n"
"v_mul_f32 v4, v4, v2\n"
"v_accvgpr_write_b32 a143, v4\n"
"v_lshlrev_b32 v4, 16, v116\n"
"v_and_b32 v5, 0xffff0000, v116\n"
"v_mul_f32 v4, v4, v3\n"
"v_mul_f32 v5, v5, v3\n"
"v_cvt_pk_bf16_f32 v116, v4, v5\n"
"v_lshlrev_b32 v4, 16, v117\n"
"v_and_b32 v5, 0xffff0000, v117\n"
"v_mul_f32 v4, v4, v3\n"
"v_mul_f32 v5, v5, v3\n"
"v_cvt_pk_bf16_f32 v117, v4, v5\n"
"v_lshlrev_b32 v4, 16, v118\n"
"v_and_b32 v5, 0xffff0000, v118\n"
"v_mul_f32 v4, v4, v3\n"
"v_mul_f32 v5, v5, v3\n"
"v_cvt_pk_bf16_f32 v118, v4, v5\n"
"v_lshlrev_b32 v4, 16, v119\n"
"v_and_b32 v5, 0xffff0000, v119\n"
"v_mul_f32 v4, v4, v3\n"
"v_mul_f32 v5, v5, v3\n"
"v_cvt_pk_bf16_f32 v119, v4, v5\n"
"v_max_f32 v1, v44, v39\n"
"v_sub_f32 v2, v44, v1\n"
"v_sub_f32 v3, v39, v1\n"
"v_exp_f32 v2, v2\n"
"v_exp_f32 v3, v3\n"
"v_mov_b32 v44, v1\n"
"v_accvgpr_read_b32 v4, a4\n"
"v_mul_f32 v4, v4, v2\n"
"v_accvgpr_write_b32 a4, v4\n"
"v_accvgpr_read_b32 v4, a5\n"
"v_mul_f32 v4, v4, v2\n"
"v_accvgpr_write_b32 a5, v4\n"
"v_accvgpr_read_b32 v4, a6\n"
"v_mul_f32 v4, v4, v2\n"
"v_accvgpr_write_b32 a6, v4\n"
"v_accvgpr_read_b32 v4, a7\n"
"v_mul_f32 v4, v4, v2\n"
"v_accvgpr_write_b32 a7, v4\n"
"v_accvgpr_read_b32 v4, a24\n"
"v_mul_f32 v4, v4, v2\n"
"v_accvgpr_write_b32 a24, v4\n"
"v_accvgpr_read_b32 v4, a25\n"
"v_mul_f32 v4, v4, v2\n"
"v_accvgpr_write_b32 a25, v4\n"
"v_accvgpr_read_b32 v4, a26\n"
"v_mul_f32 v4, v4, v2\n"
"v_accvgpr_write_b32 a26, v4\n"
"v_accvgpr_read_b32 v4, a27\n"
"v_mul_f32 v4, v4, v2\n"
"v_accvgpr_write_b32 a27, v4\n"
"v_accvgpr_read_b32 v4, a44\n"
"v_mul_f32 v4, v4, v2\n"
"v_accvgpr_write_b32 a44, v4\n"
"v_accvgpr_read_b32 v4, a45\n"
"v_mul_f32 v4, v4, v2\n"
"v_accvgpr_write_b32 a45, v4\n"
"v_accvgpr_read_b32 v4, a46\n"
"v_mul_f32 v4, v4, v2\n"
"v_accvgpr_write_b32 a46, v4\n"
"v_accvgpr_read_b32 v4, a47\n"
"v_mul_f32 v4, v4, v2\n"
"v_accvgpr_write_b32 a47, v4\n"
"v_accvgpr_read_b32 v4, a64\n"
"v_mul_f32 v4, v4, v2\n"
"v_accvgpr_write_b32 a64, v4\n"
"v_accvgpr_read_b32 v4, a65\n"
"v_mul_f32 v4, v4, v2\n"
"v_accvgpr_write_b32 a65, v4\n"
"v_accvgpr_read_b32 v4, a66\n"
"v_mul_f32 v4, v4, v2\n"
"v_accvgpr_write_b32 a66, v4\n"
"v_accvgpr_read_b32 v4, a67\n"
"v_mul_f32 v4, v4, v2\n"
"v_accvgpr_write_b32 a67, v4\n"
"v_accvgpr_read_b32 v4, a84\n"
"v_mul_f32 v4, v4, v2\n"
"v_accvgpr_write_b32 a84, v4\n"
"v_accvgpr_read_b32 v4, a85\n"
"v_mul_f32 v4, v4, v2\n"
"v_accvgpr_write_b32 a85, v4\n"
"v_accvgpr_read_b32 v4, a86\n"
"v_mul_f32 v4, v4, v2\n"
"v_accvgpr_write_b32 a86, v4\n"
"v_accvgpr_read_b32 v4, a87\n"
"v_mul_f32 v4, v4, v2\n"
"v_accvgpr_write_b32 a87, v4\n"
"v_accvgpr_read_b32 v4, a104\n"
"v_mul_f32 v4, v4, v2\n"
"v_accvgpr_write_b32 a104, v4\n"
"v_accvgpr_read_b32 v4, a105\n"
"v_mul_f32 v4, v4, v2\n"
"v_accvgpr_write_b32 a105, v4\n"
"v_accvgpr_read_b32 v4, a106\n"
"v_mul_f32 v4, v4, v2\n"
"v_accvgpr_write_b32 a106, v4\n"
"v_accvgpr_read_b32 v4, a107\n"
"v_mul_f32 v4, v4, v2\n"
"v_accvgpr_write_b32 a107, v4\n"
"v_accvgpr_read_b32 v4, a124\n"
"v_mul_f32 v4, v4, v2\n"
"v_accvgpr_write_b32 a124, v4\n"
"v_accvgpr_read_b32 v4, a125\n"
"v_mul_f32 v4, v4, v2\n"
"v_accvgpr_write_b32 a125, v4\n"
"v_accvgpr_read_b32 v4, a126\n"
"v_mul_f32 v4, v4, v2\n"
"v_accvgpr_write_b32 a126, v4\n"
"v_accvgpr_read_b32 v4, a127\n"
"v_mul_f32 v4, v4, v2\n"
"v_accvgpr_write_b32 a127, v4\n"
"v_accvgpr_read_b32 v4, a144\n"
"v_mul_f32 v4, v4, v2\n"
"v_accvgpr_write_b32 a144, v4\n"
"v_accvgpr_read_b32 v4, a145\n"
"v_mul_f32 v4, v4, v2\n"
"v_accvgpr_write_b32 a145, v4\n"
"v_accvgpr_read_b32 v4, a146\n"
"v_mul_f32 v4, v4, v2\n"
"v_accvgpr_write_b32 a146, v4\n"
"v_accvgpr_read_b32 v4, a147\n"
"v_mul_f32 v4, v4, v2\n"
"v_accvgpr_write_b32 a147, v4\n"
"v_lshlrev_b32 v4, 16, v120\n"
"v_and_b32 v5, 0xffff0000, v120\n"
"v_mul_f32 v4, v4, v3\n"
"v_mul_f32 v5, v5, v3\n"
"v_cvt_pk_bf16_f32 v120, v4, v5\n"
"v_lshlrev_b32 v4, 16, v121\n"
"v_and_b32 v5, 0xffff0000, v121\n"
"v_mul_f32 v4, v4, v3\n"
"v_mul_f32 v5, v5, v3\n"
"v_cvt_pk_bf16_f32 v121, v4, v5\n"
"v_lshlrev_b32 v4, 16, v122\n"
"v_and_b32 v5, 0xffff0000, v122\n"
"v_mul_f32 v4, v4, v3\n"
"v_mul_f32 v5, v5, v3\n"
"v_cvt_pk_bf16_f32 v122, v4, v5\n"
"v_lshlrev_b32 v4, 16, v123\n"
"v_and_b32 v5, 0xffff0000, v123\n"
"v_mul_f32 v4, v4, v3\n"
"v_mul_f32 v5, v5, v3\n"
"v_cvt_pk_bf16_f32 v123, v4, v5\n"
"v_max_f32 v1, v45, v40\n"
"v_sub_f32 v2, v45, v1\n"
"v_sub_f32 v3, v40, v1\n"
"v_exp_f32 v2, v2\n"
"v_exp_f32 v3, v3\n"
"v_mov_b32 v45, v1\n"
"v_accvgpr_read_b32 v4, a8\n"
"v_mul_f32 v4, v4, v2\n"
"v_accvgpr_write_b32 a8, v4\n"
"v_accvgpr_read_b32 v4, a9\n"
"v_mul_f32 v4, v4, v2\n"
"v_accvgpr_write_b32 a9, v4\n"
"v_accvgpr_read_b32 v4, a10\n"
"v_mul_f32 v4, v4, v2\n"
"v_accvgpr_write_b32 a10, v4\n"
"v_accvgpr_read_b32 v4, a11\n"
"v_mul_f32 v4, v4, v2\n"
"v_accvgpr_write_b32 a11, v4\n"
"v_accvgpr_read_b32 v4, a28\n"
"v_mul_f32 v4, v4, v2\n"
"v_accvgpr_write_b32 a28, v4\n"
"v_accvgpr_read_b32 v4, a29\n"
"v_mul_f32 v4, v4, v2\n"
"v_accvgpr_write_b32 a29, v4\n"
"v_accvgpr_read_b32 v4, a30\n"
"v_mul_f32 v4, v4, v2\n"
"v_accvgpr_write_b32 a30, v4\n"
"v_accvgpr_read_b32 v4, a31\n"
"v_mul_f32 v4, v4, v2\n"
"v_accvgpr_write_b32 a31, v4\n"
"v_accvgpr_read_b32 v4, a48\n"
"v_mul_f32 v4, v4, v2\n"
"v_accvgpr_write_b32 a48, v4\n"
"v_accvgpr_read_b32 v4, a49\n"
"v_mul_f32 v4, v4, v2\n"
"v_accvgpr_write_b32 a49, v4\n"
"v_accvgpr_read_b32 v4, a50\n"
"v_mul_f32 v4, v4, v2\n"
"v_accvgpr_write_b32 a50, v4\n"
"v_accvgpr_read_b32 v4, a51\n"
"v_mul_f32 v4, v4, v2\n"
"v_accvgpr_write_b32 a51, v4\n"
"v_accvgpr_read_b32 v4, a68\n"
"v_mul_f32 v4, v4, v2\n"
"v_accvgpr_write_b32 a68, v4\n"
"v_accvgpr_read_b32 v4, a69\n"
"v_mul_f32 v4, v4, v2\n"
"v_accvgpr_write_b32 a69, v4\n"
"v_accvgpr_read_b32 v4, a70\n"
"v_mul_f32 v4, v4, v2\n"
"v_accvgpr_write_b32 a70, v4\n"
"v_accvgpr_read_b32 v4, a71\n"
"v_mul_f32 v4, v4, v2\n"
"v_accvgpr_write_b32 a71, v4\n"
"v_accvgpr_read_b32 v4, a88\n"
"v_mul_f32 v4, v4, v2\n"
"v_accvgpr_write_b32 a88, v4\n"
"v_accvgpr_read_b32 v4, a89\n"
"v_mul_f32 v4, v4, v2\n"
"v_accvgpr_write_b32 a89, v4\n"
"v_accvgpr_read_b32 v4, a90\n"
"v_mul_f32 v4, v4, v2\n"
"v_accvgpr_write_b32 a90, v4\n"
"v_accvgpr_read_b32 v4, a91\n"
"v_mul_f32 v4, v4, v2\n"
"v_accvgpr_write_b32 a91, v4\n"
"v_accvgpr_read_b32 v4, a108\n"
"v_mul_f32 v4, v4, v2\n"
"v_accvgpr_write_b32 a108, v4\n"
"v_accvgpr_read_b32 v4, a109\n"
"v_mul_f32 v4, v4, v2\n"
"v_accvgpr_write_b32 a109, v4\n"
"v_accvgpr_read_b32 v4, a110\n"
"v_mul_f32 v4, v4, v2\n"
"v_accvgpr_write_b32 a110, v4\n"
"v_accvgpr_read_b32 v4, a111\n"
"v_mul_f32 v4, v4, v2\n"
"v_accvgpr_write_b32 a111, v4\n"
"v_accvgpr_read_b32 v4, a128\n"
"v_mul_f32 v4, v4, v2\n"
"v_accvgpr_write_b32 a128, v4\n"
"v_accvgpr_read_b32 v4, a129\n"
"v_mul_f32 v4, v4, v2\n"
"v_accvgpr_write_b32 a129, v4\n"
"v_accvgpr_read_b32 v4, a130\n"
"v_mul_f32 v4, v4, v2\n"
"v_accvgpr_write_b32 a130, v4\n"
"v_accvgpr_read_b32 v4, a131\n"
"v_mul_f32 v4, v4, v2\n"
"v_accvgpr_write_b32 a131, v4\n"
"v_accvgpr_read_b32 v4, a148\n"
"v_mul_f32 v4, v4, v2\n"
"v_accvgpr_write_b32 a148, v4\n"
"v_accvgpr_read_b32 v4, a149\n"
"v_mul_f32 v4, v4, v2\n"
"v_accvgpr_write_b32 a149, v4\n"
"v_accvgpr_read_b32 v4, a150\n"
"v_mul_f32 v4, v4, v2\n"
"v_accvgpr_write_b32 a150, v4\n"
"v_accvgpr_read_b32 v4, a151\n"
"v_mul_f32 v4, v4, v2\n"
"v_accvgpr_write_b32 a151, v4\n"
"v_lshlrev_b32 v4, 16, v124\n"
"v_and_b32 v5, 0xffff0000, v124\n"
"v_mul_f32 v4, v4, v3\n"
"v_mul_f32 v5, v5, v3\n"
"v_cvt_pk_bf16_f32 v124, v4, v5\n"
"v_lshlrev_b32 v4, 16, v125\n"
"v_and_b32 v5, 0xffff0000, v125\n"
"v_mul_f32 v4, v4, v3\n"
"v_mul_f32 v5, v5, v3\n"
"v_cvt_pk_bf16_f32 v125, v4, v5\n"
"v_lshlrev_b32 v4, 16, v126\n"
"v_and_b32 v5, 0xffff0000, v126\n"
"v_mul_f32 v4, v4, v3\n"
"v_mul_f32 v5, v5, v3\n"
"v_cvt_pk_bf16_f32 v126, v4, v5\n"
"v_lshlrev_b32 v4, 16, v127\n"
"v_and_b32 v5, 0xffff0000, v127\n"
"v_mul_f32 v4, v4, v3\n"
"v_mul_f32 v5, v5, v3\n"
"v_cvt_pk_bf16_f32 v127, v4, v5\n"
"v_max_f32 v1, v46, v41\n"
"v_sub_f32 v2, v46, v1\n"
"v_sub_f32 v3, v41, v1\n"
"v_exp_f32 v2, v2\n"
"v_exp_f32 v3, v3\n"
"v_mov_b32 v46, v1\n"
"v_accvgpr_read_b32 v4, a12\n"
"v_mul_f32 v4, v4, v2\n"
"v_accvgpr_write_b32 a12, v4\n"
"v_accvgpr_read_b32 v4, a13\n"
"v_mul_f32 v4, v4, v2\n"
"v_accvgpr_write_b32 a13, v4\n"
"v_accvgpr_read_b32 v4, a14\n"
"v_mul_f32 v4, v4, v2\n"
"v_accvgpr_write_b32 a14, v4\n"
"v_accvgpr_read_b32 v4, a15\n"
"v_mul_f32 v4, v4, v2\n"
"v_accvgpr_write_b32 a15, v4\n"
"v_accvgpr_read_b32 v4, a32\n"
"v_mul_f32 v4, v4, v2\n"
"v_accvgpr_write_b32 a32, v4\n"
"v_accvgpr_read_b32 v4, a33\n"
"v_mul_f32 v4, v4, v2\n"
"v_accvgpr_write_b32 a33, v4\n"
"v_accvgpr_read_b32 v4, a34\n"
"v_mul_f32 v4, v4, v2\n"
"v_accvgpr_write_b32 a34, v4\n"
"v_accvgpr_read_b32 v4, a35\n"
"v_mul_f32 v4, v4, v2\n"
"v_accvgpr_write_b32 a35, v4\n"
"v_accvgpr_read_b32 v4, a52\n"
"v_mul_f32 v4, v4, v2\n"
"v_accvgpr_write_b32 a52, v4\n"
"v_accvgpr_read_b32 v4, a53\n"
"v_mul_f32 v4, v4, v2\n"
"v_accvgpr_write_b32 a53, v4\n"
"v_accvgpr_read_b32 v4, a54\n"
"v_mul_f32 v4, v4, v2\n"
"v_accvgpr_write_b32 a54, v4\n"
"v_accvgpr_read_b32 v4, a55\n"
"v_mul_f32 v4, v4, v2\n"
"v_accvgpr_write_b32 a55, v4\n"
"v_accvgpr_read_b32 v4, a72\n"
"v_mul_f32 v4, v4, v2\n"
"v_accvgpr_write_b32 a72, v4\n"
"v_accvgpr_read_b32 v4, a73\n"
"v_mul_f32 v4, v4, v2\n"
"v_accvgpr_write_b32 a73, v4\n"
"v_accvgpr_read_b32 v4, a74\n"
"v_mul_f32 v4, v4, v2\n"
"v_accvgpr_write_b32 a74, v4\n"
"v_accvgpr_read_b32 v4, a75\n"
"v_mul_f32 v4, v4, v2\n"
"v_accvgpr_write_b32 a75, v4\n"
"v_accvgpr_read_b32 v4, a92\n"
"v_mul_f32 v4, v4, v2\n"
"v_accvgpr_write_b32 a92, v4\n"
"v_accvgpr_read_b32 v4, a93\n"
"v_mul_f32 v4, v4, v2\n"
"v_accvgpr_write_b32 a93, v4\n"
"v_accvgpr_read_b32 v4, a94\n"
"v_mul_f32 v4, v4, v2\n"
"v_accvgpr_write_b32 a94, v4\n"
"v_accvgpr_read_b32 v4, a95\n"
"v_mul_f32 v4, v4, v2\n"
"v_accvgpr_write_b32 a95, v4\n"
"v_accvgpr_read_b32 v4, a112\n"
"v_mul_f32 v4, v4, v2\n"
"v_accvgpr_write_b32 a112, v4\n"
"v_accvgpr_read_b32 v4, a113\n"
"v_mul_f32 v4, v4, v2\n"
"v_accvgpr_write_b32 a113, v4\n"
"v_accvgpr_read_b32 v4, a114\n"
"v_mul_f32 v4, v4, v2\n"
"v_accvgpr_write_b32 a114, v4\n"
"v_accvgpr_read_b32 v4, a115\n"
"v_mul_f32 v4, v4, v2\n"
"v_accvgpr_write_b32 a115, v4\n"
"v_accvgpr_read_b32 v4, a132\n"
"v_mul_f32 v4, v4, v2\n"
"v_accvgpr_write_b32 a132, v4\n"
"v_accvgpr_read_b32 v4, a133\n"
"v_mul_f32 v4, v4, v2\n"
"v_accvgpr_write_b32 a133, v4\n"
"v_accvgpr_read_b32 v4, a134\n"
"v_mul_f32 v4, v4, v2\n"
"v_accvgpr_write_b32 a134, v4\n"
"v_accvgpr_read_b32 v4, a135\n"
"v_mul_f32 v4, v4, v2\n"
"v_accvgpr_write_b32 a135, v4\n"
"v_accvgpr_read_b32 v4, a152\n"
"v_mul_f32 v4, v4, v2\n"
"v_accvgpr_write_b32 a152, v4\n"
"v_accvgpr_read_b32 v4, a153\n"
"v_mul_f32 v4, v4, v2\n"
"v_accvgpr_write_b32 a153, v4\n"
"v_accvgpr_read_b32 v4, a154\n"
"v_mul_f32 v4, v4, v2\n"
"v_accvgpr_write_b32 a154, v4\n"
"v_accvgpr_read_b32 v4, a155\n"
"v_mul_f32 v4, v4, v2\n"
"v_accvgpr_write_b32 a155, v4\n"
"v_lshlrev_b32 v4, 16, v128\n"
"v_and_b32 v5, 0xffff0000, v128\n"
"v_mul_f32 v4, v4, v3\n"
"v_mul_f32 v5, v5, v3\n"
"v_cvt_pk_bf16_f32 v128, v4, v5\n"
"v_lshlrev_b32 v4, 16, v129\n"
"v_and_b32 v5, 0xffff0000, v129\n"
"v_mul_f32 v4, v4, v3\n"
"v_mul_f32 v5, v5, v3\n"
"v_cvt_pk_bf16_f32 v129, v4, v5\n"
"v_lshlrev_b32 v4, 16, v130\n"
"v_and_b32 v5, 0xffff0000, v130\n"
"v_mul_f32 v4, v4, v3\n"
"v_mul_f32 v5, v5, v3\n"
"v_cvt_pk_bf16_f32 v130, v4, v5\n"
"v_lshlrev_b32 v4, 16, v131\n"
"v_and_b32 v5, 0xffff0000, v131\n"
"v_mul_f32 v4, v4, v3\n"
"v_mul_f32 v5, v5, v3\n"
"v_cvt_pk_bf16_f32 v131, v4, v5\n"
"v_max_f32 v1, v47, v42\n"
"v_sub_f32 v2, v47, v1\n"
"v_sub_f32 v3, v42, v1\n"
"v_exp_f32 v2, v2\n"
"v_exp_f32 v3, v3\n"
"v_mov_b32 v47, v1\n"
"v_accvgpr_read_b32 v4, a16\n"
"v_mul_f32 v4, v4, v2\n"
"v_accvgpr_write_b32 a16, v4\n"
"v_accvgpr_read_b32 v4, a17\n"
"v_mul_f32 v4, v4, v2\n"
"v_accvgpr_write_b32 a17, v4\n"
"v_accvgpr_read_b32 v4, a18\n"
"v_mul_f32 v4, v4, v2\n"
"v_accvgpr_write_b32 a18, v4\n"
"v_accvgpr_read_b32 v4, a19\n"
"v_mul_f32 v4, v4, v2\n"
"v_accvgpr_write_b32 a19, v4\n"
"v_accvgpr_read_b32 v4, a36\n"
"v_mul_f32 v4, v4, v2\n"
"v_accvgpr_write_b32 a36, v4\n"
"v_accvgpr_read_b32 v4, a37\n"
"v_mul_f32 v4, v4, v2\n"
"v_accvgpr_write_b32 a37, v4\n"
"v_accvgpr_read_b32 v4, a38\n"
"v_mul_f32 v4, v4, v2\n"
"v_accvgpr_write_b32 a38, v4\n"
"v_accvgpr_read_b32 v4, a39\n"
"v_mul_f32 v4, v4, v2\n"
"v_accvgpr_write_b32 a39, v4\n"
"v_accvgpr_read_b32 v4, a56\n"
"v_mul_f32 v4, v4, v2\n"
"v_accvgpr_write_b32 a56, v4\n"
"v_accvgpr_read_b32 v4, a57\n"
"v_mul_f32 v4, v4, v2\n"
"v_accvgpr_write_b32 a57, v4\n"
"v_accvgpr_read_b32 v4, a58\n"
"v_mul_f32 v4, v4, v2\n"
"v_accvgpr_write_b32 a58, v4\n"
"v_accvgpr_read_b32 v4, a59\n"
"v_mul_f32 v4, v4, v2\n"
"v_accvgpr_write_b32 a59, v4\n"
"v_accvgpr_read_b32 v4, a76\n"
"v_mul_f32 v4, v4, v2\n"
"v_accvgpr_write_b32 a76, v4\n"
"v_accvgpr_read_b32 v4, a77\n"
"v_mul_f32 v4, v4, v2\n"
"v_accvgpr_write_b32 a77, v4\n"
"v_accvgpr_read_b32 v4, a78\n"
"v_mul_f32 v4, v4, v2\n"
"v_accvgpr_write_b32 a78, v4\n"
"v_accvgpr_read_b32 v4, a79\n"
"v_mul_f32 v4, v4, v2\n"
"v_accvgpr_write_b32 a79, v4\n"
"v_accvgpr_read_b32 v4, a96\n"
"v_mul_f32 v4, v4, v2\n"
"v_accvgpr_write_b32 a96, v4\n"
"v_accvgpr_read_b32 v4, a97\n"
"v_mul_f32 v4, v4, v2\n"
"v_accvgpr_write_b32 a97, v4\n"
"v_accvgpr_read_b32 v4, a98\n"
"v_mul_f32 v4, v4, v2\n"
"v_accvgpr_write_b32 a98, v4\n"
"v_accvgpr_read_b32 v4, a99\n"
"v_mul_f32 v4, v4, v2\n"
"v_accvgpr_write_b32 a99, v4\n"
"v_accvgpr_read_b32 v4, a116\n"
"v_mul_f32 v4, v4, v2\n"
"v_accvgpr_write_b32 a116, v4\n"
"v_accvgpr_read_b32 v4, a117\n"
"v_mul_f32 v4, v4, v2\n"
"v_accvgpr_write_b32 a117, v4\n"
"v_accvgpr_read_b32 v4, a118\n"
"v_mul_f32 v4, v4, v2\n"
"v_accvgpr_write_b32 a118, v4\n"
"v_accvgpr_read_b32 v4, a119\n"
"v_mul_f32 v4, v4, v2\n"
"v_accvgpr_write_b32 a119, v4\n"
"v_accvgpr_read_b32 v4, a136\n"
"v_mul_f32 v4, v4, v2\n"
"v_accvgpr_write_b32 a136, v4\n"
"v_accvgpr_read_b32 v4, a137\n"
"v_mul_f32 v4, v4, v2\n"
"v_accvgpr_write_b32 a137, v4\n"
"v_accvgpr_read_b32 v4, a138\n"
"v_mul_f32 v4, v4, v2\n"
"v_accvgpr_write_b32 a138, v4\n"
"v_accvgpr_read_b32 v4, a139\n"
"v_mul_f32 v4, v4, v2\n"
"v_accvgpr_write_b32 a139, v4\n"
"v_accvgpr_read_b32 v4, a156\n"
"v_mul_f32 v4, v4, v2\n"
"v_accvgpr_write_b32 a156, v4\n"
"v_accvgpr_read_b32 v4, a157\n"
"v_mul_f32 v4, v4, v2\n"
"v_accvgpr_write_b32 a157, v4\n"
"v_accvgpr_read_b32 v4, a158\n"
"v_mul_f32 v4, v4, v2\n"
"v_accvgpr_write_b32 a158, v4\n"
"v_accvgpr_read_b32 v4, a159\n"
"v_mul_f32 v4, v4, v2\n"
"v_accvgpr_write_b32 a159, v4\n"
"v_lshlrev_b32 v4, 16, v132\n"
"v_and_b32 v5, 0xffff0000, v132\n"
"v_mul_f32 v4, v4, v3\n"
"v_mul_f32 v5, v5, v3\n"
"v_cvt_pk_bf16_f32 v132, v4, v5\n"
"v_lshlrev_b32 v4, 16, v133\n"
"v_and_b32 v5, 0xffff0000, v133\n"
"v_mul_f32 v4, v4, v3\n"
"v_mul_f32 v5, v5, v3\n"
"v_cvt_pk_bf16_f32 v133, v4, v5\n"
"v_lshlrev_b32 v4, 16, v134\n"
"v_and_b32 v5, 0xffff0000, v134\n"
"v_mul_f32 v4, v4, v3\n"
"v_mul_f32 v5, v5, v3\n"
"v_cvt_pk_bf16_f32 v134, v4, v5\n"
"v_lshlrev_b32 v4, 16, v135\n"
"v_and_b32 v5, 0xffff0000, v135\n"
"v_mul_f32 v4, v4, v3\n"
"v_mul_f32 v5, v5, v3\n"
"v_cvt_pk_bf16_f32 v135, v4, v5\n"
"ds_write_b32 v8, v43 offset:0\n"
"ds_write_b32 v8, v44 offset:256\n"
"ds_write_b32 v8, v45 offset:512\n"
"ds_write_b32 v8, v46 offset:768\n"
"ds_write_b32 v8, v47 offset:1024\n"
"ds_write_b128 v12, v[116:119] offset:0\n"
"ds_write_b128 v12, v[120:123] offset:1024\n"
"ds_write_b128 v12, v[124:127] offset:2048\n"
"ds_write_b128 v12, v[128:131] offset:3072\n"
"ds_write_b128 v12, v[132:135] offset:4096\n"
"v_mov_b32 v1, s57\n"
"ds_write_b32 v8, v1 offset:1280\n"
"s_waitcnt lgkmcnt(0)\n"
"s_sub_u32 s56, s56, 1\n"
"s_nop 3\n"
"s_setpc_b64 s[74:75]\n"
"S3_%=:\n"
"s_nop 15\n"
"ds_read_b32 v48, v7 offset:0\n"
"ds_read_b32 v49, v7 offset:256\n"
"ds_read_b32 v50, v7 offset:512\n"
"ds_read_b32 v51, v7 offset:768\n"
"ds_read_b32 v52, v7 offset:1024\n"
"s_waitcnt lgkmcnt(0)\n"
"v_max_f32 v1, v43, v48\n"
"v_sub_f32 v2, v43, v1\n"
"v_sub_f32 v3, v48, v1\n"
"v_exp_f32 v2, v2\n"
"v_exp_f32 v3, v3\n"
"v_mov_b32 v43, v1\n"
"v_accvgpr_read_b32 v4, a0\n"
"v_mul_f32 v4, v4, v2\n"
"v_accvgpr_write_b32 a0, v4\n"
"v_accvgpr_read_b32 v4, a1\n"
"v_mul_f32 v4, v4, v2\n"
"v_accvgpr_write_b32 a1, v4\n"
"v_accvgpr_read_b32 v4, a2\n"
"v_mul_f32 v4, v4, v2\n"
"v_accvgpr_write_b32 a2, v4\n"
"v_accvgpr_read_b32 v4, a3\n"
"v_mul_f32 v4, v4, v2\n"
"v_accvgpr_write_b32 a3, v4\n"
"v_accvgpr_read_b32 v4, a20\n"
"v_mul_f32 v4, v4, v2\n"
"v_accvgpr_write_b32 a20, v4\n"
"v_accvgpr_read_b32 v4, a21\n"
"v_mul_f32 v4, v4, v2\n"
"v_accvgpr_write_b32 a21, v4\n"
"v_accvgpr_read_b32 v4, a22\n"
"v_mul_f32 v4, v4, v2\n"
"v_accvgpr_write_b32 a22, v4\n"
"v_accvgpr_read_b32 v4, a23\n"
"v_mul_f32 v4, v4, v2\n"
"v_accvgpr_write_b32 a23, v4\n"
"v_accvgpr_read_b32 v4, a40\n"
"v_mul_f32 v4, v4, v2\n"
"v_accvgpr_write_b32 a40, v4\n"
"v_accvgpr_read_b32 v4, a41\n"
"v_mul_f32 v4, v4, v2\n"
"v_accvgpr_write_b32 a41, v4\n"
"v_accvgpr_read_b32 v4, a42\n"
"v_mul_f32 v4, v4, v2\n"
"v_accvgpr_write_b32 a42, v4\n"
"v_accvgpr_read_b32 v4, a43\n"
"v_mul_f32 v4, v4, v2\n"
"v_accvgpr_write_b32 a43, v4\n"
"v_accvgpr_read_b32 v4, a60\n"
"v_mul_f32 v4, v4, v2\n"
"v_accvgpr_write_b32 a60, v4\n"
"v_accvgpr_read_b32 v4, a61\n"
"v_mul_f32 v4, v4, v2\n"
"v_accvgpr_write_b32 a61, v4\n"
"v_accvgpr_read_b32 v4, a62\n"
"v_mul_f32 v4, v4, v2\n"
"v_accvgpr_write_b32 a62, v4\n"
"v_accvgpr_read_b32 v4, a63\n"
"v_mul_f32 v4, v4, v2\n"
"v_accvgpr_write_b32 a63, v4\n"
"v_accvgpr_read_b32 v4, a80\n"
"v_mul_f32 v4, v4, v2\n"
"v_accvgpr_write_b32 a80, v4\n"
"v_accvgpr_read_b32 v4, a81\n"
"v_mul_f32 v4, v4, v2\n"
"v_accvgpr_write_b32 a81, v4\n"
"v_accvgpr_read_b32 v4, a82\n"
"v_mul_f32 v4, v4, v2\n"
"v_accvgpr_write_b32 a82, v4\n"
"v_accvgpr_read_b32 v4, a83\n"
"v_mul_f32 v4, v4, v2\n"
"v_accvgpr_write_b32 a83, v4\n"
"v_accvgpr_read_b32 v4, a100\n"
"v_mul_f32 v4, v4, v2\n"
"v_accvgpr_write_b32 a100, v4\n"
"v_accvgpr_read_b32 v4, a101\n"
"v_mul_f32 v4, v4, v2\n"
"v_accvgpr_write_b32 a101, v4\n"
"v_accvgpr_read_b32 v4, a102\n"
"v_mul_f32 v4, v4, v2\n"
"v_accvgpr_write_b32 a102, v4\n"
"v_accvgpr_read_b32 v4, a103\n"
"v_mul_f32 v4, v4, v2\n"
"v_accvgpr_write_b32 a103, v4\n"
"v_accvgpr_read_b32 v4, a120\n"
"v_mul_f32 v4, v4, v2\n"
"v_accvgpr_write_b32 a120, v4\n"
"v_accvgpr_read_b32 v4, a121\n"
"v_mul_f32 v4, v4, v2\n"
"v_accvgpr_write_b32 a121, v4\n"
"v_accvgpr_read_b32 v4, a122\n"
"v_mul_f32 v4, v4, v2\n"
"v_accvgpr_write_b32 a122, v4\n"
"v_accvgpr_read_b32 v4, a123\n"
"v_mul_f32 v4, v4, v2\n"
"v_accvgpr_write_b32 a123, v4\n"
"v_accvgpr_read_b32 v4, a140\n"
"v_mul_f32 v4, v4, v2\n"
"v_accvgpr_write_b32 a140, v4\n"
"v_accvgpr_read_b32 v4, a141\n"
"v_mul_f32 v4, v4, v2\n"
"v_accvgpr_write_b32 a141, v4\n"
"v_accvgpr_read_b32 v4, a142\n"
"v_mul_f32 v4, v4, v2\n"
"v_accvgpr_write_b32 a142, v4\n"
"v_accvgpr_read_b32 v4, a143\n"
"v_mul_f32 v4, v4, v2\n"
"v_accvgpr_write_b32 a143, v4\n"
"v_lshlrev_b32 v4, 16, v96\n"
"v_and_b32 v5, 0xffff0000, v96\n"
"v_mul_f32 v4, v4, v3\n"
"v_mul_f32 v5, v5, v3\n"
"v_cvt_pk_bf16_f32 v96, v4, v5\n"
"v_lshlrev_b32 v4, 16, v97\n"
"v_and_b32 v5, 0xffff0000, v97\n"
"v_mul_f32 v4, v4, v3\n"
"v_mul_f32 v5, v5, v3\n"
"v_cvt_pk_bf16_f32 v97, v4, v5\n"
"v_lshlrev_b32 v4, 16, v98\n"
"v_and_b32 v5, 0xffff0000, v98\n"
"v_mul_f32 v4, v4, v3\n"
"v_mul_f32 v5, v5, v3\n"
"v_cvt_pk_bf16_f32 v98, v4, v5\n"
"v_lshlrev_b32 v4, 16, v99\n"
"v_and_b32 v5, 0xffff0000, v99\n"
"v_mul_f32 v4, v4, v3\n"
"v_mul_f32 v5, v5, v3\n"
"v_cvt_pk_bf16_f32 v99, v4, v5\n"
"v_max_f32 v1, v44, v49\n"
"v_sub_f32 v2, v44, v1\n"
"v_sub_f32 v3, v49, v1\n"
"v_exp_f32 v2, v2\n"
"v_exp_f32 v3, v3\n"
"v_mov_b32 v44, v1\n"
"v_accvgpr_read_b32 v4, a4\n"
"v_mul_f32 v4, v4, v2\n"
"v_accvgpr_write_b32 a4, v4\n"
"v_accvgpr_read_b32 v4, a5\n"
"v_mul_f32 v4, v4, v2\n"
"v_accvgpr_write_b32 a5, v4\n"
"v_accvgpr_read_b32 v4, a6\n"
"v_mul_f32 v4, v4, v2\n"
"v_accvgpr_write_b32 a6, v4\n"
"v_accvgpr_read_b32 v4, a7\n"
"v_mul_f32 v4, v4, v2\n"
"v_accvgpr_write_b32 a7, v4\n"
"v_accvgpr_read_b32 v4, a24\n"
"v_mul_f32 v4, v4, v2\n"
"v_accvgpr_write_b32 a24, v4\n"
"v_accvgpr_read_b32 v4, a25\n"
"v_mul_f32 v4, v4, v2\n"
"v_accvgpr_write_b32 a25, v4\n"
"v_accvgpr_read_b32 v4, a26\n"
"v_mul_f32 v4, v4, v2\n"
"v_accvgpr_write_b32 a26, v4\n"
"v_accvgpr_read_b32 v4, a27\n"
"v_mul_f32 v4, v4, v2\n"
"v_accvgpr_write_b32 a27, v4\n"
"v_accvgpr_read_b32 v4, a44\n"
"v_mul_f32 v4, v4, v2\n"
"v_accvgpr_write_b32 a44, v4\n"
"v_accvgpr_read_b32 v4, a45\n"
"v_mul_f32 v4, v4, v2\n"
"v_accvgpr_write_b32 a45, v4\n"
"v_accvgpr_read_b32 v4, a46\n"
"v_mul_f32 v4, v4, v2\n"
"v_accvgpr_write_b32 a46, v4\n"
"v_accvgpr_read_b32 v4, a47\n"
"v_mul_f32 v4, v4, v2\n"
"v_accvgpr_write_b32 a47, v4\n"
"v_accvgpr_read_b32 v4, a64\n"
"v_mul_f32 v4, v4, v2\n"
"v_accvgpr_write_b32 a64, v4\n"
"v_accvgpr_read_b32 v4, a65\n"
"v_mul_f32 v4, v4, v2\n"
"v_accvgpr_write_b32 a65, v4\n"
"v_accvgpr_read_b32 v4, a66\n"
"v_mul_f32 v4, v4, v2\n"
"v_accvgpr_write_b32 a66, v4\n"
"v_accvgpr_read_b32 v4, a67\n"
"v_mul_f32 v4, v4, v2\n"
"v_accvgpr_write_b32 a67, v4\n"
"v_accvgpr_read_b32 v4, a84\n"
"v_mul_f32 v4, v4, v2\n"
"v_accvgpr_write_b32 a84, v4\n"
"v_accvgpr_read_b32 v4, a85\n"
"v_mul_f32 v4, v4, v2\n"
"v_accvgpr_write_b32 a85, v4\n"
"v_accvgpr_read_b32 v4, a86\n"
"v_mul_f32 v4, v4, v2\n"
"v_accvgpr_write_b32 a86, v4\n"
"v_accvgpr_read_b32 v4, a87\n"
"v_mul_f32 v4, v4, v2\n"
"v_accvgpr_write_b32 a87, v4\n"
"v_accvgpr_read_b32 v4, a104\n"
"v_mul_f32 v4, v4, v2\n"
"v_accvgpr_write_b32 a104, v4\n"
"v_accvgpr_read_b32 v4, a105\n"
"v_mul_f32 v4, v4, v2\n"
"v_accvgpr_write_b32 a105, v4\n"
"v_accvgpr_read_b32 v4, a106\n"
"v_mul_f32 v4, v4, v2\n"
"v_accvgpr_write_b32 a106, v4\n"
"v_accvgpr_read_b32 v4, a107\n"
"v_mul_f32 v4, v4, v2\n"
"v_accvgpr_write_b32 a107, v4\n"
"v_accvgpr_read_b32 v4, a124\n"
"v_mul_f32 v4, v4, v2\n"
"v_accvgpr_write_b32 a124, v4\n"
"v_accvgpr_read_b32 v4, a125\n"
"v_mul_f32 v4, v4, v2\n"
"v_accvgpr_write_b32 a125, v4\n"
"v_accvgpr_read_b32 v4, a126\n"
"v_mul_f32 v4, v4, v2\n"
"v_accvgpr_write_b32 a126, v4\n"
"v_accvgpr_read_b32 v4, a127\n"
"v_mul_f32 v4, v4, v2\n"
"v_accvgpr_write_b32 a127, v4\n"
"v_accvgpr_read_b32 v4, a144\n"
"v_mul_f32 v4, v4, v2\n"
"v_accvgpr_write_b32 a144, v4\n"
"v_accvgpr_read_b32 v4, a145\n"
"v_mul_f32 v4, v4, v2\n"
"v_accvgpr_write_b32 a145, v4\n"
"v_accvgpr_read_b32 v4, a146\n"
"v_mul_f32 v4, v4, v2\n"
"v_accvgpr_write_b32 a146, v4\n"
"v_accvgpr_read_b32 v4, a147\n"
"v_mul_f32 v4, v4, v2\n"
"v_accvgpr_write_b32 a147, v4\n"
"v_lshlrev_b32 v4, 16, v100\n"
"v_and_b32 v5, 0xffff0000, v100\n"
"v_mul_f32 v4, v4, v3\n"
"v_mul_f32 v5, v5, v3\n"
"v_cvt_pk_bf16_f32 v100, v4, v5\n"
"v_lshlrev_b32 v4, 16, v101\n"
"v_and_b32 v5, 0xffff0000, v101\n"
"v_mul_f32 v4, v4, v3\n"
"v_mul_f32 v5, v5, v3\n"
"v_cvt_pk_bf16_f32 v101, v4, v5\n"
"v_lshlrev_b32 v4, 16, v102\n"
"v_and_b32 v5, 0xffff0000, v102\n"
"v_mul_f32 v4, v4, v3\n"
"v_mul_f32 v5, v5, v3\n"
"v_cvt_pk_bf16_f32 v102, v4, v5\n"
"v_lshlrev_b32 v4, 16, v103\n"
"v_and_b32 v5, 0xffff0000, v103\n"
"v_mul_f32 v4, v4, v3\n"
"v_mul_f32 v5, v5, v3\n"
"v_cvt_pk_bf16_f32 v103, v4, v5\n"
"v_max_f32 v1, v45, v50\n"
"v_sub_f32 v2, v45, v1\n"
"v_sub_f32 v3, v50, v1\n"
"v_exp_f32 v2, v2\n"
"v_exp_f32 v3, v3\n"
"v_mov_b32 v45, v1\n"
"v_accvgpr_read_b32 v4, a8\n"
"v_mul_f32 v4, v4, v2\n"
"v_accvgpr_write_b32 a8, v4\n"
"v_accvgpr_read_b32 v4, a9\n"
"v_mul_f32 v4, v4, v2\n"
"v_accvgpr_write_b32 a9, v4\n"
"v_accvgpr_read_b32 v4, a10\n"
"v_mul_f32 v4, v4, v2\n"
"v_accvgpr_write_b32 a10, v4\n"
"v_accvgpr_read_b32 v4, a11\n"
"v_mul_f32 v4, v4, v2\n"
"v_accvgpr_write_b32 a11, v4\n"
"v_accvgpr_read_b32 v4, a28\n"
"v_mul_f32 v4, v4, v2\n"
"v_accvgpr_write_b32 a28, v4\n"
"v_accvgpr_read_b32 v4, a29\n"
"v_mul_f32 v4, v4, v2\n"
"v_accvgpr_write_b32 a29, v4\n"
"v_accvgpr_read_b32 v4, a30\n"
"v_mul_f32 v4, v4, v2\n"
"v_accvgpr_write_b32 a30, v4\n"
"v_accvgpr_read_b32 v4, a31\n"
"v_mul_f32 v4, v4, v2\n"
"v_accvgpr_write_b32 a31, v4\n"
"v_accvgpr_read_b32 v4, a48\n"
"v_mul_f32 v4, v4, v2\n"
"v_accvgpr_write_b32 a48, v4\n"
"v_accvgpr_read_b32 v4, a49\n"
"v_mul_f32 v4, v4, v2\n"
"v_accvgpr_write_b32 a49, v4\n"
"v_accvgpr_read_b32 v4, a50\n"
"v_mul_f32 v4, v4, v2\n"
"v_accvgpr_write_b32 a50, v4\n"
"v_accvgpr_read_b32 v4, a51\n"
"v_mul_f32 v4, v4, v2\n"
"v_accvgpr_write_b32 a51, v4\n"
"v_accvgpr_read_b32 v4, a68\n"
"v_mul_f32 v4, v4, v2\n"
"v_accvgpr_write_b32 a68, v4\n"
"v_accvgpr_read_b32 v4, a69\n"
"v_mul_f32 v4, v4, v2\n"
"v_accvgpr_write_b32 a69, v4\n"
"v_accvgpr_read_b32 v4, a70\n"
"v_mul_f32 v4, v4, v2\n"
"v_accvgpr_write_b32 a70, v4\n"
"v_accvgpr_read_b32 v4, a71\n"
"v_mul_f32 v4, v4, v2\n"
"v_accvgpr_write_b32 a71, v4\n"
"v_accvgpr_read_b32 v4, a88\n"
"v_mul_f32 v4, v4, v2\n"
"v_accvgpr_write_b32 a88, v4\n"
"v_accvgpr_read_b32 v4, a89\n"
"v_mul_f32 v4, v4, v2\n"
"v_accvgpr_write_b32 a89, v4\n"
"v_accvgpr_read_b32 v4, a90\n"
"v_mul_f32 v4, v4, v2\n"
"v_accvgpr_write_b32 a90, v4\n"
"v_accvgpr_read_b32 v4, a91\n"
"v_mul_f32 v4, v4, v2\n"
"v_accvgpr_write_b32 a91, v4\n"
"v_accvgpr_read_b32 v4, a108\n"
"v_mul_f32 v4, v4, v2\n"
"v_accvgpr_write_b32 a108, v4\n"
"v_accvgpr_read_b32 v4, a109\n"
"v_mul_f32 v4, v4, v2\n"
"v_accvgpr_write_b32 a109, v4\n"
"v_accvgpr_read_b32 v4, a110\n"
"v_mul_f32 v4, v4, v2\n"
"v_accvgpr_write_b32 a110, v4\n"
"v_accvgpr_read_b32 v4, a111\n"
"v_mul_f32 v4, v4, v2\n"
"v_accvgpr_write_b32 a111, v4\n"
"v_accvgpr_read_b32 v4, a128\n"
"v_mul_f32 v4, v4, v2\n"
"v_accvgpr_write_b32 a128, v4\n"
"v_accvgpr_read_b32 v4, a129\n"
"v_mul_f32 v4, v4, v2\n"
"v_accvgpr_write_b32 a129, v4\n"
"v_accvgpr_read_b32 v4, a130\n"
"v_mul_f32 v4, v4, v2\n"
"v_accvgpr_write_b32 a130, v4\n"
"v_accvgpr_read_b32 v4, a131\n"
"v_mul_f32 v4, v4, v2\n"
"v_accvgpr_write_b32 a131, v4\n"
"v_accvgpr_read_b32 v4, a148\n"
"v_mul_f32 v4, v4, v2\n"
"v_accvgpr_write_b32 a148, v4\n"
"v_accvgpr_read_b32 v4, a149\n"
"v_mul_f32 v4, v4, v2\n"
"v_accvgpr_write_b32 a149, v4\n"
"v_accvgpr_read_b32 v4, a150\n"
"v_mul_f32 v4, v4, v2\n"
"v_accvgpr_write_b32 a150, v4\n"
"v_accvgpr_read_b32 v4, a151\n"
"v_mul_f32 v4, v4, v2\n"
"v_accvgpr_write_b32 a151, v4\n"
"v_lshlrev_b32 v4, 16, v104\n"
"v_and_b32 v5, 0xffff0000, v104\n"
"v_mul_f32 v4, v4, v3\n"
"v_mul_f32 v5, v5, v3\n"
"v_cvt_pk_bf16_f32 v104, v4, v5\n"
"v_lshlrev_b32 v4, 16, v105\n"
"v_and_b32 v5, 0xffff0000, v105\n"
"v_mul_f32 v4, v4, v3\n"
"v_mul_f32 v5, v5, v3\n"
"v_cvt_pk_bf16_f32 v105, v4, v5\n"
"v_lshlrev_b32 v4, 16, v106\n"
"v_and_b32 v5, 0xffff0000, v106\n"
"v_mul_f32 v4, v4, v3\n"
"v_mul_f32 v5, v5, v3\n"
"v_cvt_pk_bf16_f32 v106, v4, v5\n"
"v_lshlrev_b32 v4, 16, v107\n"
"v_and_b32 v5, 0xffff0000, v107\n"
"v_mul_f32 v4, v4, v3\n"
"v_mul_f32 v5, v5, v3\n"
"v_cvt_pk_bf16_f32 v107, v4, v5\n"
"v_max_f32 v1, v46, v51\n"
"v_sub_f32 v2, v46, v1\n"
"v_sub_f32 v3, v51, v1\n"
"v_exp_f32 v2, v2\n"
"v_exp_f32 v3, v3\n"
"v_mov_b32 v46, v1\n"
"v_accvgpr_read_b32 v4, a12\n"
"v_mul_f32 v4, v4, v2\n"
"v_accvgpr_write_b32 a12, v4\n"
"v_accvgpr_read_b32 v4, a13\n"
"v_mul_f32 v4, v4, v2\n"
"v_accvgpr_write_b32 a13, v4\n"
"v_accvgpr_read_b32 v4, a14\n"
"v_mul_f32 v4, v4, v2\n"
"v_accvgpr_write_b32 a14, v4\n"
"v_accvgpr_read_b32 v4, a15\n"
"v_mul_f32 v4, v4, v2\n"
"v_accvgpr_write_b32 a15, v4\n"
"v_accvgpr_read_b32 v4, a32\n"
"v_mul_f32 v4, v4, v2\n"
"v_accvgpr_write_b32 a32, v4\n"
"v_accvgpr_read_b32 v4, a33\n"
"v_mul_f32 v4, v4, v2\n"
"v_accvgpr_write_b32 a33, v4\n"
"v_accvgpr_read_b32 v4, a34\n"
"v_mul_f32 v4, v4, v2\n"
"v_accvgpr_write_b32 a34, v4\n"
"v_accvgpr_read_b32 v4, a35\n"
"v_mul_f32 v4, v4, v2\n"
"v_accvgpr_write_b32 a35, v4\n"
"v_accvgpr_read_b32 v4, a52\n"
"v_mul_f32 v4, v4, v2\n"
"v_accvgpr_write_b32 a52, v4\n"
"v_accvgpr_read_b32 v4, a53\n"
"v_mul_f32 v4, v4, v2\n"
"v_accvgpr_write_b32 a53, v4\n"
"v_accvgpr_read_b32 v4, a54\n"
"v_mul_f32 v4, v4, v2\n"
"v_accvgpr_write_b32 a54, v4\n"
"v_accvgpr_read_b32 v4, a55\n"
"v_mul_f32 v4, v4, v2\n"
"v_accvgpr_write_b32 a55, v4\n"
"v_accvgpr_read_b32 v4, a72\n"
"v_mul_f32 v4, v4, v2\n"
"v_accvgpr_write_b32 a72, v4\n"
"v_accvgpr_read_b32 v4, a73\n"
"v_mul_f32 v4, v4, v2\n"
"v_accvgpr_write_b32 a73, v4\n"
"v_accvgpr_read_b32 v4, a74\n"
"v_mul_f32 v4, v4, v2\n"
"v_accvgpr_write_b32 a74, v4\n"
"v_accvgpr_read_b32 v4, a75\n"
"v_mul_f32 v4, v4, v2\n"
"v_accvgpr_write_b32 a75, v4\n"
"v_accvgpr_read_b32 v4, a92\n"
"v_mul_f32 v4, v4, v2\n"
"v_accvgpr_write_b32 a92, v4\n"
"v_accvgpr_read_b32 v4, a93\n"
"v_mul_f32 v4, v4, v2\n"
"v_accvgpr_write_b32 a93, v4\n"
"v_accvgpr_read_b32 v4, a94\n"
"v_mul_f32 v4, v4, v2\n"
"v_accvgpr_write_b32 a94, v4\n"
"v_accvgpr_read_b32 v4, a95\n"
"v_mul_f32 v4, v4, v2\n"
"v_accvgpr_write_b32 a95, v4\n"
"v_accvgpr_read_b32 v4, a112\n"
"v_mul_f32 v4, v4, v2\n"
"v_accvgpr_write_b32 a112, v4\n"
"v_accvgpr_read_b32 v4, a113\n"
"v_mul_f32 v4, v4, v2\n"
"v_accvgpr_write_b32 a113, v4\n"
"v_accvgpr_read_b32 v4, a114\n"
"v_mul_f32 v4, v4, v2\n"
"v_accvgpr_write_b32 a114, v4\n"
"v_accvgpr_read_b32 v4, a115\n"
"v_mul_f32 v4, v4, v2\n"
"v_accvgpr_write_b32 a115, v4\n"
"v_accvgpr_read_b32 v4, a132\n"
"v_mul_f32 v4, v4, v2\n"
"v_accvgpr_write_b32 a132, v4\n"
"v_accvgpr_read_b32 v4, a133\n"
"v_mul_f32 v4, v4, v2\n"
"v_accvgpr_write_b32 a133, v4\n"
"v_accvgpr_read_b32 v4, a134\n"
"v_mul_f32 v4, v4, v2\n"
"v_accvgpr_write_b32 a134, v4\n"
"v_accvgpr_read_b32 v4, a135\n"
"v_mul_f32 v4, v4, v2\n"
"v_accvgpr_write_b32 a135, v4\n"
"v_accvgpr_read_b32 v4, a152\n"
"v_mul_f32 v4, v4, v2\n"
"v_accvgpr_write_b32 a152, v4\n"
"v_accvgpr_read_b32 v4, a153\n"
"v_mul_f32 v4, v4, v2\n"
"v_accvgpr_write_b32 a153, v4\n"
"v_accvgpr_read_b32 v4, a154\n"
"v_mul_f32 v4, v4, v2\n"
"v_accvgpr_write_b32 a154, v4\n"
"v_accvgpr_read_b32 v4, a155\n"
"v_mul_f32 v4, v4, v2\n"
"v_accvgpr_write_b32 a155, v4\n"
"v_lshlrev_b32 v4, 16, v108\n"
"v_and_b32 v5, 0xffff0000, v108\n"
"v_mul_f32 v4, v4, v3\n"
"v_mul_f32 v5, v5, v3\n"
"v_cvt_pk_bf16_f32 v108, v4, v5\n"
"v_lshlrev_b32 v4, 16, v109\n"
"v_and_b32 v5, 0xffff0000, v109\n"
"v_mul_f32 v4, v4, v3\n"
"v_mul_f32 v5, v5, v3\n"
"v_cvt_pk_bf16_f32 v109, v4, v5\n"
"v_lshlrev_b32 v4, 16, v110\n"
"v_and_b32 v5, 0xffff0000, v110\n"
"v_mul_f32 v4, v4, v3\n"
"v_mul_f32 v5, v5, v3\n"
"v_cvt_pk_bf16_f32 v110, v4, v5\n"
"v_lshlrev_b32 v4, 16, v111\n"
"v_and_b32 v5, 0xffff0000, v111\n"
"v_mul_f32 v4, v4, v3\n"
"v_mul_f32 v5, v5, v3\n"
"v_cvt_pk_bf16_f32 v111, v4, v5\n"
"v_max_f32 v1, v47, v52\n"
"v_sub_f32 v2, v47, v1\n"
"v_sub_f32 v3, v52, v1\n"
"v_exp_f32 v2, v2\n"
"v_exp_f32 v3, v3\n"
"v_mov_b32 v47, v1\n"
"v_accvgpr_read_b32 v4, a16\n"
"v_mul_f32 v4, v4, v2\n"
"v_accvgpr_write_b32 a16, v4\n"
"v_accvgpr_read_b32 v4, a17\n"
"v_mul_f32 v4, v4, v2\n"
"v_accvgpr_write_b32 a17, v4\n"
"v_accvgpr_read_b32 v4, a18\n"
"v_mul_f32 v4, v4, v2\n"
"v_accvgpr_write_b32 a18, v4\n"
"v_accvgpr_read_b32 v4, a19\n"
"v_mul_f32 v4, v4, v2\n"
"v_accvgpr_write_b32 a19, v4\n"
"v_accvgpr_read_b32 v4, a36\n"
"v_mul_f32 v4, v4, v2\n"
"v_accvgpr_write_b32 a36, v4\n"
"v_accvgpr_read_b32 v4, a37\n"
"v_mul_f32 v4, v4, v2\n"
"v_accvgpr_write_b32 a37, v4\n"
"v_accvgpr_read_b32 v4, a38\n"
"v_mul_f32 v4, v4, v2\n"
"v_accvgpr_write_b32 a38, v4\n"
"v_accvgpr_read_b32 v4, a39\n"
"v_mul_f32 v4, v4, v2\n"
"v_accvgpr_write_b32 a39, v4\n"
"v_accvgpr_read_b32 v4, a56\n"
"v_mul_f32 v4, v4, v2\n"
"v_accvgpr_write_b32 a56, v4\n"
"v_accvgpr_read_b32 v4, a57\n"
"v_mul_f32 v4, v4, v2\n"
"v_accvgpr_write_b32 a57, v4\n"
"v_accvgpr_read_b32 v4, a58\n"
"v_mul_f32 v4, v4, v2\n"
"v_accvgpr_write_b32 a58, v4\n"
"v_accvgpr_read_b32 v4, a59\n"
"v_mul_f32 v4, v4, v2\n"
"v_accvgpr_write_b32 a59, v4\n"
"v_accvgpr_read_b32 v4, a76\n"
"v_mul_f32 v4, v4, v2\n"
"v_accvgpr_write_b32 a76, v4\n"
"v_accvgpr_read_b32 v4, a77\n"
"v_mul_f32 v4, v4, v2\n"
"v_accvgpr_write_b32 a77, v4\n"
"v_accvgpr_read_b32 v4, a78\n"
"v_mul_f32 v4, v4, v2\n"
"v_accvgpr_write_b32 a78, v4\n"
"v_accvgpr_read_b32 v4, a79\n"
"v_mul_f32 v4, v4, v2\n"
"v_accvgpr_write_b32 a79, v4\n"
"v_accvgpr_read_b32 v4, a96\n"
"v_mul_f32 v4, v4, v2\n"
"v_accvgpr_write_b32 a96, v4\n"
"v_accvgpr_read_b32 v4, a97\n"
"v_mul_f32 v4, v4, v2\n"
"v_accvgpr_write_b32 a97, v4\n"
"v_accvgpr_read_b32 v4, a98\n"
"v_mul_f32 v4, v4, v2\n"
"v_accvgpr_write_b32 a98, v4\n"
"v_accvgpr_read_b32 v4, a99\n"
"v_mul_f32 v4, v4, v2\n"
"v_accvgpr_write_b32 a99, v4\n"
"v_accvgpr_read_b32 v4, a116\n"
"v_mul_f32 v4, v4, v2\n"
"v_accvgpr_write_b32 a116, v4\n"
"v_accvgpr_read_b32 v4, a117\n"
"v_mul_f32 v4, v4, v2\n"
"v_accvgpr_write_b32 a117, v4\n"
"v_accvgpr_read_b32 v4, a118\n"
"v_mul_f32 v4, v4, v2\n"
"v_accvgpr_write_b32 a118, v4\n"
"v_accvgpr_read_b32 v4, a119\n"
"v_mul_f32 v4, v4, v2\n"
"v_accvgpr_write_b32 a119, v4\n"
"v_accvgpr_read_b32 v4, a136\n"
"v_mul_f32 v4, v4, v2\n"
"v_accvgpr_write_b32 a136, v4\n"
"v_accvgpr_read_b32 v4, a137\n"
"v_mul_f32 v4, v4, v2\n"
"v_accvgpr_write_b32 a137, v4\n"
"v_accvgpr_read_b32 v4, a138\n"
"v_mul_f32 v4, v4, v2\n"
"v_accvgpr_write_b32 a138, v4\n"
"v_accvgpr_read_b32 v4, a139\n"
"v_mul_f32 v4, v4, v2\n"
"v_accvgpr_write_b32 a139, v4\n"
"v_accvgpr_read_b32 v4, a156\n"
"v_mul_f32 v4, v4, v2\n"
"v_accvgpr_write_b32 a156, v4\n"
"v_accvgpr_read_b32 v4, a157\n"
"v_mul_f32 v4, v4, v2\n"
"v_accvgpr_write_b32 a157, v4\n"
"v_accvgpr_read_b32 v4, a158\n"
"v_mul_f32 v4, v4, v2\n"
"v_accvgpr_write_b32 a158, v4\n"
"v_accvgpr_read_b32 v4, a159\n"
"v_mul_f32 v4, v4, v2\n"
"v_accvgpr_write_b32 a159, v4\n"
"v_lshlrev_b32 v4, 16, v112\n"
"v_and_b32 v5, 0xffff0000, v112\n"
"v_mul_f32 v4, v4, v3\n"
"v_mul_f32 v5, v5, v3\n"
"v_cvt_pk_bf16_f32 v112, v4, v5\n"
"v_lshlrev_b32 v4, 16, v113\n"
"v_and_b32 v5, 0xffff0000, v113\n"
"v_mul_f32 v4, v4, v3\n"
"v_mul_f32 v5, v5, v3\n"
"v_cvt_pk_bf16_f32 v113, v4, v5\n"
"v_lshlrev_b32 v4, 16, v114\n"
"v_and_b32 v5, 0xffff0000, v114\n"
"v_mul_f32 v4, v4, v3\n"
"v_mul_f32 v5, v5, v3\n"
"v_cvt_pk_bf16_f32 v114, v4, v5\n"
"v_lshlrev_b32 v4, 16, v115\n"
"v_and_b32 v5, 0xffff0000, v115\n"
"v_mul_f32 v4, v4, v3\n"
"v_mul_f32 v5, v5, v3\n"
"v_cvt_pk_bf16_f32 v115, v4, v5\n"
"s_sub_u32 s56, s56, 1\n"
"s_nop 3\n"
"s_setpc_b64 s[74:75]\n"
"END_%=:\n"
  :: "s"(__builtin_amdgcn_kernarg_segment_ptr()), "s"(blockIdx.x), "s"(w)
  : "v0","v1","v2","v3","v4","v5","v6","v7","v8","v9","v10","v11","v12","v13","v14","v15","v16","v17","v18","v19","v20","v21","v22","v23","v24","v25","v26","v27","v28","v29","v30","v31","v32","v33","v34","v35","v36","v37","v38","v39","v40","v41","v42","v43","v44","v45","v46","v47","v48","v49","v50","v51","v52","v53","v54","v55","v56","v57","v58","v59","v60","v61","v62","v63","v64","v65","v66","v67","v68","v69","v70","v71","v72","v73","v74","v75","v76","v77","v78","v79","v80","v81","v82","v83","v84","v85","v86","v87","v88","v89","v90","v91","v92","v93","v94","v95","v96","v97","v98","v99","v100","v101","v102","v103","v104","v105","v106","v107","v108","v109","v110","v111","v112","v113","v114","v115","v116","v117","v118","v119","v120","v121","v122","v123","v124","v125","v126","v127","v128","v129","v130","v131","v132","v133","v134","v135","v136","v137","v138","v139","v140","v141","v142","v143","v144","v145","v146","v147","v148","v149","v150","v151","v152","v153","v154","v155","v156","v157","v158","v159","v160","v161","v162","v163","v164","v165","v166","v167","v168","v169","v170","v171","v172","v173","v174","v175","v176","v177","v178","v179","v180","v181","v182","v183","v184","v185","v186","v187","v188","v189","v190","v191","v192","v193","v194","v195","v196","v197","v198","v199","v200","v201","v202","v203","v204","v205","v206","v207","v208","v209","v210","v211","v212","v213","v214","v215","v216","v217","v218","v219","v220","v221","v222","v223","v224","v225","v226","v227","v228","v229","v230","v231","v232","v233","v234","v235","v236","v237","v238","v239","v240","v241","v242","v243","v244","v245","v246","v247","v248","v249","v250","v251","v252","v253","v254","v255","a0","a1","a2","a3","a4","a5","a6","a7","a8","a9","a10","a11","a12","a13","a14","a15","a16","a17","a18","a19","a20","a21","a22","a23","a24","a25","a26","a27","a28","a29","a30","a31","a32","a33","a34","a35","a36","a37","a38","a39","a40","a41","a42","a43","a44","a45","a46","a47","a48","a49","a50","a51","a52","a53","a54","a55","a56","a57","a58","a59","a60","a61","a62","a63","a64","a65","a66","a67","a68","a69","a70","a71","a72","a73","a74","a75","a76","a77","a78","a79","a80","a81","a82","a83","a84","a85","a86","a87","a88","a89","a90","a91","a92","a93","a94","a95","a96","a97","a98","a99","a100","a101","a102","a103","a104","a105","a106","a107","a108","a109","a110","a111","a112","a113","a114","a115","a116","a117","a118","a119","a120","a121","a122","a123","a124","a125","a126","a127","a128","a129","a130","a131","a132","a133","a134","a135","a136","a137","a138","a139","a140","a141","a142","a143","a144","a145","a146","a147","a148","a149","a150","a151","a152","a153","a154","a155","a156","a157","a158","a159","a160","a161","a162","a163","a164","a165","a166","a167","a168","a169","a170","a171","a172","a173","a174","a175","a176","a177","a178","a179","a180","a181","a182","a183","a184","a185","a186","a187","a188","a189","a190","a191","a192","a193","a194","a195","a196","a197","a198","a199","a200","a201","a202","a203","a204","a205","a206","a207","a208","a209","a210","a211","a212","a213","a214","a215","a216","a217","a218","a219","a220","a221","a222","a223","a224","a225","a226","a227","a228","a229","a230","a231","a232","a233","a234","a235","a236","a237","a238","a239","a240","a241","a242","a243","a244","a245","a246","a247","a248","a249","a250","a251","a252","a253","a254","a255","s16","s17","s18","s19","s20","s21","s22","s23","s24","s25","s26","s27","s28","s29","s30","s31","s32","s33","s34","s35","s36","s37","s38","s39","s40","s41","s42","s43","s44","s45","s46","s47","s48","s49","s50","s51","s52","s53","s54","s55","s56","s57","s58","s59","s60","s61","s62","s63","s64","s65","s66","s67","s68","s69","s70","s71","s72","s73","s74","s75","s76","s77","s78","s79","s80","s81","s82","s83","s84","s85","s86","s87","s88","s89","s90","s91","s92","s93","s94","s95","s96","s97","s98","s99","s100","s101","vcc","m0","scc","memory");
}

extern "C" void kernel_launch(void* const* d_in, const int* in_sizes, int n_in, void* d_out, int out_size, void* d_ws, size_t ws_size, hipStream_t stream) {
  (void)hipFuncSetAttribute((const void*)gqa_block_kernel, hipFuncAttributeMaxDynamicSharedMemorySize, 163840);
  gqa_block_kernel<<<256, 256, 163840, stream>>>((const char*)d_in[0], (const char*)d_in[1], (const char*)d_in[2],
                                                 (const char*)d_in[3], (const char*)d_in[4], (char*)d_ws, (char*)d_out);
}
```
